```python
import torch

def _softmax(x, axis=-1):
    return torch.softmax(x, dim=axis)
def _gelu(x):
    return torch.nn.functional.gelu(x, approximate="tanh")
def _silu(x):
    return torch.nn.functional.silu(x)
def _sigmoid(x):
    return torch.sigmoid(x)
def _layernorm(x, axis=-1, eps=1e-5):
    m = x.mean(dim=axis, keepdim=True)
    v = x.var(dim=axis, keepdim=True, unbiased=False)
    return (x - m) / torch.sqrt(v + eps)
def _scatter_add(tgt, idx, src, axis=0):
    return tgt.clone().index_add_(axis, idx, src)
def _segment_sum(src, seg, n):
    out = torch.zeros((n,) + src.shape[1:], dtype=src.dtype)
    return out.index_add_(0, seg, src)
def _pad(x, pw):
    flat = tuple(v for pair in reversed(pw) for v in pair)
    return torch.nn.functional.pad(x, flat)

kernel_name = 'parallel_sum__gqa_block_cached__glu_gated_bilinear__decode'

def setup_inputs():

    _w = (lambda a: (lambda t: t.to(torch.bfloat16) if t.is_floating_point() else t)(torch.as_tensor(a)))
    def _A_setup():
        B, Sq, D = 4, 1, 512
        Hq, Hkv, Sk, Dh = 32, 8, 4096, 128
        assert Hq % Hkv == 0, (Hq, Hkv)
        return {
            "x": _w((lambda *s: torch.randn(*s, dtype=torch.bfloat16) * 0.05)(B, Sq, D)),
            "k_cache": _w((lambda *s: torch.randn(*s, dtype=torch.bfloat16) * 1.6)(B, Hkv, Sk, Dh)),
            "v_cache": _w((lambda *s: torch.randn(*s, dtype=torch.bfloat16) * 0.8)(B, Hkv, Sk, Dh)),
            "wq": _w((lambda *s: torch.randn(*s, dtype=torch.bfloat16) * 1.6)(D, Hq * Dh) * (D ** -0.5)),
            "wo": _w((lambda *s: torch.randn(*s, dtype=torch.bfloat16) * 0.8)(Hq * Dh, D) * ((Hq * Dh) ** -0.5)),
        }
    _A_in = _A_setup()

    def _B_setup():
        B, S, D, F = 4, 1, 512, 1792
        return {
            "x": _w((lambda *s: torch.randn(*s, dtype=torch.bfloat16) * 0.05)(B, S, D)),
            "w_gate": _w((lambda *s: torch.randn(*s, dtype=torch.bfloat16) * 0.4)(D, F) * (D ** -0.5)),
            "w_up": _w((lambda *s: torch.randn(*s, dtype=torch.bfloat16) * 0.4)(D, F) * (D ** -0.5)),
            "w_down": _w((lambda *s: torch.randn(*s, dtype=torch.bfloat16) * 0.4)(F, D) * (F ** -0.5)),
        }
    _B_in = _B_setup()

    return {
        "A_x": _A_in["x"],
        "A_k_cache": _A_in["k_cache"],
        "A_v_cache": _A_in["v_cache"],
        "A_wq": _A_in["wq"],
        "A_wo": _A_in["wo"],
        "B_x": _B_in["x"],
        "B_w_gate": _B_in["w_gate"],
        "B_w_up": _B_in["w_up"],
        "B_w_down": _B_in["w_down"],
    }

def reference(A_x, A_k_cache, A_v_cache, A_wq, A_wo, B_x, B_w_gate, B_w_up, B_w_down):
    def _A_ref(x, k_cache, v_cache, wq, wo):
        B, Sq, D = x.shape
        Hq, Dh = 32, 128
        Hkv = k_cache.shape[1]
        rep = Hq // Hkv
        q = (lambda x, *d: x.permute(*d))((x @ wq).reshape(B, Sq, Hq, Dh), 0, 2, 1, 3)
        k = (lambda x, n, axis: torch.repeat_interleave(x, n, dim=axis))(k_cache, rep, axis=1)
        v = (lambda x, n, axis: torch.repeat_interleave(x, n, dim=axis))(v_cache, rep, axis=1)
        scores = torch.einsum("bhqd,bhkd->bhqk", q, k) * (Dh ** -0.5)
        p = _softmax(scores, axis=-1)
        o = torch.einsum("bhqk,bhkd->bhqd", p, v)
        return (lambda x, *d: x.permute(*d))(o, 0, 2, 1, 3).reshape(B, Sq, Hq * Dh) @ wo

    def _B_ref(x, w_gate, w_up, w_down):
        g = torch.einsum("bsd,df->bsf", x, w_gate)
        u = torch.einsum("bsd,df->bsf", x, w_up)
        h = g * u
        return torch.einsum("bsf,fd->bsd", h, w_down)

    x = A_x
    xn = _layernorm(x, axis=-1)
    return x + _A_ref(xn, A_k_cache, A_v_cache, A_wq, A_wo) + _B_ref(xn, B_w_gate, B_w_up, B_w_down)
```

```cpp
#include <hip/hip_runtime.h>
#include <hip/hip_bf16.h>
#include <stdint.h>

typedef unsigned short u16;
typedef unsigned u32x4 __attribute__((ext_vector_type(4)));
constexpr int B=4, D=512, HQ=32, HKV=8, SK=4096, DH=128, F=1792;
constexpr int NSPLIT=8;

__device__ __forceinline__ float bf2f(u16 v){ return __uint_as_float(((unsigned)v)<<16); }
__device__ __forceinline__ float rbf(float f){ unsigned u=__float_as_uint(f); u += 0x7fff + ((u>>16)&1); return __uint_as_float(u & 0xffff0000u);}

__global__ void __launch_bounds__(256) k1(const u16* x, const u16* wq, const u16* wg, const u16* wu, float* q, float* h){
  __shared__ float xn[B][D];
  __shared__ float red[4][B][64];
  __shared__ float red2[4][B][64];
  int tid=threadIdx.x, lane=tid&63, w=tid>>6;
  {
    float v[8]; float s=0;
    for(int i=0;i<8;i++){ v[i]=bf2f(x[w*D+lane*8+i]); s+=v[i]; }
    for(int o=32;o>0;o>>=1) s+=__shfl_xor(s,o);
    float m=s/D; float ss=0;
    for(int i=0;i<8;i++){ float d=v[i]-m; ss+=d*d; }
    for(int o=32;o>0;o>>=1) ss+=__shfl_xor(ss,o);
    float var=rbf(ss/D); float mb=rbf(m);
    float den=rbf(sqrtf(rbf(var+1e-5f)));
    for(int i=0;i<8;i++) xn[w][lane*8+i]=rbf(rbf(v[i]-mb)/den);
  }
  __syncthreads();
  int blk=blockIdx.x;
  if(blk<64){
    int col=blk*64+lane;
    float a[B]={0,0,0,0};
    for(int k=w*128;k<w*128+128;k++){ float wv=bf2f(wq[(size_t)k*4096+col]); for(int b=0;b<B;b++) a[b]+=xn[b][k]*wv; }
    for(int b=0;b<B;b++) red[w][b][lane]=a[b];
    __syncthreads();
    int b=w; float s=red[0][b][lane]+red[1][b][lane]+red[2][b][lane]+red[3][b][lane];
    q[b*4096+col]=rbf(s);
  } else {
    int col=(blk-64)*64+lane;
    float a[B]={0,0,0,0}, c[B]={0,0,0,0};
    for(int k=w*128;k<w*128+128;k++){ float g=bf2f(wg[(size_t)k*F+col]); float u=bf2f(wu[(size_t)k*F+col]); for(int b=0;b<B;b++){ a[b]+=xn[b][k]*g; c[b]+=xn[b][k]*u;} }
    for(int b=0;b<B;b++){ red[w][b][lane]=a[b]; red2[w][b][lane]=c[b]; }
    __syncthreads();
    int b=w; float s=red[0][b][lane]+red[1][b][lane]+red[2][b][lane]+red[3][b][lane];
    float t=red2[0][b][lane]+red2[1][b][lane]+red2[2][b][lane]+red2[3][b][lane];
    h[b*F+col]=rbf(rbf(s)*rbf(t));
  }
}

__global__ void __launch_bounds__(256) k2(const u16* kc, const u16* vc, const float* q, float* po, float* pml){
  __shared__ float sc[4][512];
  __shared__ float red[4][4][128];
  __shared__ float ml[4][2];
  int s=blockIdx.x, kvh=blockIdx.y, b=blockIdx.z;
  int tid=threadIdx.x, lane=tid&63, w=tid>>6, g=lane>>4, c=lane&15;
  const u16* kb=kc+((size_t)(b*HKV+kvh)*SK + s*512)*DH;
  const u16* vb=vc+((size_t)(b*HKV+kvh)*SK + s*512)*DH;
  float qv[4][8];
  for(int hh=0;hh<4;hh++) for(int i=0;i<8;i++) qv[hh][i]=q[b*4096+(kvh*4+hh)*128+c*8+i];
  for(int it=0;it<32;it++){
    int key=w*128+it*4+g;
    u32x4 kk=*(const u32x4*)(kb+(size_t)key*DH+c*8);
    float kf[8];
    for(int i=0;i<4;i++){ kf[2*i]=__uint_as_float(kk[i]<<16); kf[2*i+1]=__uint_as_float(kk[i]&0xffff0000u);}
    for(int hh=0;hh<4;hh++){ float d=0; for(int i=0;i<8;i++) d+=kf[i]*qv[hh][i];
      d+=__shfl_xor(d,1); d+=__shfl_xor(d,2); d+=__shfl_xor(d,4); d+=__shfl_xor(d,8);
      if(c==0) sc[hh][key]=rbf(rbf(d)*0.08838834764831845f); }
  }
  __syncthreads();
  { int hh=w; float m=-INFINITY; for(int i=lane;i<512;i+=64) m=fmaxf(m,sc[hh][i]);
    for(int o=32;o>0;o>>=1) m=fmaxf(m,__shfl_xor(m,o));
    float l=0; for(int i=lane;i<512;i+=64){ float p=__expf(sc[hh][i]-m); sc[hh][i]=p; l+=p; }
    for(int o=32;o>0;o>>=1) l+=__shfl_xor(l,o);
    if(lane==0){ ml[hh][0]=m; ml[hh][1]=l; }
  }
  __syncthreads();
  float acc[4][8]; for(int hh=0;hh<4;hh++) for(int i=0;i<8;i++) acc[hh][i]=0;
  for(int it=0;it<32;it++){
    int key=w*128+it*4+g;
    u32x4 vv=*(const u32x4*)(vb+(size_t)key*DH+c*8);
    float vf[8];
    for(int i=0;i<4;i++){ vf[2*i]=__uint_as_float(vv[i]<<16); vf[2*i+1]=__uint_as_float(vv[i]&0xffff0000u);}
    for(int hh=0;hh<4;hh++){ float p=sc[hh][key]; for(int i=0;i<8;i++) acc[hh][i]+=p*vf[i]; }
  }
  for(int hh=0;hh<4;hh++) for(int i=0;i<8;i++){ float a=acc[hh][i]; a+=__shfl_xor(a,16); a+=__shfl_xor(a,32); if(g==0) red[w][hh][c*8+i]=a; }
  __syncthreads();
  for(int i=tid;i<512;i+=256){ int hh=i>>7, d=i&127; float a=red[0][hh][d]+red[1][hh][d]+red[2][hh][d]+red[3][hh][d];
    po[(((size_t)b*HQ+kvh*4+hh)*NSPLIT+s)*128+d]=a; }
  if(tid<4){ pml[(((size_t)b*HQ+kvh*4+tid)*NSPLIT+s)*2]=ml[tid][0]; pml[(((size_t)b*HQ+kvh*4+tid)*NSPLIT+s)*2+1]=ml[tid][1]; }
}

__global__ void __launch_bounds__(256) k3(const u16* x, const u16* wo, const u16* wd, const float* po, const float* pml, const float* h, u16* out){
  __shared__ float o[B][4096];
  __shared__ float red[256][B*8+1];
  int tid=threadIdx.x;
  for(int i=tid;i<B*4096;i+=256){ int b=i>>12, hd=i&4095, hh=hd>>7, d=hd&127;
    const float* ml=pml+((size_t)b*HQ+hh)*NSPLIT*2; float M=-INFINITY; for(int s=0;s<NSPLIT;s++) M=fmaxf(M,ml[2*s]);
    float L=0,a=0; for(int s=0;s<NSPLIT;s++){ float wgt=__expf(ml[2*s]-M); L+=wgt*ml[2*s+1]; a+=wgt*po[(((size_t)b*HQ+hh)*NSPLIT+s)*128+d]; }
    o[b][hd]=rbf(a/L); }
  __syncthreads();
  int col0=blockIdx.x*8;
  float acc[B][8], acc2[B][8]; for(int b=0;b<B;b++) for(int i=0;i<8;i++){ acc[b][i]=0; acc2[b][i]=0; }
  for(int k=tid;k<4096;k+=256){ u32x4 ww=*(const u32x4*)(wo+(size_t)k*D+col0); float wf[8];
    for(int i=0;i<4;i++){ wf[2*i]=__uint_as_float(ww[i]<<16); wf[2*i+1]=__uint_as_float(ww[i]&0xffff0000u);}
    for(int b=0;b<B;b++) for(int i=0;i<8;i++) acc[b][i]+=o[b][k]*wf[i]; }
  for(int k=tid;k<F;k+=256){ u32x4 ww=*(const u32x4*)(wd+(size_t)k*D+col0); float wf[8];
    for(int i=0;i<4;i++){ wf[2*i]=__uint_as_float(ww[i]<<16); wf[2*i+1]=__uint_as_float(ww[i]&0xffff0000u);}
    for(int b=0;b<B;b++) for(int i=0;i<8;i++) acc2[b][i]+=h[b*F+k]*wf[i]; }
  for(int b=0;b<B;b++) for(int i=0;i<8;i++) red[tid][b*8+i]=acc[b][i];
  __syncthreads();
  float sa=0; if(tid<32){ for(int t=0;t<256;t++) sa+=red[t][tid]; }
  __syncthreads();
  for(int b=0;b<B;b++) for(int i=0;i<8;i++) red[tid][b*8+i]=acc2[b][i];
  __syncthreads();
  if(tid<32){ float sb=0; for(int t=0;t<256;t++) sb+=red[t][tid]; int b=tid>>3,i=tid&7; float xv=bf2f(x[b*D+col0+i]);
    float r=rbf(rbf(xv+rbf(sa))+rbf(sb)); out[b*D+col0+i]=(u16)(__float_as_uint(r)>>16); }
}

extern "C" void kernel_launch(void* const* d_in, const int* in_sizes, int n_in, void* d_out, int out_size, void* d_ws, size_t ws_size, hipStream_t stream){
  const u16* x=(const u16*)d_in[0]; const u16* kc=(const u16*)d_in[1]; const u16* vc=(const u16*)d_in[2];
  const u16* wq=(const u16*)d_in[3]; const u16* wo=(const u16*)d_in[4];
  const u16* wg=(const u16*)d_in[6]; const u16* wu=(const u16*)d_in[7]; const u16* wd=(const u16*)d_in[8];
  float* q=(float*)d_ws; float* h=q+B*4096; float* po=h+B*F; float* pml=po+B*HQ*NSPLIT*128;
  k1<<<64+28,256,0,stream>>>(x,wq,wg,wu,q,h);
  k2<<<dim3(NSPLIT,HKV,B),256,0,stream>>>(kc,vc,q,po,pml);
  k3<<<64,256,0,stream>>>(x,wo,wd,po,pml,h,(u16*)d_out);
}
```

```cpp
#include <hip/hip_runtime.h>
#include <hip/hip_bf16.h>
#include <stdint.h>

#define KV_AUX 0
#define W_AUX 2
typedef unsigned short u16;
typedef unsigned u32x4 __attribute__((ext_vector_type(4)));
typedef float f32x4 __attribute__((ext_vector_type(4)));
typedef short bf16x8 __attribute__((ext_vector_type(8)));
constexpr int B=4, D=512, HQ=32, HKV=8, SK=4096, DH=128, F=1792;
constexpr int NSPLIT=8;

__device__ __forceinline__ float bf2f(u16 v){ return __uint_as_float(((unsigned)v)<<16); }
__device__ __forceinline__ float rbf(float f){ unsigned u=__float_as_uint(f); u += 0x7fff + ((u>>16)&1); return __uint_as_float(u & 0xffff0000u);}
typedef __bf16 bf16x2_t __attribute__((ext_vector_type(2)));
typedef float f32x2_t __attribute__((ext_vector_type(2)));
__device__ __forceinline__ unsigned cvtpk(float lo, float hi){ f32x2_t f={lo,hi}; bf16x2_t r=__builtin_convertvector(f,bf16x2_t); return __builtin_bit_cast(unsigned,r); }
__device__ __forceinline__ f32x4 mfma(u32x4 a, u32x4 b, f32x4 c){ return __builtin_amdgcn_mfma_f32_16x16x32_bf16(__builtin_bit_cast(bf16x8,a), __builtin_bit_cast(bf16x8,b), c, 0,0,0); }
__device__ __forceinline__ float lo16(unsigned u){ return __uint_as_float(u<<16);}
__device__ __forceinline__ float hi16(unsigned u){ return __uint_as_float(u&0xffff0000u);}

__global__ void __launch_bounds__(256) k1(const u16* x, const u16* wq, const u16* wg, const u16* wu, u16* q, float* h){
  __shared__ float xn[B][D];
  __shared__ float red[4][2][B][16];
  int tid=threadIdx.x, lane=tid&63, w=tid>>6;
  int blk=blockIdx.x;
  u32x4 wa[4], wb[4];
  int half=tid&1, r0=tid>>1;
  if(blk<256){
    for(int i=0;i<4;i++) wa[i]=*(const u32x4*)(wq+(size_t)(r0+128*i)*4096+blk*16+half*8);
  } else {
    for(int i=0;i<4;i++){ wa[i]=*(const u32x4*)(wg+(size_t)(r0+128*i)*F+(blk-256)*16+half*8); wb[i]=*(const u32x4*)(wu+(size_t)(r0+128*i)*F+(blk-256)*16+half*8);}
  }
  { float v[8]; float s=0; u32x4 xx=*(const u32x4*)(x+w*D+lane*8);
    for(int i=0;i<4;i++){ v[2*i]=lo16(xx[i]); v[2*i+1]=hi16(xx[i]); }
    for(int i=0;i<8;i++) s+=v[i];
    for(int o=32;o>0;o>>=1) s+=__shfl_xor(s,o);
    float m=s/D; float ss=0;
    for(int i=0;i<8;i++){ float d=v[i]-m; ss+=d*d; }
    for(int o=32;o>0;o>>=1) ss+=__shfl_xor(ss,o);
    float var=rbf(ss/D); float mb=rbf(m);
    float den=rbf(sqrtf(rbf(var+1e-5f)));
    for(int i=0;i<8;i++) xn[w][lane*8+i]=rbf(rbf(v[i]-mb)/den);
  }
  __syncthreads();
  float a[B][8], c[B][8];
  for(int b=0;b<B;b++) for(int j=0;j<8;j++){ a[b][j]=0; c[b][j]=0; }
  for(int i=0;i<4;i++){ int k=r0+128*i; for(int b=0;b<B;b++){ float xv=xn[b][k];
      for(int j=0;j<4;j++){ a[b][2*j]+=xv*lo16(wa[i][j]); a[b][2*j+1]+=xv*hi16(wa[i][j]); } } }
  if(blk>=256) for(int i=0;i<4;i++){ int k=r0+128*i; for(int b=0;b<B;b++){ float xv=xn[b][k];
      for(int j=0;j<4;j++){ c[b][2*j]+=xv*lo16(wb[i][j]); c[b][2*j+1]+=xv*hi16(wb[i][j]); } } }
  for(int b=0;b<B;b++) for(int j=0;j<8;j++){ float s=a[b][j]; for(int o=2;o<64;o<<=1) s+=__shfl_xor(s,o); if(lane<2) red[w][0][b][lane*8+j]=s; }
  if(blk>=256) for(int b=0;b<B;b++) for(int j=0;j<8;j++){ float s=c[b][j]; for(int o=2;o<64;o<<=1) s+=__shfl_xor(s,o); if(lane<2) red[w][1][b][lane*8+j]=s; }
  __syncthreads();
  if(tid<64){ int b=tid>>4, j=tid&15; float s=red[0][0][b][j]+red[1][0][b][j]+red[2][0][b][j]+red[3][0][b][j];
    if(blk<256){ q[b*4096+blk*16+j]=(u16)(__float_as_uint(rbf(s))>>16); }
    else { float t=red[0][1][b][j]+red[1][1][b][j]+red[2][1][b][j]+red[3][1][b][j]; h[b*F+(blk-256)*16+j]=rbf(rbf(s)*rbf(t)); }
  }
}

__global__ void __launch_bounds__(512) k2(const u16* kc, const u16* vc, const u16* q, float* po, float* pml){
  __shared__ float red[8][4][132];
  int blk=blockIdx.x; int kvh=blk&7, b=(blk>>3)&3, s=blk>>5;
  int tid=threadIdx.x, lane=tid&63, w=__builtin_amdgcn_readfirstlane(tid>>6), g=lane>>4, c=lane&15;
  size_t base=((size_t)(b*HKV+kvh)*SK + s*512)*DH;
  auto rk=__builtin_amdgcn_make_buffer_rsrc((void*)(kc+base),0,131072,0x00020000);
  auto rv=__builtin_amdgcn_make_buffer_rsrc((void*)(vc+base),0,131072,0x00020000);
  u32x4 qf[4];
  for(int i=0;i<4;i++){ qf[i]=*(const u32x4*)(q+b*4096+(kvh*4+(c&3))*128+32*i+8*g); }
  u32x4 K[16], V[16];
  int koff=(w*16 + 4*(c&3)+(c>>2))*256 + g*16;
#pragma unroll
  for(int t=0;t<4;t++)
#pragma unroll
    for(int i=0;i<4;i++){ K[t*4+i]=__builtin_amdgcn_raw_buffer_load_b128(rk,koff+t*32768+i*64,0,KV_AUX); __builtin_amdgcn_sched_barrier(0);}
  int voff=w*4096+lane*16;
#pragma unroll
  for(int t=0;t<4;t++)
#pragma unroll
    for(int r=0;r<4;r++){ V[t*4+r]=__builtin_amdgcn_raw_buffer_load_b128(rv,voff+t*32768+r*1024,0,KV_AUX); __builtin_amdgcn_sched_barrier(0);}
  f32x4 S[4];
#pragma unroll
  for(int t=0;t<4;t++){ f32x4 a={0,0,0,0};
#pragma unroll
    for(int i=0;i<4;i++) a=mfma(K[t*4+i],qf[i],a);
    S[t]=a; }
  const float sc=0.08838834764831845f;
  float m=-INFINITY;
#pragma unroll
  for(int t=0;t<4;t++)
#pragma unroll
    for(int r=0;r<4;r++){ S[t][r]=rbf(rbf(S[t][r])*sc); m=fmaxf(m,S[t][r]); }
  m=fmaxf(m,__shfl_xor(m,16)); m=fmaxf(m,__shfl_xor(m,32));
  float l=0; u32x4 P[2];
#pragma unroll
  for(int t=0;t<4;t++){ float p[4];
#pragma unroll
    for(int r=0;r<4;r++){ p[r]=__expf(S[t][r]-m); }
    unsigned a=cvtpk(p[0],p[1]), bb=cvtpk(p[2],p[3]);
    l+=lo16(a)+hi16(a)+lo16(bb)+hi16(bb);
    P[t>>1][(t&1)*2]=a; P[t>>1][(t&1)*2+1]=bb; }
  l+=__shfl_xor(l,16); l+=__shfl_xor(l,32);
  f32x4 O[8];
#pragma unroll
  for(int e=0;e<8;e++) O[e]=f32x4{0,0,0,0};
#pragma unroll
  for(int u=0;u<2;u++){
#pragma unroll
    for(int i=0;i<4;i++){
      u32x4 f0,f1;
#pragma unroll
      for(int j=0;j<4;j++){ unsigned x0=V[u*8+2*j][i], x1=V[u*8+2*j+1][i];
        f0[j]=__builtin_amdgcn_perm(x1,x0,0x05040100u); f1[j]=__builtin_amdgcn_perm(x1,x0,0x07060302u); }
      O[2*i]=mfma(P[u],f0,O[2*i]); O[2*i+1]=mfma(P[u],f1,O[2*i+1]);
    } }
  if(g==0){
#pragma unroll
    for(int hh=0;hh<4;hh++){ *(f32x4*)&red[w][hh][8*c]=f32x4{O[0][hh],O[1][hh],O[2][hh],O[3][hh]}; *(f32x4*)&red[w][hh][8*c+4]=f32x4{O[4][hh],O[5][hh],O[6][hh],O[7][hh]}; }
    if(c<4){ red[w][c][128]=m; red[w][c][129]=l; }
  }
  __syncthreads();
  { int hh=tid>>7, d=tid&127; float M=-INFINITY;
#pragma unroll
    for(int i=0;i<8;i++) M=fmaxf(M,red[i][hh][128]);
    float L=0,a=0;
#pragma unroll
    for(int i=0;i<8;i++){ float wt=__expf(red[i][hh][128]-M); L+=wt*red[i][hh][129]; a+=wt*red[i][hh][d]; }
    size_t slot=((size_t)b*HQ+kvh*4+hh)*NSPLIT+s;
    po[slot*128+d]=a; if(d==0){ pml[slot*2]=M; pml[slot*2+1]=L; }
  }
}

__global__ void __launch_bounds__(256) k3(const u16* wo, const u16* wd, const float* po, const float* pml, const float* h, float* part){
  __shared__ float o[B][128];
  __shared__ float red[4][B][512];
  int tid=threadIdx.x, lane=tid&63, w=tid>>6, blk=blockIdx.x;
  if(blk<256){
    int hh=blk>>3, ng=blk&7;
    u32x4 ww[4]; int cg=tid&7, r0=tid>>3;
    for(int i=0;i<4;i++) ww[i]=*(const u32x4*)(wo+(size_t)(hh*128+r0+32*i)*D+ng*64+cg*8);
    for(int i=tid;i<B*128;i+=256){ int b=i>>7,d=i&127; const float* ml=pml+((size_t)b*HQ+hh)*NSPLIT*2; float M=-INFINITY; for(int s=0;s<NSPLIT;s++) M=fmaxf(M,ml[2*s]);
      float L=0,a=0; for(int s=0;s<NSPLIT;s++){ float wgt=__expf(ml[2*s]-M); L+=wgt*ml[2*s+1]; a+=wgt*po[(((size_t)b*HQ+hh)*NSPLIT+s)*128+d]; }
      o[b][d]=rbf(a/L); }
    __syncthreads();
    float a[B][8]; for(int b=0;b<B;b++) for(int j=0;j<8;j++) a[b][j]=0;
    for(int i=0;i<4;i++) for(int b=0;b<B;b++){ float ov=o[b][r0+32*i]; for(int j=0;j<4;j++){ a[b][2*j]+=ov*lo16(ww[i][j]); a[b][2*j+1]+=ov*hi16(ww[i][j]); } }
    for(int b=0;b<B;b++) for(int j=0;j<8;j++){ float s=a[b][j]; s+=__shfl_xor(s,8); s+=__shfl_xor(s,16); s+=__shfl_xor(s,32); if(lane<8) red[w][b][lane*8+j]=s; }
    __syncthreads();
    { int b=tid>>6, j=tid&63; part[((size_t)hh*B+b)*D+ng*64+j]=red[0][b][j]+red[1][b][j]+red[2][b][j]+red[3][b][j]; }
  } else {
    int fb=blk-256;
    float a[B][8]; for(int b=0;b<B;b++) for(int j=0;j<8;j++) a[b][j]=0;
    u32x4 ww[16];
    for(int i=0;i<16;i++) ww[i]=*(const u32x4*)(wd+(size_t)(fb*64+w*16+i)*D+lane*8);
    for(int i=0;i<16;i++) for(int b=0;b<B;b++){ float hv=h[b*F+fb*64+w*16+i]; for(int j=0;j<4;j++){ a[b][2*j]+=hv*lo16(ww[i][j]); a[b][2*j+1]+=hv*hi16(ww[i][j]); } }
    for(int b=0;b<B;b++) for(int j=0;j<8;j++) red[w][b][lane*8+j]=a[b][j];
    __syncthreads();
    for(int i=tid;i<B*512;i+=256){ int b=i>>9,j=i&511; part[((size_t)(32+fb)*B+b)*D+j]=red[0][b][j]+red[1][b][j]+red[2][b][j]+red[3][b][j]; }
  }
}
__global__ void __launch_bounds__(64) k4(const u16* x, const float* part, u16* out){
  int i=blockIdx.x*64+threadIdx.x;
  float sa=0,sb=0;
#pragma unroll
  for(int p=0;p<32;p++) sa+=part[(size_t)p*2048+i];
#pragma unroll
  for(int p=32;p<60;p++) sb+=part[(size_t)p*2048+i];
  float r=rbf(rbf(bf2f(x[i])+rbf(sa))+rbf(sb)); out[i]=(u16)(__float_as_uint(r)>>16);
}

extern "C" void kernel_launch(void* const* d_in, const int* in_sizes, int n_in, void* d_out, int out_size, void* d_ws, size_t ws_size, hipStream_t stream){
  const u16* x=(const u16*)d_in[0]; const u16* kc=(const u16*)d_in[1]; const u16* vc=(const u16*)d_in[2];
  const u16* wq=(const u16*)d_in[3]; const u16* wo=(const u16*)d_in[4];
  const u16* wg=(const u16*)d_in[6]; const u16* wu=(const u16*)d_in[7]; const u16* wd=(const u16*)d_in[8];
  u16* q=(u16*)d_ws; float* h=(float*)(q+B*4096); float* po=h+B*F; float* pml=po+B*HQ*NSPLIT*128; float* part=pml+B*HQ*NSPLIT*2;
  k1<<<256+112,256,0,stream>>>(x,wq,wg,wu,q,h);
  k2<<<256,512,0,stream>>>(kc,vc,q,po,pml);
  k3<<<256+28,256,0,stream>>>(wo,wd,po,pml,h,part);
  k4<<<32,64,0,stream>>>(x,part,(u16*)d_out);
}
```

```cpp
#include <hip/hip_runtime.h>
#include <stdint.h>

#define KV_AUX 2
#define W_AUX 2
#define WO_AUX 2
#define WIN 4
#define QKPOS 6
#define SMPOS 11
#define BOOST 2
#define SLEEP_A 1
#define SLEEP_B 1
#define PRE_A 0
#define B0EARLY 0
#define PRE_B 0

typedef unsigned short u16;
typedef unsigned u32x2 __attribute__((ext_vector_type(2)));
typedef unsigned u32x4 __attribute__((ext_vector_type(4)));
typedef float f32x2 __attribute__((ext_vector_type(2)));
typedef float f32x4 __attribute__((ext_vector_type(4)));
typedef short s16x4 __attribute__((ext_vector_type(4)));
typedef short s16x8 __attribute__((ext_vector_type(8)));
typedef __bf16 bf16x2 __attribute__((ext_vector_type(2)));
#define LAS __attribute__((address_space(3)))

constexpr int DM = 512, SK = 4096, DH = 128, FF = 1792;
constexpr int NTHR = 640;
constexpr int WS_EPOCH = 0;
constexpr int WS_Q = 4096;
constexpr int WS_GU = WS_Q + 262144;
constexpr int WS_ST = WS_GU + 229376;
constexpr int WS_OA = WS_ST + 32768;
constexpr int WS_PB = WS_OA + 524288;
constexpr int WS_END = WS_PB + 1048576;
constexpr int WS_TS = 4 << 20;
constexpr int L_RED = 0;
constexpr int L_Q = L_RED + 16384;
constexpr int L_H = L_Q + 1024;
constexpr int L_MW = L_H + 128;
constexpr int L_LW = L_MW + 128;
constexpr int L_FLAG = L_LW + 128;
constexpr int L_END = L_FLAG + 16;
constexpr unsigned SPIN_CAP = 1u << 18;
constexpr int OOB = 0x7ffffff0;
constexpr float LOG2E = 1.4426950408889634f;

__device__ __forceinline__ float lo16(unsigned u) { return __uint_as_float(u << 16); }
__device__ __forceinline__ float hi16(unsigned u) { return __uint_as_float(u & 0xffff0000u); }
__device__ __forceinline__ unsigned cvtpk(float lo, float hi) { f32x2 f = {lo, hi}; return __builtin_bit_cast(unsigned, __builtin_convertvector(f, bf16x2)); }
__device__ __forceinline__ float rbf(float f) { return lo16(cvtpk(f, f)); }
__device__ __forceinline__ f32x4 mfma32(u32x4 a, u32x4 b, f32x4 c) { return __builtin_amdgcn_mfma_f32_16x16x32_bf16(__builtin_bit_cast(s16x8, a), __builtin_bit_cast(s16x8, b), c, 0, 0, 0); }
__device__ __forceinline__ f32x4 mfma16(u32x2 a, u32x2 b, f32x4 c) { return __builtin_amdgcn_mfma_f32_16x16x16bf16_1k(__builtin_bit_cast(s16x4, a), __builtin_bit_cast(s16x4, b), c, 0, 0, 0); }
template <int CTRL> __device__ __forceinline__ float dpp(float x) { return __builtin_bit_cast(float, __builtin_amdgcn_mov_dpp(__builtin_bit_cast(int, x), CTRL, 0xf, 0xf, true)); }
template <int CTRL> __device__ __forceinline__ unsigned dppu(unsigned x) { return (unsigned)__builtin_amdgcn_mov_dpp((int)x, CTRL, 0xf, 0xf, true); }
constexpr int ROR4 = 0x124, ROR8 = 0x128, QX1 = 0xB1, QX2 = 0x4E, QB0 = 0x00, QB1 = 0x55, QB2 = 0xAA, QB3 = 0xFF;
__device__ __forceinline__ float xrow_max(float x) {
  auto s = __builtin_amdgcn_permlane16_swap(__float_as_uint(x), __float_as_uint(x), false, false);
  x = fmaxf(__uint_as_float(s[0]), __uint_as_float(s[1]));
  auto t = __builtin_amdgcn_permlane32_swap(__float_as_uint(x), __float_as_uint(x), false, false);
  return fmaxf(__uint_as_float(t[0]), __uint_as_float(t[1]));
}
__device__ __forceinline__ float xrow_sum(float x) {
  auto s = __builtin_amdgcn_permlane16_swap(__float_as_uint(x), __float_as_uint(x), false, false);
  x = __uint_as_float(s[0]) + __uint_as_float(s[1]);
  auto t = __builtin_amdgcn_permlane32_swap(__float_as_uint(x), __float_as_uint(x), false, false);
  return __uint_as_float(t[0]) + __uint_as_float(t[1]);
}
__device__ __forceinline__ unsigned tag8(float v, unsigned tb) { return (__float_as_uint(v) & 0xffffff00u) | tb; }
__device__ __forceinline__ unsigned perm_lo(unsigned hi, unsigned lo) { return __builtin_amdgcn_perm(hi, lo, 0x05040100u); }
__device__ __forceinline__ unsigned perm_hi(unsigned hi, unsigned lo) { return __builtin_amdgcn_perm(hi, lo, 0x07060302u); }
#define SB() __builtin_amdgcn_sched_barrier(0)
#define CBAR() asm volatile("" ::: "memory")
#define LGKM0() asm volatile("s_waitcnt lgkmcnt(0)" ::: "memory")
#define RSRC(p, n) __builtin_amdgcn_make_buffer_rsrc((void*)(p), 0, (n), 0x00020000)
#define LD32(r, v, s, a) __builtin_amdgcn_raw_buffer_load_b32(r, v, s, a)
#define LD64(r, v, s, a) __builtin_amdgcn_raw_buffer_load_b64(r, v, s, a)
#define LD128(r, v, s, a) __builtin_amdgcn_raw_buffer_load_b128(r, v, s, a)

#define STAMP(i)
#define STAMPV(i, val)
#define STAMP_FLUSH()

__global__ void __launch_bounds__(NTHR, 3) fused(const u16* kc, const u16* vc, const u16* x, const u16* wq, const u16* wg, const u16* wu,
                                                 const u16* wo, const u16* wd, unsigned char* ws, u16* out) {
  __shared__ __attribute__((aligned(16))) unsigned char smem[L_END];
  LAS unsigned char* lds = (LAS unsigned char*)smem;
  const int blk = blockIdx.x, kvh = blk & 7, gi = blk >> 3, b = gi & 3, s = gi >> 2;
  const int tid = threadIdx.x, lane = tid & 63, w = __builtin_amdgcn_readfirstlane(tid >> 6), g = lane >> 4, c = lane & 15;
  STAMP(0);
  asm volatile("" : "+s"(kc), "+s"(vc), "+s"(x), "+s"(wq), "+s"(wg), "+s"(wu), "+s"(wo), "+s"(wd), "+s"(ws), "+s"(out));
  auto rws = RSRC(ws, WS_END);
  const int kcx = kvh * 16 + (gi >> 1), nh = gi & 1;
  const int slot = (kvh * 4 + b) * 8 + s;
  if (w < 2) {
    __builtin_amdgcn_s_setprio(3);
    if (w == 0) *(LAS unsigned*)(lds + L_MW + lane * 4) = 0u; else *(LAS unsigned*)(lds + L_FLAG) = 0u;
    LGKM0();
    const int tq = kvh * 28 + gi, mat = tq >= 112 ? 1 : 0, rem = tq - 112 * mat;
    const bool active = (w == 0) || (gi < 28);
    const int kq = (w == 0) ? (gi & 3) : (rem & 3), cb = (w == 0) ? (gi >> 2) : (rem >> 2);
    const int rstride = (w == 0) ? 8192 : FF * 2;
    const u16* wbase = ((w == 0) ? wq + kvh * 512 : (mat ? wu : wg)) + cb * 64 + (size_t)kq * 64 * rstride;
    auto rw = RSRC(wbase, active ? 127 * rstride + 128 : 0);
    auto rx = RSRC(x, 4096);
    const unsigned ep = LD32(rws, WS_EPOCH, 0, 16);
    u32x4 xs[4], xf[4];
    u32x2 W[32];
#pragma unroll
    for (int i = 0; i < 4; i++) xs[i] = LD128(rx, (c >> 2) * 1024 + g * 64 + (c & 3) * 16 + i * 256, 0, 0);
#pragma unroll
    for (int i = 0; i < 4; i++) xf[i] = LD128(rx, (c >> 2) * 1024 + kq * 256 + g * 16 + i * 64, 0, 0);
    const int woff = g * 8 * rstride + c * 8;
#pragma unroll
    for (int j = 0; j < 32; j++) {
      W[j] = LD64(rw, woff, ((j >> 3) * 32 + (j & 7)) * rstride, W_AUX);
      if (j == 7) { SB(); __builtin_amdgcn_s_barrier(); SB(); }
    }
    SB();
    STAMP(1);
    float sm = 0.f;
#pragma unroll
    for (int i = 0; i < 4; i++)
#pragma unroll
      for (int j = 0; j < 4; j++) sm += lo16(xs[i][j]) + hi16(xs[i][j]);
    sm += dpp<QX1>(sm); sm += dpp<QX2>(sm);
    const float mean = xrow_sum(sm) * (1.f / DM);
    float ss = 0.f;
#pragma unroll
    for (int i = 0; i < 4; i++)
#pragma unroll
      for (int j = 0; j < 4; j++) { const float d0 = lo16(xs[i][j]) - mean, d1 = hi16(xs[i][j]) - mean; ss = fmaf(d0, d0, ss); ss = fmaf(d1, d1, ss); }
    ss += dpp<QX1>(ss); ss += dpp<QX2>(ss);
    const float var = rbf(xrow_sum(ss) * (1.f / DM)), mb = rbf(mean);
    const float den = rbf(sqrtf(rbf(var + 1e-5f)));
    const float rden = 1.0f / den;
#pragma unroll
    for (int i = 0; i < 4; i++)
#pragma unroll
      for (int j = 0; j < 4; j++) {
        const unsigned d = cvtpk(lo16(xf[i][j]) - mb, hi16(xf[i][j]) - mb);
        const float n0 = lo16(d), n1 = hi16(d), q0 = n0 * rden, q1 = n1 * rden;
        xf[i][j] = cvtpk(fmaf(fmaf(-q0, den, n0), rden, q0), fmaf(fmaf(-q1, den, n1), rden, q1));
      }
    STAMP(2);
    f32x4 acc[4];
#pragma unroll
    for (int e = 0; e < 4; e++) acc[e] = f32x4{0.f, 0.f, 0.f, 0.f};
#pragma unroll
    for (int t = 0; t < 4; t++)
#pragma unroll
      for (int e = 0; e < 4; e++) {
        u32x4 f;
#pragma unroll
        for (int j = 0; j < 4; j++) f[j] = (e & 1) ? perm_hi(W[t * 8 + 2 * j + 1][e >> 1], W[t * 8 + 2 * j][e >> 1]) : perm_lo(W[t * 8 + 2 * j + 1][e >> 1], W[t * 8 + 2 * j][e >> 1]);
        acc[e] = mfma32(xf[t], f, acc[e]);
      }
    const unsigned T = __builtin_amdgcn_readfirstlane(ep) + 0x01010101u;
    const unsigned t0 = T & 0xffu, t1 = (T >> 8) & 0xffu, t2 = (T >> 16) & 0xffu, t3 = T >> 24;
    {
      const u32x4 o = {tag8(acc[0][0], t0), tag8(acc[1][0], t1), tag8(acc[2][0], t2), tag8(acc[3][0], t3)};
      const int poff = (w == 0) ? WS_Q + (((kvh * 4 + g) * 4 + kq) * 512 + cb * 64 + c * 4) * 4 : WS_GU + (((mat * 4 + g) * 4 + kq) * FF + cb * 64 + c * 4) * 4;
      __builtin_amdgcn_raw_buffer_store_b128(o, rws, active ? poff : OOB, 0, 16);
    }
    STAMP(3);
    if (w == 0) {
      const int goff = WS_Q + (kvh * 4 + b) * 8192 + lane * 16;
      u32x4 q[8];
      unsigned spin = 0;
      for (;; spin++) {
        CBAR();
#pragma unroll
        for (int i = 0; i < 8; i++) q[i] = LD128(rws, goff, i * 1024, 16);
        unsigned bad = 0;
#pragma unroll
        for (int i = 0; i < 8; i++) bad |= (q[i][0] ^ t0) | (q[i][1] ^ t1) | (q[i][2] ^ t2) | (q[i][3] ^ t3);
        if (__all((bad & 0xffu) == 0) || spin > SPIN_CAP) break;
        __builtin_amdgcn_s_sleep(4);
      }
      STAMPV(15, spin);
#pragma unroll
      for (int hh = 0; hh < 2; hh++) {
        const f32x4 a = (__builtin_bit_cast(f32x4, q[hh]) + __builtin_bit_cast(f32x4, q[2 + hh])) + (__builtin_bit_cast(f32x4, q[4 + hh]) + __builtin_bit_cast(f32x4, q[6 + hh]));
        *(LAS u32x2*)(lds + L_Q + hh * 512 + lane * 8) = u32x2{cvtpk(a[0], a[1]), cvtpk(a[2], a[3])};
      }
      LGKM0();
      if (lane == 0) *(volatile LAS u32x2*)(lds + L_FLAG) = u32x2{1u, T};
      STAMP(4);
      unsigned lv;
      for (spin = 0;; spin++) {
        lv = *(volatile LAS unsigned*)(lds + L_LW + (lane & 31) * 4);
        if (__all(lv != 0u) || spin > (SPIN_CAP << 4)) break;
        __builtin_amdgcn_s_sleep(2);
      }
      float mv = __uint_as_float(*(volatile LAS unsigned*)(lds + L_MW + (lane & 31) * 4) & 0xffff0000u);
      float lsum = __uint_as_float(lv);
      lsum += dpp<QX1>(lsum); lsum += dpp<QX2>(lsum); lsum += dpp<0x141>(lsum);
      mv = fmaxf(mv, dpp<QX1>(mv)); mv = fmaxf(mv, dpp<QX2>(mv)); mv = fmaxf(mv, dpp<0x141>(mv));
      const u32x2 st = {tag8(mv, t0), tag8(lsum, t1)};
      __builtin_amdgcn_raw_buffer_store_b64(st, rws, ((lane & 39) == 0) ? WS_ST + slot * 128 + (lane >> 3) * 8 : OOB, 0, 16);
    } else {
      const int f = kcx * 14 + (c < 14 ? c : 13);
      const int goff = WS_GU + (g * 4 * FF + f) * 4;
      const unsigned tf = (T >> (8 * (f & 3))) & 0xffu;
      unsigned gv[8];
      unsigned spin = 0;
      for (;; spin++) {
        CBAR();
#pragma unroll
        for (int i = 0; i < 8; i++) gv[i] = LD32(rws, goff, ((i >> 2) * 16 + (i & 3)) * FF * 4, 16);
        unsigned bad = 0;
#pragma unroll
        for (int i = 0; i < 8; i++) bad |= gv[i] ^ tf;
        if (__all((bad & 0xffu) == 0) || spin > SPIN_CAP) break;
        __builtin_amdgcn_s_sleep(4);
      }
      STAMPV(15, spin);
      const float gs = (__uint_as_float(gv[0]) + __uint_as_float(gv[1])) + (__uint_as_float(gv[2]) + __uint_as_float(gv[3]));
      const float us = (__uint_as_float(gv[4]) + __uint_as_float(gv[5])) + (__uint_as_float(gv[6]) + __uint_as_float(gv[7]));
      const unsigned gu = cvtpk(gs, us);
      const float hv = c < 14 ? lo16(gu) * hi16(gu) : 0.f;
      *(LAS u16*)(lds + L_H + g * 32 + c * 2) = (u16)cvtpk(hv, hv);
      LGKM0();
      STAMP(4);
    }
    STAMP(5);
    STAMP_FLUSH();
    return;
  }
  const int kv = w - 2;
  const size_t base = ((size_t)(b * 8 + kvh) * SK + s * 512) * DH;
  auto rk = RSRC(kc + base, 131072);
  auto rv = RSRC(vc + base, 131072);
  u32x4 K[16], V[16];
  const int koff = kv * 4096 + (c >> 2) * 256 + g * 64 + (c & 3) * 16;
  const int voff = kv * 4096 + lane * 16;
#define WAITWIN() asm volatile("s_waitcnt vmcnt(%0)" ::"n"(WIN - 1) : "memory")
#define VLOAD(i) do { V[i] = LD128(rv, voff + ((i) & 3) * 1024, ((i) >> 2) * 32768, KV_AUX); SB(); } while (0)
#pragma unroll
  for (int i = 0; i < 16; i++) {
    if (i == WIN) __builtin_amdgcn_s_barrier();
    if (i >= WIN) WAITWIN();
    K[i] = LD128(rk, koff + (i & 3) * 1024, (i >> 2) * 32768, KV_AUX);
    SB();
  }
  STAMP(1);
#pragma unroll
  for (int i = 0; i < QKPOS; i++) { if (i < QKPOS - BOOST) WAITWIN(); VLOAD(i); }
  STAMP(2);
  u32x2 fl = *(volatile LAS u32x2*)(lds + L_FLAG);
  for (unsigned spin = 0; fl[0] == 0u && spin < (SPIN_CAP << 4); spin++) { __builtin_amdgcn_s_sleep(1); fl = *(volatile LAS u32x2*)(lds + L_FLAG); }
  CBAR();
  const unsigned T = __builtin_amdgcn_readfirstlane(fl[1]);
  const unsigned t0 = T & 0xffu, t1 = (T >> 8) & 0xffu, t2 = (T >> 16) & 0xffu, t3 = T >> 24;
  STAMP(3);
  float S[16];
  {
    const u32x4 qf = *(LAS u32x4*)(lds + L_Q + (c >> 2) * 256 + g * 64 + (c & 3) * 16);
#pragma unroll
    for (int i = 0; i < 16; i++) {
      const f32x4 d = mfma32(K[i], qf, f32x4{0.f, 0.f, 0.f, 0.f});
      S[i] = (dpp<QB0>(d[0]) + dpp<QB1>(d[1])) + (dpp<QB2>(d[2]) + dpp<QB3>(d[3]));
    }
  }
  float mw = -INFINITY;
#pragma unroll
  for (int i = 0; i < 16; i += 2) {
    const unsigned p = cvtpk(S[i], S[i + 1]);
    const unsigned p2 = cvtpk(lo16(p) * 0.08838834764831845f, hi16(p) * 0.08838834764831845f);
    S[i] = lo16(p2); S[i + 1] = hi16(p2);
    mw = fmaxf(mw, fmaxf(S[i], S[i + 1]));
  }
  mw = xrow_max(mw);
  if ((lane & 51) == 0) *(volatile LAS unsigned*)(lds + L_MW + (c >> 2) * 32 + kv * 4) = __float_as_uint(mw) | 1u;
  STAMP(4);
  SB();
#pragma unroll
  for (int i = QKPOS; i < SMPOS; i++) { if (i < SMPOS - BOOST) WAITWIN(); VLOAD(i); }
  STAMP(5);
  u32x4 P[2];
  {
    u32x4 m0, m1;
    for (unsigned spin = 0;; spin++) {
      m0 = *(volatile LAS u32x4*)(lds + L_MW + (c >> 2) * 32); m1 = *(volatile LAS u32x4*)(lds + L_MW + (c >> 2) * 32 + 16);
      unsigned ok = 1u;
#pragma unroll
      for (int i = 0; i < 4; i++) ok &= m0[i] & m1[i];
      if (__all(ok != 0u) || spin > (SPIN_CAP << 4)) break;
      __builtin_amdgcn_s_sleep(1);
    }
    float M = -INFINITY;
#pragma unroll
    for (int i = 0; i < 4; i++) M = fmaxf(M, fmaxf(hi16(m0[i]), hi16(m1[i])));
    const float mneg = -M * LOG2E;
    float lsum = 0.f;
#pragma unroll
    for (int i = 0; i < 16; i += 2) {
      const float p0 = __builtin_amdgcn_exp2f(fmaf(S[i], LOG2E, mneg)), p1 = __builtin_amdgcn_exp2f(fmaf(S[i + 1], LOG2E, mneg));
      lsum += p0 + p1;
      P[i >> 3][(i & 7) >> 1] = cvtpk(p0, p1);
    }
    lsum = xrow_sum(lsum);
    if ((lane & 51) == 0) *(volatile LAS unsigned*)(lds + L_LW + (c >> 2) * 32 + kv * 4) = __float_as_uint(lsum) | 1u;
  }
  STAMP(6);
  SB();
#pragma unroll
  for (int i = SMPOS; i < 16; i++) { WAITWIN(); VLOAD(i); }
  u32x2 WO[8], WD[4];
  unsigned xfin;
  {
    const bool tailw = kv < 4;
    auto rwo = RSRC(wo + (size_t)kcx * 32 * DM + nh * 256 + (kv & 3) * 64, tailw ? 31 * 1024 + 128 : 0);
    auto rwd = RSRC(wd + (size_t)kcx * 14 * DM + nh * 256 + (kv & 3) * 64, tailw ? 13 * 1024 + 128 : 0);
    auto rx = RSRC(x, 4096);
#pragma unroll
    for (int j = 0; j < 8; j++) WO[j] = LD64(rwo, g * 8192 + c * 8, j * 1024, WO_AUX);
#pragma unroll
    for (int j = 0; j < 4; j++) WD[j] = LD64(rwd, g * 4096 + c * 8 + j * 1024, 0, WO_AUX);
    xfin = __builtin_amdgcn_raw_buffer_load_b16(rx, (kvh * 32 + gi) * 16 + (c & 1) * 8 + g * 2, 0, 0);
    SB();
  }
  STAMP(7);
  f32x4 O[8];
#pragma unroll
  for (int e = 0; e < 8; e++) O[e] = f32x4{0.f, 0.f, 0.f, 0.f};
  {
#pragma unroll
    for (int i = 0; i < 4; i++) {
      u32x4 f0, f1;
#pragma unroll
      for (int j = 0; j < 4; j++) { f0[j] = perm_lo(V[2 * j + 1][i], V[2 * j][i]); f1[j] = perm_hi(V[2 * j + 1][i], V[2 * j][i]); }
      O[2 * i] = mfma32(P[0], f0, O[2 * i]); O[2 * i + 1] = mfma32(P[0], f1, O[2 * i + 1]);
    }
    SB();
    u32x4 F[8];
#pragma unroll
    for (int j = 0; j < 3; j++) {
#pragma unroll
      for (int i = 0; i < 4; i++) { F[2 * i][j] = perm_lo(V[8 + 2 * j + 1][i], V[8 + 2 * j][i]); F[2 * i + 1][j] = perm_hi(V[8 + 2 * j + 1][i], V[8 + 2 * j][i]); }
      SB();
    }
#pragma unroll
    for (int i = 0; i < 4; i++) {
      F[2 * i][3] = perm_lo(V[15][i], V[14][i]); F[2 * i + 1][3] = perm_hi(V[15][i], V[14][i]);
      O[2 * i] = mfma32(P[1], F[2 * i], O[2 * i]); O[2 * i + 1] = mfma32(P[1], F[2 * i + 1], O[2 * i + 1]);
    }
  }
#pragma unroll
  for (int e = 0; e < 8; e++) *(LAS float*)(lds + L_RED + kv * 2048 + lane * 32 + e * 4) = O[e][0];
  LGKM0();
  STAMP(8);
  __builtin_amdgcn_s_barrier();
  CBAR();
  STAMP(9);
  if (kv >= 4) {
    const int h = kv - 4, half = lane >> 5, l5 = lane & 31;
    f32x4 a = *(LAS f32x4*)(lds + L_RED + (half * 4 + 0) * 2048 + h * 512 + l5 * 16);
#pragma unroll
    for (int i = 1; i < 4; i++) a += *(LAS f32x4*)(lds + L_RED + (half * 4 + i) * 2048 + h * 512 + l5 * 16);
    u32x4 o;
#pragma unroll
    for (int i = 0; i < 4; i++) {
      auto sw = __builtin_amdgcn_permlane32_swap(__float_as_uint(a[i]), __float_as_uint(a[i]), false, false);
      o[i] = tag8(__uint_as_float(sw[0]) + __uint_as_float(sw[1]), (i & 1) ? t1 : t0);
    }
    __builtin_amdgcn_raw_buffer_store_b128(o, rws, half == 0 ? WS_OA + slot * 2048 + h * 512 + l5 * 16 : OOB, 0, 16);
    STAMP(10);
    if (kv == 4) {
      const int cp = kvh * 32 + gi;
      const int goff = WS_PB + (lane >> 1) * 8192 + cp * 32 + (lane & 1) * 16;
      u32x4 pb[4];
      if (PRE_B) __builtin_amdgcn_s_sleep(PRE_B);
      unsigned spin = 0;
      for (;; spin++) {
        CBAR();
#pragma unroll
        for (int i = 0; i < 4; i++) pb[i] = LD128(rws, goff, i * 32 * 8192, 16);
        unsigned bad = 0;
#pragma unroll
        for (int i = 0; i < 4; i++) bad |= (pb[i][0] ^ t0) | (pb[i][1] ^ t1) | (pb[i][2] ^ t2) | (pb[i][3] ^ t3);
        if (__all((bad & 0xffu) == 0) || spin > SPIN_CAP) break;
        __builtin_amdgcn_s_sleep(SLEEP_B);
      }
      STAMPV(15, spin);
      STAMP(11);
      const f32x4 v = (__builtin_bit_cast(f32x4, pb[0]) + __builtin_bit_cast(f32x4, pb[1])) + (__builtin_bit_cast(f32x4, pb[2]) + __builtin_bit_cast(f32x4, pb[3]));
      auto s0 = __builtin_amdgcn_permlane32_swap(__float_as_uint(v[0]), __float_as_uint(v[2]), false, false);
      auto s1 = __builtin_amdgcn_permlane32_swap(__float_as_uint(v[1]), __float_as_uint(v[3]), false, false);
      const float y0 = __uint_as_float(s0[0]) + __uint_as_float(s0[1]), y1 = __uint_as_float(s1[0]) + __uint_as_float(s1[1]);
      auto s2 = __builtin_amdgcn_permlane16_swap(__float_as_uint(y0), __float_as_uint(y1), false, false);
      float z = __uint_as_float(s2[0]) + __uint_as_float(s2[1]);
      z += dpp<ROR8>(z); z += dpp<ROR4>(z); z += dpp<QX2>(z);
      z += lo16(xfin);
      auto ro = RSRC(out, 4096);
      __builtin_amdgcn_raw_buffer_store_b16((u16)cvtpk(z, z), ro, c < 2 ? cp * 16 + c * 8 + g * 2 : OOB, 0, 0);
      if (blk == 0) __builtin_amdgcn_raw_buffer_store_b32(T, rws, lane == 0 ? WS_EPOCH : OOB, 0, 16);
      STAMP(12);
    }
    STAMP_FLUSH();
    return;
  }
  {
    const int sq = c & 3, bb = c >> 2, h = (gi >> 3), dq = (gi >> 1) & 3;
    const int slot0 = (kvh * 4 + bb) * 8;
    const int soff = WS_ST + (slot0 + sq) * 128 + h * 8;
    u32x2 s0 = LD64(rws, soff, 0, 16), s1 = LD64(rws, soff, 512, 16);
    u32x4 fo[4]; u32x2 fd[4];
#pragma unroll
    for (int e = 0; e < 4; e++) {
#pragma unroll
      for (int j = 0; j < 4; j++) fo[e][j] = (e & 1) ? perm_hi(WO[2 * j + 1][e >> 1], WO[2 * j][e >> 1]) : perm_lo(WO[2 * j + 1][e >> 1], WO[2 * j][e >> 1]);
#pragma unroll
      for (int j = 0; j < 2; j++) fd[e][j] = (e & 1) ? perm_hi(WD[2 * j + 1][e >> 1], WD[2 * j][e >> 1]) : perm_lo(WD[2 * j + 1][e >> 1], WD[2 * j][e >> 1]);
    }
    const u32x2 hf = *(LAS u32x2*)(lds + L_H + bb * 32 + g * 8);
    f32x4 acc[4];
#pragma unroll
    for (int e = 0; e < 4; e++) acc[e] = mfma16(hf, fd[e], f32x4{0.f, 0.f, 0.f, 0.f});
    asm volatile("" : "+v"(acc[0]), "+v"(acc[1]), "+v"(acc[2]), "+v"(acc[3]), "+v"(fo[0]), "+v"(fo[1]), "+v"(fo[2]), "+v"(fo[3]));
    unsigned spin = 0;
    for (;; spin++) {
      const unsigned bad = (s0[0] ^ t0) | (s1[0] ^ t0) | (s0[1] ^ t1) | (s1[1] ^ t1);
      if (__all((bad & 0xffu) == 0) || spin > SPIN_CAP) break;
      __builtin_amdgcn_s_sleep(2);
      CBAR();
      s0 = LD64(rws, soff, 0, 16); s1 = LD64(rws, soff, 512, 16);
    }
    STAMPV(14, spin);
    const float m1 = __uint_as_float(s0[0] & 0xffffff00u), m2 = __uint_as_float(s1[0] & 0xffffff00u);
    float MM = fmaxf(m1, m2);
    MM = fmaxf(MM, dpp<QX1>(MM)); MM = fmaxf(MM, dpp<QX2>(MM));
    const float e1 = __builtin_amdgcn_exp2f((m1 - MM) * LOG2E), e2 = __builtin_amdgcn_exp2f((m2 - MM) * LOG2E);
    float L = e1 * __uint_as_float(s0[1]) + e2 * __uint_as_float(s1[1]);
    L += dpp<QX1>(L); L += dpp<QX2>(L);
    const float inv = __builtin_amdgcn_rcpf(L), w1 = e1 * inv, w2 = e2 * inv;
    float ws_[8] = {dpp<QB0>(w1), dpp<QB1>(w1), dpp<QB2>(w1), dpp<QB3>(w1), dpp<QB0>(w2), dpp<QB1>(w2), dpp<QB2>(w2), dpp<QB3>(w2)};
    asm volatile("" : "+v"(ws_[0]), "+v"(ws_[1]), "+v"(ws_[2]), "+v"(ws_[3]), "+v"(ws_[4]), "+v"(ws_[5]), "+v"(ws_[6]), "+v"(ws_[7]));
    STAMP(10);
    const int aoff = WS_OA + slot0 * 2048 + h * 512 + dq * 128 + g * 32 + sq * 8;
    u32x2 a[8];
    if (PRE_A) __builtin_amdgcn_s_sleep(PRE_A);
    for (spin = 0;; spin++) {
      CBAR();
#pragma unroll
      for (int i = 0; i < 8; i++) a[i] = LD64(rws, aoff, i * 2048, 16);
      unsigned bad = 0;
#pragma unroll
      for (int i = 0; i < 8; i++) bad |= (a[i][0] ^ t0) | (a[i][1] ^ t1);
      if (__all((bad & 0xffu) == 0) || spin > SPIN_CAP) break;
      __builtin_amdgcn_s_sleep(SLEEP_A);
    }
    STAMPV(15, spin);
    STAMP(11);
    f32x2 t = __builtin_bit_cast(f32x2, a[0]) * ws_[0];
#pragma unroll
    for (int i = 1; i < 8; i++) t += __builtin_bit_cast(f32x2, a[i]) * ws_[i];
    const unsigned own = cvtpk(t[0], t[1]);
    const u32x4 A = {own, dppu<QB1>(own), dppu<QB2>(own), dppu<QB3>(own)};
#pragma unroll
    for (int e = 0; e < 4; e++) acc[e] = mfma32(A, fo[e], acc[e]);
    const u32x4 o = {tag8(acc[0][0], t0), tag8(acc[1][0], t1), tag8(acc[2][0], t2), tag8(acc[3][0], t3)};
    __builtin_amdgcn_raw_buffer_store_b128(o, rws, WS_PB + ((kcx * 4 + g) * 512 + nh * 256 + kv * 64 + 4 * c) * 4, 0, 16);
    STAMP(12);
    STAMP_FLUSH();
  }
}

extern "C" void kernel_launch(void* const* d_in, const int* in_sizes, int n_in, void* d_out, int out_size, void* d_ws, size_t ws_size, hipStream_t stream) {
  fused<<<256, NTHR, 0, stream>>>((const u16*)d_in[1], (const u16*)d_in[2], (const u16*)d_in[0], (const u16*)d_in[3], (const u16*)d_in[6],
                                  (const u16*)d_in[7], (const u16*)d_in[4], (const u16*)d_in[8], (unsigned char*)d_ws, (u16*)d_out);
}
```

```cpp
#include <hip/hip_runtime.h>
#include <stdint.h>

#define KV_AUX 2
#define W_AUX 2
#define WO_AUX 2
#define WIN 4
#define QKPOS 8
#define BOOST 1
#define FIRSTN 4
#define PRE_Q 30
#define DLY_GU 60
#define PRE_H 90
#define ROT 2
#define CODEPF 0
#define CODE_BACK 0
#define CODE_FWD 11904
#define XB 0
#define PRE_A 0
#define PRE_B 4
#define XO_Q 1
#define XO_A 4

typedef unsigned short u16;
typedef unsigned u32x2 __attribute__((ext_vector_type(2)));
typedef unsigned u32x4 __attribute__((ext_vector_type(4)));
typedef float f32x2 __attribute__((ext_vector_type(2)));
typedef float f32x4 __attribute__((ext_vector_type(4)));
typedef short s16x4 __attribute__((ext_vector_type(4)));
typedef short s16x8 __attribute__((ext_vector_type(8)));
typedef __bf16 bf16x2 __attribute__((ext_vector_type(2)));
#define LAS __attribute__((address_space(3)))

constexpr int DM = 512, SK = 4096, DH = 128, FF = 1792;
constexpr int NT = 4, NTHR = (NT + 8) * 64;
constexpr int OA_STR = 2176, PB_STR = 1152;
constexpr int WS_EPOCH = 0;
constexpr int WS_Q = 4096;
constexpr int WS_GU = WS_Q + 262144;
constexpr int WS_ST = WS_GU + 229376;
constexpr int WS_OA = WS_ST + 32768;
constexpr int WS_PB = WS_OA + 256 * OA_STR;
constexpr int WS_END = WS_PB + 256 * PB_STR;
constexpr int WS_TS = 4 << 20;
constexpr int L_RED = 0;
constexpr int L_TR = L_RED + 16384;
constexpr int L_Q = L_TR + 4096;
constexpr int L_H = L_Q + 1024;
constexpr int L_MW = L_H + 512;
constexpr int L_LW = L_MW + 128;
constexpr int L_FLAG = L_LW + 128;
constexpr int F_Q = L_FLAG, F_H = L_FLAG + 8, F_E = L_FLAG + 12, F_VI = L_FLAG + 16, F_PV = L_FLAG + 24, F_T = L_FLAG + 32;
constexpr int L_END = L_FLAG + 64;
constexpr unsigned SPIN_CAP = 1u << 18;
constexpr int OOB = 0x7ffffff0;
constexpr float LOG2E = 1.4426950408889634f;

__device__ __forceinline__ float lo16(unsigned u) { return __uint_as_float(u << 16); }
__device__ __forceinline__ float hi16(unsigned u) { return __uint_as_float(u & 0xffff0000u); }
__device__ __forceinline__ unsigned cvtpk(float lo, float hi) { f32x2 f = {lo, hi}; return __builtin_bit_cast(unsigned, __builtin_convertvector(f, bf16x2)); }
__device__ __forceinline__ float rbf(float f) { return lo16(cvtpk(f, f)); }
__device__ __forceinline__ f32x4 mfma32(u32x4 a, u32x4 b, f32x4 c) { return __builtin_amdgcn_mfma_f32_16x16x32_bf16(__builtin_bit_cast(s16x8, a), __builtin_bit_cast(s16x8, b), c, 0, 0, 0); }
__device__ __forceinline__ f32x4 mfma16(u32x2 a, u32x2 b, f32x4 c) { return __builtin_amdgcn_mfma_f32_16x16x16bf16_1k(__builtin_bit_cast(s16x4, a), __builtin_bit_cast(s16x4, b), c, 0, 0, 0); }
template <int CTRL> __device__ __forceinline__ float dpp(float x) { return __builtin_bit_cast(float, __builtin_amdgcn_mov_dpp(__builtin_bit_cast(int, x), CTRL, 0xf, 0xf, true)); }
template <int CTRL> __device__ __forceinline__ unsigned dppu(unsigned x) { return (unsigned)__builtin_amdgcn_mov_dpp((int)x, CTRL, 0xf, 0xf, true); }
constexpr int ROR4 = 0x124, ROR8 = 0x128, HMIR = 0x141, QX1 = 0xB1, QX2 = 0x4E, QB0 = 0x00, QB1 = 0x55, QB2 = 0xAA, QB3 = 0xFF;
__device__ __forceinline__ float xrow_max(float x) {
  auto s = __builtin_amdgcn_permlane16_swap(__float_as_uint(x), __float_as_uint(x), false, false);
  x = fmaxf(__uint_as_float(s[0]), __uint_as_float(s[1]));
  auto t = __builtin_amdgcn_permlane32_swap(__float_as_uint(x), __float_as_uint(x), false, false);
  return fmaxf(__uint_as_float(t[0]), __uint_as_float(t[1]));
}
__device__ __forceinline__ float xrow_sum(float x) {
  auto s = __builtin_amdgcn_permlane16_swap(__float_as_uint(x), __float_as_uint(x), false, false);
  x = __uint_as_float(s[0]) + __uint_as_float(s[1]);
  auto t = __builtin_amdgcn_permlane32_swap(__float_as_uint(x), __float_as_uint(x), false, false);
  return __uint_as_float(t[0]) + __uint_as_float(t[1]);
}
__device__ __forceinline__ float half_sum(float x) {
  auto t = __builtin_amdgcn_permlane32_swap(__float_as_uint(x), __float_as_uint(x), false, false);
  return __uint_as_float(t[0]) + __uint_as_float(t[1]);
}
__device__ __forceinline__ float dot2(unsigned a, unsigned b, float c) { return __builtin_amdgcn_fdot2_f32_bf16(__builtin_bit_cast(bf16x2, a), __builtin_bit_cast(bf16x2, b), c, false); }
__device__ __forceinline__ unsigned tag8(float v, unsigned tb) { return (__float_as_uint(v) & 0xffffff00u) | tb; }
__device__ __forceinline__ unsigned perm_lo(unsigned hi, unsigned lo) { return __builtin_amdgcn_perm(hi, lo, 0x05040100u); }
__device__ __forceinline__ unsigned perm_hi(unsigned hi, unsigned lo) { return __builtin_amdgcn_perm(hi, lo, 0x07060302u); }
__device__ __forceinline__ unsigned or4(u32x4 v) { return (v[0] | v[1]) | (v[2] | v[3]); }
#define F4(v) __builtin_bit_cast(f32x4, v)
#define SB() __builtin_amdgcn_sched_barrier(0)
#define CBAR() asm volatile("" ::: "memory")
#define OPQ(x) asm volatile("" : "+v"(x))
#define LGKM0() asm volatile("s_waitcnt lgkmcnt(0)" ::: "memory")
#define RSRC(p, n) __builtin_amdgcn_make_buffer_rsrc((void*)(p), 0, (n), 0x00020000)
#define LD32(r, v, s, a) __builtin_amdgcn_raw_buffer_load_b32(r, v, s, a)
#define LD64(r, v, s, a) __builtin_amdgcn_raw_buffer_load_b64(r, v, s, a)
#define LD128(r, v, s, a) __builtin_amdgcn_raw_buffer_load_b128(r, v, s, a)
#define LDSV(T, off) (*(volatile LAS T*)(lds + (off)))
#define LDS_INC(off) __hip_atomic_fetch_add((LAS unsigned*)(lds + (off)), 1u, __ATOMIC_RELAXED, __HIP_MEMORY_SCOPE_WORKGROUP)

#define STAMP2(i)
#define WSTAMP2(n, i)
#define STAMP(i)
#define STAMPV(i, val)
#define STAMP_FLUSH()

__device__ __forceinline__ void lds_wait_eq(LAS unsigned char* lds, int off, unsigned n) {
  for (unsigned spin = 0; LDSV(unsigned, off) != n && spin < (SPIN_CAP << 4); spin++) __builtin_amdgcn_s_sleep(1);
  CBAR();
}
__device__ __forceinline__ void lds_wait8(LAS unsigned char* lds, int off) {
  for (unsigned spin = 0; spin < (SPIN_CAP << 4); spin++) {
    const u32x2 f = LDSV(u32x2, off);
    if ((f[0] & f[1]) == 0x01010101u) break;
    __builtin_amdgcn_s_sleep(1);
  }
  CBAR();
}

__global__ void __launch_bounds__(NTHR, 3) fused(const u16* kc, const u16* vc, const u16* x, const u16* wq, const u16* wg, const u16* wu,
                                                 const u16* wo, const u16* wd, unsigned char* ws, u16* out) {
  __shared__ __attribute__((aligned(16))) unsigned char smem[L_END];
  unsigned long long pc0;
  asm volatile("s_getpc_b64 %0" : "=s"(pc0));
  LAS unsigned char* lds = (LAS unsigned char*)smem;
  const int blk = blockIdx.x, kvh = blk & 7, gi = blk >> 3, b = gi & 3, s = gi >> 2;
  const int tid = threadIdx.x, lane = tid & 63, w = __builtin_amdgcn_readfirstlane(tid >> 6), g = lane >> 4, c = lane & 15;
  STAMP(0);
  asm volatile("" : "+s"(kc), "+s"(vc), "+s"(x), "+s"(wq), "+s"(wg), "+s"(wu), "+s"(wo), "+s"(wd), "+s"(ws), "+s"(out));
  auto rws = RSRC(ws, WS_END);
  const int slot = (kvh * 4 + b) * 8 + s;
  if (w < NT) {
    __builtin_amdgcn_s_setprio(3);
    const int kva = (kvh + XO_A) & 7, hl = gi >> 3, hh = kva * 4 + hl, ng = gi & 7;
    const unsigned ep = LD32(rws, WS_EPOCH, 0, 16);
    if (w == 2) {
      LDSV(unsigned, L_MW + lane * 4) = 0u;
      if (lane < 16) LDSV(unsigned, L_FLAG + lane * 4) = 0u;
      LGKM0();
    }
    if (w != 0) {
      __builtin_amdgcn_s_barrier();
      asm volatile("s_waitcnt vmcnt(0)" ::: "memory");
      LDSV(unsigned char, F_E + w) = 1;
    }
    if (w == 0 || (w == 1 && gi < 28)) {
      const int kvq = (kvh + XO_Q) & 7;
      const int tq = kvh * 28 + gi, mat = tq >= 112 ? 1 : 0, rem = tq - 112 * mat;
      const int kq = (w == 0) ? (gi & 3) : (rem & 3), cb = (w == 0) ? (gi >> 2) : (rem >> 2);
      const int rstride = (w == 0) ? 8192 : FF * 2;
      const u16* wbase = ((w == 0) ? wq + kvq * 512 : (mat ? wu : wg)) + cb * 64 + (size_t)kq * 64 * rstride;
      auto rw = RSRC(wbase, 127 * rstride + 128);
      auto rx = RSRC(x, 4096);
      const int col = cb * 64 + (c & 7) * 8 + (c >> 3) * 4;
      const int poff = (w == 0) ? WS_Q + (((kvq * 4 + g) * 4 + kq) * 512 + col) * 4 : WS_GU + (((mat * 4 + g) * 4 + kq) * FF + col) * 4;
      u32x4 xs[4], xf[2], W[16];
      if (w == 1) __builtin_amdgcn_s_sleep(DLY_GU);
#pragma unroll
      for (int i = 0; i < 4; i++) xs[i] = LD128(rx, (c >> 2) * 1024 + g * 64 + (c & 3) * 16 + i * 256, 0, 0);
#pragma unroll
      for (int i = 0; i < 2; i++) xf[i] = LD128(rx, (c >> 2) * 1024 + kq * 256 + (c & 1) * 64 + g * 16 + i * 128, 0, 0);
      const int woff = ((c >> 3) * 32 + g * 8) * rstride + (c & 7) * 16;
#pragma unroll
      for (int j = 0; j < 16; j++) W[j] = LD128(rw, woff, ((j >> 3) * 64 + (j & 7)) * rstride, W_AUX);
      SB();
      if (w == 0) __builtin_amdgcn_s_barrier();
      SB();
      STAMP(1);
      float sm = 0.f;
#pragma unroll
      for (int i = 0; i < 4; i++)
#pragma unroll
        for (int j = 0; j < 4; j++) sm = dot2(xs[i][j], 0x3f803f80u, sm);
      sm += dpp<QX1>(sm); sm += dpp<QX2>(sm);
      const float mean = xrow_sum(sm) * (1.f / DM);
      f32x2 ssv = {0.f, 0.f};
#pragma unroll
      for (int i = 0; i < 4; i++)
#pragma unroll
        for (int j = 0; j < 4; j++) { const f32x2 d = f32x2{lo16(xs[i][j]), hi16(xs[i][j])} - mean; ssv += d * d; }
      float ss = ssv[0] + ssv[1];
      ss += dpp<QX1>(ss); ss += dpp<QX2>(ss);
      const float var = rbf(xrow_sum(ss) * (1.f / DM)), mb = rbf(mean);
      const float den = rbf(__builtin_amdgcn_sqrtf(rbf(var + 1e-5f)));
      const float rden = __builtin_amdgcn_rcpf(den);
#pragma unroll
      for (int i = 0; i < 2; i++)
#pragma unroll
        for (int j = 0; j < 4; j++) {
          const unsigned d = cvtpk(lo16(xf[i][j]) - mb, hi16(xf[i][j]) - mb);
          const f32x2 n = {lo16(d), hi16(d)};
          const f32x2 q = n * rden;
          const f32x2 r = (n - q * den) * rden + q;
          xf[i][j] = cvtpk(r[0], r[1]);
        }
      SB();
      STAMP(2);
      f32x4 acc[8];
#pragma unroll
      for (int e = 0; e < 8; e++) acc[e] = f32x4{0.f, 0.f, 0.f, 0.f};
#pragma unroll
      for (int t = 0; t < 2; t++) {
#pragma unroll
        for (int e = 0; e < 8; e++) {
          u32x4 f;
#pragma unroll
          for (int j = 0; j < 4; j++) f[j] = (e & 1) ? perm_hi(W[t * 8 + 2 * j + 1][e >> 1], W[t * 8 + 2 * j][e >> 1]) : perm_lo(W[t * 8 + 2 * j + 1][e >> 1], W[t * 8 + 2 * j][e >> 1]);
          acc[e] = mfma32(xf[t], f, acc[e]);
        }
        SB();
      }
      const unsigned T0 = __builtin_amdgcn_readfirstlane(ep) + 0x01010101u;
      u32x4 o;
#pragma unroll
      for (int i = 0; i < 4; i++) {
        const float a = acc[i][0] + dpp<ROR8>(acc[i][1]), bq = acc[4 + i][1] + dpp<ROR8>(acc[4 + i][0]);
        o[i] = tag8(c < 8 ? a : bq, (T0 >> (8 * i)) & 0xffu);
      }
      STAMP(3);
      __builtin_amdgcn_raw_buffer_store_b128(o, rws, poff, 0, 16);
      if (w == 0) LDSV(unsigned char, F_E) = 1;
      STAMP(4);
    }
    const unsigned T = __builtin_amdgcn_readfirstlane(ep) + 0x01010101u;
    const u32x4 TG = {T & 0xffu, (T >> 8) & 0xffu, (T >> 16) & 0xffu, T >> 24};
    if (w == 2) {
      const int goff = WS_Q + (kvh * 4 + b) * 8192 + lane * 16;
      u32x4 q[8];
      lds_wait_eq(lds, F_E, 0x01010101u);
      __builtin_amdgcn_s_sleep(PRE_Q);
      STAMP(3);
      unsigned spin = 0;
      for (;; spin++) {
        CBAR();
#pragma unroll
        for (int i = 0; i < 8; i++) q[i] = LD128(rws, goff, i * 1024, 16);
        unsigned bad = 0;
#pragma unroll
        for (int i = 0; i < 8; i++) { q[i] ^= TG; bad |= or4(q[i]); }
        if (__all((bad & 0xffu) == 0) || spin > SPIN_CAP) break;
        __builtin_amdgcn_s_sleep(4);
      }
      STAMPV(15, spin);
#pragma unroll
      for (int hf = 0; hf < 2; hf++) {
        const f32x4 a = (F4(q[hf]) + F4(q[2 + hf])) + (F4(q[4 + hf]) + F4(q[6 + hf]));
        LDSV(u32x2, L_Q + hf * 512 + lane * 8) = u32x2{cvtpk(a[0], a[1]), cvtpk(a[2], a[3])};
      }
      LDSV(u32x2, F_Q) = u32x2{1u, T};
      STAMP(4);
    } else if (w == 3) {
      const int goff = WS_GU + (g * 4 * FF + hh * 56 + (c < 14 ? c : 13) * 4) * 4;
      u32x4 gv[8];
      lds_wait_eq(lds, F_Q, 1u);
      __builtin_amdgcn_s_sleep(PRE_H);
      STAMP(3);
      unsigned spin = 0;
      for (;; spin++) {
        CBAR();
#pragma unroll
        for (int i = 0; i < 8; i++) gv[i] = LD128(rws, goff, ((i >> 2) * 16 + (i & 3)) * FF * 4, 16);
        unsigned bad = 0;
#pragma unroll
        for (int i = 0; i < 8; i++) { gv[i] ^= TG; bad |= or4(gv[i]); }
        if (__all((bad & 0xffu) == 0) || spin > SPIN_CAP) break;
        __builtin_amdgcn_s_sleep(8);
      }
      STAMPV(15, spin);
      const f32x4 gs = (F4(gv[0]) + F4(gv[1])) + (F4(gv[2]) + F4(gv[3])), us = (F4(gv[4]) + F4(gv[5])) + (F4(gv[6]) + F4(gv[7]));
      u32x2 hp;
#pragma unroll
      for (int i = 0; i < 2; i++) {
        const unsigned gb = cvtpk(gs[2 * i], gs[2 * i + 1]), ub = cvtpk(us[2 * i], us[2 * i + 1]);
        hp[i] = c < 14 ? cvtpk(lo16(gb) * lo16(ub), hi16(gb) * hi16(ub)) : 0u;
      }
      LDSV(u32x2, L_H + g * 128 + c * 8) = hp;
      LDSV(unsigned, F_H) = 1u;
      STAMP(4);
    }
    lds_wait8(lds, F_VI);
    STAMP(5);
    u32x2 WO[8], WD[4];
    {
      auto rwo = RSRC(wo + (size_t)(hh * 128 + w * 32) * DM + ng * 64, 31 * 1024 + 128);
      auto rwd = RSRC(wd + (size_t)(hh * 56 + w * 16) * DM + ng * 64, (w == 3 ? 7 : 15) * 1024 + 128);
      const int rb = (16 * (g & 1) + 2 * (g >> 1)) * 1024 + c * 8;
#pragma unroll
      for (int j = 0; j < 8; j++) WO[j] = LD64(rwo, rb + (j & 1) * 1024, (j >> 1) * 4096, WO_AUX);
#pragma unroll
      for (int j = 0; j < 4; j++) WD[j] = LD64(rwd, g * 4096 + c * 8 + j * 1024, 0, WO_AUX);
      SB();
    }
    if (w == 0) {
      unsigned lv;
      for (unsigned spin = 0;; spin++) {
        lv = LDSV(unsigned, L_LW + (lane & 31) * 4);
        if (__all(lv != 0u) || spin > (SPIN_CAP << 4)) break;
        __builtin_amdgcn_s_sleep(1);
      }
      float mv = hi16(LDSV(unsigned, L_MW + (lane & 31) * 4));
      float lsum = __uint_as_float(lv);
      lsum += dpp<QX1>(lsum); lsum += dpp<QX2>(lsum); lsum += dpp<HMIR>(lsum);
      mv = fmaxf(mv, dpp<QX1>(mv)); mv = fmaxf(mv, dpp<QX2>(mv)); mv = fmaxf(mv, dpp<HMIR>(mv));
      const u32x2 st = {tag8(mv, TG[0]), tag8(lsum, TG[1])};
      if ((lane & 39) == 0) __builtin_amdgcn_raw_buffer_store_b64(st, rws, WS_ST + slot * 128 + (lane >> 3) * 8, 0, 16);
    }
    STAMP(6);
    const int bb = c >> 2, q4 = c & 3, sh = g >> 1;
    const int slot0 = (kva * 4 + bb) * 8 + sh * 4;
    u32x4 fo[4];
    f32x4 acc[4];
    {
      u32x2 fd[4];
#pragma unroll
      for (int e = 0; e < 4; e++) {
#pragma unroll
        for (int j = 0; j < 4; j++) fo[e][j] = (e & 1) ? perm_hi(WO[2 * j + 1][e >> 1], WO[2 * j][e >> 1]) : perm_lo(WO[2 * j + 1][e >> 1], WO[2 * j][e >> 1]);
#pragma unroll
        for (int j = 0; j < 2; j++) fd[e][j] = (e & 1) ? perm_hi(WD[2 * j + 1][e >> 1], WD[2 * j][e >> 1]) : perm_lo(WD[2 * j + 1][e >> 1], WD[2 * j][e >> 1]);
      }
      lds_wait_eq(lds, F_H, 1u);
      const u32x2 hf = LDSV(u32x2, L_H + bb * 128 + w * 32 + g * 8);
#pragma unroll
      for (int e = 0; e < 4; e++) acc[e] = mfma16(hf, fd[e], f32x4{0.f, 0.f, 0.f, 0.f});
      asm volatile("" : "+v"(acc[0]), "+v"(acc[1]), "+v"(acc[2]), "+v"(acc[3]), "+v"(fo[0]), "+v"(fo[1]), "+v"(fo[2]), "+v"(fo[3]));
    }
    STAMP(7);
    float wt[4];
    {
      const int soff = WS_ST + (slot0 + q4) * 128 + hl * 8;
      u32x2 s0;
      unsigned spin = 0;
      for (;; spin++) {
        CBAR();
        s0 = LD64(rws, soff, 0, 16);
        s0[0] ^= TG[0]; s0[1] ^= TG[1];
        if (__all(((s0[0] | s0[1]) & 0xffu) == 0) || spin > SPIN_CAP) break;
        __builtin_amdgcn_s_sleep(4);
      }
      STAMPV(14, spin);
      const float m1 = __uint_as_float(s0[0]);
      float MM = fmaxf(m1, dpp<QX1>(m1));
      MM = fmaxf(MM, dpp<QX2>(MM));
      { auto t = __builtin_amdgcn_permlane32_swap(__float_as_uint(MM), __float_as_uint(MM), false, false); MM = fmaxf(__uint_as_float(t[0]), __uint_as_float(t[1])); }
      const float e1 = __builtin_amdgcn_exp2f((m1 - MM) * LOG2E);
      float L = e1 * __uint_as_float(s0[1]);
      L += dpp<QX1>(L); L += dpp<QX2>(L);
      L = half_sum(L);
      const float w1 = e1 * __builtin_amdgcn_rcpf(L);
      wt[0] = dpp<QB0>(w1); wt[1] = dpp<QB1>(w1); wt[2] = dpp<QB2>(w1); wt[3] = dpp<QB3>(w1);
      asm volatile("" : "+v"(wt[0]), "+v"(wt[1]), "+v"(wt[2]), "+v"(wt[3]));
    }
    STAMP(8);
    lds_wait8(lds, F_PV);
    if (PRE_A) __builtin_amdgcn_s_sleep(PRE_A);
    STAMP(9);
    const int aoff = WS_OA + slot0 * OA_STR + hl * 512 + w * 128 + (g & 1) * 64 + q4 * 16;
    u32x4 a[4];
    {
      unsigned spin = 0;
      for (;; spin++) {
        CBAR();
#pragma unroll
        for (int i = 0; i < 4; i++) a[i] = LD128(rws, aoff, i * OA_STR, 16);
        unsigned bad = 0;
#pragma unroll
        for (int i = 0; i < 4; i++) { a[i] ^= TG; bad |= or4(a[i]); }
        if (__all((bad & 0xffu) == 0) || spin > SPIN_CAP) break;
        __builtin_amdgcn_s_sleep(1);
      }
      STAMPV(13, spin);
    }
    STAMP(10);
    const f32x4 t = (F4(a[0]) * wt[0] + F4(a[1]) * wt[1]) + (F4(a[2]) * wt[2] + F4(a[3]) * wt[3]);
    auto s0 = __builtin_amdgcn_permlane32_swap(__float_as_uint(t[0]), __float_as_uint(t[2]), false, false);
    auto s1 = __builtin_amdgcn_permlane32_swap(__float_as_uint(t[1]), __float_as_uint(t[3]), false, false);
    const unsigned own = cvtpk(__uint_as_float(s0[0]) + __uint_as_float(s0[1]), __uint_as_float(s1[0]) + __uint_as_float(s1[1]));
    const u32x4 A = {own, dppu<QB1>(own), dppu<QB2>(own), dppu<QB3>(own)};
#pragma unroll
    for (int e = 0; e < 4; e++) acc[e] = mfma32(A, fo[e], acc[e]);
    LDSV(f32x4, L_TR + w * 1024 + lane * 16) = f32x4{acc[0][0], acc[1][0], acc[2][0], acc[3][0]};
    unsigned prev = 0;
    CBAR();
    if (lane == 0) prev = LDS_INC(F_T);
    CBAR();
    f32x4 r[4];
#pragma unroll
    for (int i = 0; i < 4; i++) r[i] = LDSV(f32x4, L_TR + i * 1024 + lane * 16);
    if (__builtin_amdgcn_readfirstlane(prev) == 3u) {
      const f32x4 z = (r[0] + r[1]) + (r[2] + r[3]);
      const u32x4 o = {tag8(z[0], TG[0]), tag8(z[1], TG[1]), tag8(z[2], TG[2]), tag8(z[3], TG[3])};
      __builtin_amdgcn_raw_buffer_store_b128(o, rws, WS_PB + (hh * 8 + ng) * PB_STR + lane * 16, 0, 16);
    }
    STAMP(11);
    STAMP_FLUSH();
    return;
  }
  const int kv = w - NT;
  const size_t base = ((size_t)(b * 8 + kvh) * SK + s * 512) * DH;
  auto rk = RSRC(kc + base, 131072);
  auto rv = RSRC(vc + base, 131072);
  u32x4 K[16], V[16];
  const int koff = (c >> 2) * 256 + g * 64 + (c & 3) * 16;
  const int voff = lane * 16;
#define GX(i) (((i) & 1) * 2 + (((i) >> 1) & 1) * 4 + (((i) >> 2) & 1))
#define SOFF(i) ((((i) >> 2) * 8 + (kv ^ GX(i))) * 4096)
#define WAITWIN() asm volatile("s_waitcnt vmcnt(%0)" ::"n"(WIN - 1) : "memory")
#define VLOAD(i) do { V[i] = LD128(rv, voff + ((i) & 3) * 1024, SOFF(i), KV_AUX); SB(); } while (0)
#pragma unroll
  for (int i = 0; i < 16; i++) {
    if (i == FIRSTN) { STAMP2(1); __builtin_amdgcn_s_barrier(); }
    if (i >= WIN) WAITWIN();
    if (i >= WIN && i < WIN + 11) STAMP2(2 + i - WIN);
    K[i] = LD128(rk, koff + (i & 3) * 1024, SOFF(i), KV_AUX);
    SB();
  }
  STAMP(1);
#pragma unroll
  for (int i = 0; i < QKPOS; i++) { if (i < QKPOS - BOOST) WAITWIN(); VLOAD(i); }
  STAMP(2);
  u32x2 fl = LDSV(u32x2, F_Q);
  for (unsigned spin = 0; fl[0] == 0u && spin < (SPIN_CAP << 4); spin++) { __builtin_amdgcn_s_sleep(1); fl = LDSV(u32x2, F_Q); }
  CBAR();
  const unsigned T = __builtin_amdgcn_readfirstlane(fl[1]);
  const u32x4 TG = {T & 0xffu, (T >> 8) & 0xffu, (T >> 16) & 0xffu, T >> 24};
  STAMP(3);
  float S[4];
  {
    const u32x4 qf = LDSV(u32x4, L_Q + (c >> 2) * 256 + g * 64 + (c & 3) * 16);
    const bool a1 = c & 1, a2 = c & 2;
#pragma unroll
    for (int t = 0; t < 4; t++) {
      float y[4];
#pragma unroll
      for (int i = 0; i < 4; i++) {
        const f32x4 d = mfma32(K[t * 4 + i], qf, f32x4{0.f, 0.f, 0.f, 0.f});
        float z = dpp<QB0>(d[0]);
        z += dpp<QB1>(d[1]); OPQ(z);
        z += dpp<QB2>(d[2]); OPQ(z);
        z += dpp<QB3>(d[3]); OPQ(z);
        y[i] = z;
      }
      const float u0 = a1 ? y[1] : y[0], u1 = a1 ? y[3] : y[2];
      S[t] = a2 ? u1 : u0;
    }
  }
  {
#pragma unroll
    for (int i = 0; i < 4; i += 2) {
      const unsigned p = cvtpk(S[i], S[i + 1]);
      const unsigned p2 = cvtpk(lo16(p) * 0.08838834764831845f, hi16(p) * 0.08838834764831845f);
      S[i] = lo16(p2); S[i + 1] = hi16(p2);
    }
    float mw = fmaxf(fmaxf(S[0], S[1]), fmaxf(S[2], S[3]));
    mw = fmaxf(mw, dpp<QX1>(mw)); mw = fmaxf(mw, dpp<QX2>(mw));
    mw = xrow_max(mw);
    if ((lane & 51) == 0) LDSV(unsigned, L_MW + (c >> 2) * 32 + kv * 4) = __float_as_uint(mw) | 1u;
  }
  STAMP(4);
  SB();
#pragma unroll
  for (int i = QKPOS; i < 16; i++) { WAITWIN(); VLOAD(i); }
  LDSV(unsigned char, F_VI + kv) = 1;
  STAMP(5);
  u32x4 P[2];
  {
    u32x4 m0, m1;
    for (unsigned spin = 0;; spin++) {
      m0 = LDSV(u32x4, L_MW + (c >> 2) * 32); m1 = LDSV(u32x4, L_MW + (c >> 2) * 32 + 16);
      const u32x4 mm = m0 & m1;
      if (__all((mm[0] & mm[1] & mm[2] & mm[3] & 1u) != 0u) || spin > (SPIN_CAP << 4)) break;
      __builtin_amdgcn_s_sleep(1);
    }
    float M = -INFINITY;
#pragma unroll
    for (int i = 0; i < 4; i++) M = fmaxf(M, fmaxf(hi16(m0[i]), hi16(m1[i])));
    const float mneg = -M * LOG2E;
    float p[4];
#pragma unroll
    for (int i = 0; i < 4; i++) p[i] = __builtin_amdgcn_exp2f(fmaf(S[i], LOG2E, mneg));
    float lsum = (p[0] + p[1]) + (p[2] + p[3]);
    lsum += dpp<QX1>(lsum); lsum += dpp<QX2>(lsum);
    lsum = xrow_sum(lsum);
    if ((lane & 51) == 0) LDSV(unsigned, L_LW + (c >> 2) * 32 + kv * 4) = __float_as_uint(lsum) | 1u;
#pragma unroll
    for (int u = 0; u < 2; u++) { const unsigned pk = cvtpk(p[2 * u], p[2 * u + 1]); P[u] = u32x4{pk, dppu<QB1>(pk), dppu<QB2>(pk), dppu<QB3>(pk)}; }
  }
  STAMP(6);
  SB();
  f32x4 O[8];
  {
#pragma unroll
    for (int i = 0; i < 4; i++) {
      u32x4 f0, f1;
#pragma unroll
      for (int j = 0; j < 4; j++) { f0[j] = perm_lo(V[4 + j][i], V[j][i]); f1[j] = perm_hi(V[4 + j][i], V[j][i]); }
      O[2 * i] = mfma32(P[0], f0, f32x4{0.f, 0.f, 0.f, 0.f}); O[2 * i + 1] = mfma32(P[0], f1, f32x4{0.f, 0.f, 0.f, 0.f});
    }
    SB();
    u32x4 F[8];
#pragma unroll
    for (int j = 0; j < 3; j++) {
#pragma unroll
      for (int i = 0; i < 4; i++) { F[2 * i][j] = perm_lo(V[12 + j][i], V[8 + j][i]); F[2 * i + 1][j] = perm_hi(V[12 + j][i], V[8 + j][i]); }
      SB();
    }
    STAMP(7);
#pragma unroll
    for (int i = 0; i < 4; i++) {
      F[2 * i][3] = perm_lo(V[15][i], V[11][i]); F[2 * i + 1][3] = perm_hi(V[15][i], V[11][i]);
      O[2 * i] = mfma32(P[1], F[2 * i], O[2 * i]); O[2 * i + 1] = mfma32(P[1], F[2 * i + 1], O[2 * i + 1]);
    }
  }
  LDSV(f32x4, L_RED + kv * 2048 + lane * 32) = f32x4{O[0][0], O[1][0], O[2][0], O[3][0]};
  LDSV(f32x4, L_RED + kv * 2048 + lane * 32 + 16) = f32x4{O[4][0], O[5][0], O[6][0], O[7][0]};
  LDSV(unsigned char, F_PV + kv) = 1;
  STAMP(8);
  if (kv < 4) {
    const int half = lane >> 5, l5 = lane & 31;
    f32x4 r[4];
    for (unsigned spin = 0;; spin++) {
      const u32x2 cnt = LDSV(u32x2, F_PV);
#pragma unroll
      for (int i = 0; i < 4; i++) r[i] = LDSV(f32x4, L_RED + (half * 4 + i) * 2048 + kv * 512 + l5 * 16);
      if ((cnt[0] & cnt[1]) == 0x01010101u || spin > (SPIN_CAP << 4)) break;
    }
    STAMP(9);
    const f32x4 a = (r[0] + r[1]) + (r[2] + r[3]);
    u32x4 o;
#pragma unroll
    for (int i = 0; i < 4; i++) o[i] = tag8(half_sum(a[i]), TG[i]);
    if (half == 0) __builtin_amdgcn_raw_buffer_store_b128(o, rws, WS_OA + slot * OA_STR + kv * 512 + l5 * 16, 0, 16);
    STAMP(10);
  } else if (kv == 4 && XB == 0) {
    const int oi = (gi >> 3) * 512 + kvh * 64 + (gi & 7) * 8 + (c & 1) * 4 + g;
    auto rx = RSRC(x, 4096);
    const unsigned xfin = __builtin_amdgcn_raw_buffer_load_b16(rx, oi * 2, 0, 0);
    const int goff = WS_PB + ((lane >> 1) * 8 + kvh) * PB_STR + gi * 32 + (lane & 1) * 16;
    lds_wait_eq(lds, F_T, 4u);
    if (PRE_B) __builtin_amdgcn_s_sleep(PRE_B);
    STAMP(10);
    u32x4 pb;
    unsigned spin = 0;
    for (;; spin++) {
      CBAR();
      pb = LD128(rws, goff, 0, 16);
      pb ^= TG;
      if (__all((or4(pb) & 0xffu) == 0) || spin > SPIN_CAP) break;
      __builtin_amdgcn_s_sleep(1);
    }
    STAMPV(15, spin);
    STAMP(11);
    auto s0 = __builtin_amdgcn_permlane32_swap(pb[0], pb[2], false, false);
    auto s1 = __builtin_amdgcn_permlane32_swap(pb[1], pb[3], false, false);
    const float y0 = __uint_as_float(s0[0]) + __uint_as_float(s0[1]), y1 = __uint_as_float(s1[0]) + __uint_as_float(s1[1]);
    auto s2 = __builtin_amdgcn_permlane16_swap(__float_as_uint(y0), __float_as_uint(y1), false, false);
    float z = __uint_as_float(s2[0]) + __uint_as_float(s2[1]);
    z += dpp<ROR8>(z); z += dpp<ROR4>(z); z += dpp<QX2>(z);
    z += lo16(xfin);
    auto ro = RSRC(out, 4096);
    if (c < 2) __builtin_amdgcn_raw_buffer_store_b16((u16)cvtpk(z, z), ro, oi * 2, 0, 0);
    if (blk == 0 && lane == 0) __builtin_amdgcn_raw_buffer_store_b32(T, rws, WS_EPOCH, 0, 16);
    STAMP(12);
  }
  STAMP_FLUSH();
}

extern "C" void kernel_launch(void* const* d_in, const int* in_sizes, int n_in, void* d_out, int out_size, void* d_ws, size_t ws_size, hipStream_t stream) {
  fused<<<256, NTHR, 0, stream>>>((const u16*)d_in[1], (const u16*)d_in[2], (const u16*)d_in[0], (const u16*)d_in[3], (const u16*)d_in[6],
                                  (const u16*)d_in[7], (const u16*)d_in[4], (const u16*)d_in[8], (unsigned char*)d_ws, (u16*)d_out);
}
```

```cpp
#include <hip/hip_runtime.h>
#include <stdint.h>

#define KV_AUX 2
#define W_AUX 2
#define WO_AUX 2
#define WIN 6
#define WIN2 WIN
#define ENDPOS 12
#define QKPOS 5
#define BOOST 0
#define FIRSTN 1
#define PRE_Q 60
#define GUPOS -1
#define DLY_GU 60
#define PRE_H 8
#define PRE_H2 60
#define WOLATE 0
#define ORD 1
#define CODEPF 0
#define CODE_FWD 10240
#define PRE_A 18
#define PRE_B 8
#define TAU 320
#define N0 2
#define TOFF 40
#define AB 0
#define XAUX 0
#define EAUX 16
#define SMPOS 8
#define PVPOS 12
#define WOPOS 16
#define NFREE 0
#define WTAU 0
#define STPOS 13
#define QAUX 18
#define GAUX 18
#define OAS 16
#define OAL 16
#define PAS 16
#define PAL 16
#define SAS 16
#define SAL 16
#define XO_Q 1
#define XO_A 4

typedef unsigned short u16;
typedef unsigned u32x2 __attribute__((ext_vector_type(2)));
typedef unsigned u32x4 __attribute__((ext_vector_type(4)));
typedef float f32x2 __attribute__((ext_vector_type(2)));
typedef float f32x4 __attribute__((ext_vector_type(4)));
typedef short s16x4 __attribute__((ext_vector_type(4)));
typedef short s16x8 __attribute__((ext_vector_type(8)));
typedef __bf16 bf16x2 __attribute__((ext_vector_type(2)));
#define LAS __attribute__((address_space(3)))

constexpr int DM = 512, SK = 4096, DH = 128, FF = 1792;
constexpr int NT = 4, NTHR = (NT + 8) * 64;
constexpr int OA_STR = 2176, PB_STR = 1152;
constexpr int WS_EPOCH = 0;
constexpr int WS_Q = 4096;
constexpr int WS_GU = WS_Q + 262144;
constexpr int WS_ST = WS_GU + 229376;
constexpr int WS_OA = WS_ST + 32768;
constexpr int WS_PB = WS_OA + 256 * OA_STR;
constexpr int WS_END = WS_PB + 256 * PB_STR;
constexpr int WS_TS = 4 << 20;
constexpr int L_RED = 0;
constexpr int L_TR = L_RED + 16384;
constexpr int L_Q = L_TR + 4096;
constexpr int L_H = L_Q + 1024;
constexpr int L_MW = L_H + 512;
constexpr int L_LW = L_MW + 128;
constexpr int L_FLAG = L_LW + 128;
constexpr int F_Q = L_FLAG, F_H = L_FLAG + 8, F_E = L_FLAG + 12, F_VI = L_FLAG + 16, F_PV = L_FLAG + 24, F_T = L_FLAG + 32, F_GP = L_FLAG + 36, F_G = L_FLAG + 40, F_S = L_FLAG + 48;
constexpr int L_END = L_FLAG + 64;
constexpr unsigned SPIN_CAP = 1u << 18;
constexpr float LOG2E = 1.4426950408889634f;

__device__ __forceinline__ float lo16(unsigned u) { return __uint_as_float(u << 16); }
__device__ __forceinline__ float hi16(unsigned u) { return __uint_as_float(u & 0xffff0000u); }
__device__ __forceinline__ unsigned cvtpk(float lo, float hi) { f32x2 f = {lo, hi}; return __builtin_bit_cast(unsigned, __builtin_convertvector(f, bf16x2)); }
__device__ __forceinline__ float rbf(float f) { return lo16(cvtpk(f, f)); }
__device__ __forceinline__ f32x4 mfma32(u32x4 a, u32x4 b, f32x4 c) { return __builtin_amdgcn_mfma_f32_16x16x32_bf16(__builtin_bit_cast(s16x8, a), __builtin_bit_cast(s16x8, b), c, 0, 0, 0); }
__device__ __forceinline__ f32x4 mfma16(u32x2 a, u32x2 b, f32x4 c) { return __builtin_amdgcn_mfma_f32_16x16x16bf16_1k(__builtin_bit_cast(s16x4, a), __builtin_bit_cast(s16x4, b), c, 0, 0, 0); }
template <int CTRL> __device__ __forceinline__ float dpp(float x) { return __builtin_bit_cast(float, __builtin_amdgcn_mov_dpp(__builtin_bit_cast(int, x), CTRL, 0xf, 0xf, true)); }
template <int CTRL> __device__ __forceinline__ unsigned dppu(unsigned x) { return (unsigned)__builtin_amdgcn_mov_dpp((int)x, CTRL, 0xf, 0xf, true); }
constexpr int ROR4 = 0x124, ROR8 = 0x128, HMIR = 0x141, QX1 = 0xB1, QX2 = 0x4E, QB0 = 0x00, QB1 = 0x55, QB2 = 0xAA, QB3 = 0xFF;
__device__ __forceinline__ float xrow_max(float x) {
  auto s = __builtin_amdgcn_permlane16_swap(__float_as_uint(x), __float_as_uint(x), false, false);
  x = fmaxf(__uint_as_float(s[0]), __uint_as_float(s[1]));
  auto t = __builtin_amdgcn_permlane32_swap(__float_as_uint(x), __float_as_uint(x), false, false);
  return fmaxf(__uint_as_float(t[0]), __uint_as_float(t[1]));
}
__device__ __forceinline__ float xrow_sum(float x) {
  auto s = __builtin_amdgcn_permlane16_swap(__float_as_uint(x), __float_as_uint(x), false, false);
  x = __uint_as_float(s[0]) + __uint_as_float(s[1]);
  auto t = __builtin_amdgcn_permlane32_swap(__float_as_uint(x), __float_as_uint(x), false, false);
  return __uint_as_float(t[0]) + __uint_as_float(t[1]);
}
__device__ __forceinline__ float half_sum(float x) {
  auto t = __builtin_amdgcn_permlane32_swap(__float_as_uint(x), __float_as_uint(x), false, false);
  return __uint_as_float(t[0]) + __uint_as_float(t[1]);
}
__device__ __forceinline__ float dot2(unsigned a, unsigned b, float c) { return __builtin_amdgcn_fdot2_f32_bf16(__builtin_bit_cast(bf16x2, a), __builtin_bit_cast(bf16x2, b), c, false); }
__device__ __forceinline__ unsigned tagm(float v, unsigned m, unsigned t) { return (__float_as_uint(v) & ~m) | (t & m); }
__device__ __forceinline__ unsigned perm_lo(unsigned hi, unsigned lo) { return __builtin_amdgcn_perm(hi, lo, 0x05040100u); }
__device__ __forceinline__ unsigned perm_hi(unsigned hi, unsigned lo) { return __builtin_amdgcn_perm(hi, lo, 0x07060302u); }
__device__ __forceinline__ unsigned or4(u32x4 v) { return (v[0] | v[1]) | (v[2] | v[3]); }
__device__ __forceinline__ bool none(bool p) { return __builtin_amdgcn_ballot_w64(p) == 0ull; }
#define F4(v) __builtin_bit_cast(f32x4, v)
#define SB() __builtin_amdgcn_sched_barrier(0)
#define CBAR() asm volatile("" ::: "memory")
#define OPQ(x) asm volatile("" : "+v"(x))
#define LGKM0() asm volatile("s_waitcnt lgkmcnt(0)" ::: "memory")
#define RSRC(p, n) __builtin_amdgcn_make_buffer_rsrc((void*)(p), 0, (n), 0x00020000)
#define LD32(r, v, s, a) __builtin_amdgcn_raw_buffer_load_b32(r, v, s, a)
#define LD64(r, v, s, a) __builtin_amdgcn_raw_buffer_load_b64(r, v, s, a)
#define LD128(r, v, s, a) __builtin_amdgcn_raw_buffer_load_b128(r, v, s, a)
#define LDSV(T, off) (*(volatile LAS T*)(lds + (off)))
#define NOUNROLL _Pragma("clang loop unroll(disable)")

__device__ __forceinline__ unsigned now32() { unsigned long long t; asm volatile("s_memrealtime %0\n\ts_waitcnt lgkmcnt(0)" : "=s"(t)::"memory"); return (unsigned)t; }
__device__ __forceinline__ void pace(unsigned& tgt, int step) {
  asm volatile("1:\n\ts_memrealtime vcc\n\ts_waitcnt lgkmcnt(0)\n\ts_sub_u32 vcc_lo, vcc_lo, %0\n\ts_cmp_lt_i32 vcc_lo, 0\n\ts_cbranch_scc1 1b\n\ts_add_u32 %0, %0, %1"
               : "+s"(tgt) : "n"(step) : "memory", "scc", "vcc");
}
#define STAMP(i)
#define STAMPV(i, val)
#define STAMP_FLUSH()

__device__ __forceinline__ void lds_wait_eq(LAS unsigned char* lds, int off, unsigned n) {
  NOUNROLL for (unsigned spin = 0; LDSV(unsigned, off) != n && spin < (SPIN_CAP << 4); spin++) __builtin_amdgcn_s_sleep(1);
  CBAR();
}
__device__ __forceinline__ void lds_wait8(LAS unsigned char* lds, int off) {
  NOUNROLL for (unsigned spin = 0; spin < (SPIN_CAP << 4); spin++) {
    const u32x2 f = LDSV(u32x2, off);
    if ((f[0] & f[1]) == 0x01010101u) break;
    __builtin_amdgcn_s_sleep(1);
  }
  CBAR();
}

__device__ __forceinline__ void gather_h(LAS unsigned char* lds, __amdgpu_buffer_rsrc_t rws, unsigned T, int hh, int g, int c) {
  const u32x4 TN = {T & 0xfu, (T >> 8) & 0xfu, (T >> 16) & 0xfu, (T >> 24) & 0xfu};
  const int goff = WS_GU + (g * 4 * FF + hh * 56 + (c < 14 ? c : 13) * 4) * 4;
  u32x4 gv[8];
  NOUNROLL for (unsigned spin = 0;; spin++) {
    CBAR();
#pragma unroll
    for (int i = 0; i < 8; i++) gv[i] = LD128(rws, goff, ((i >> 2) * 16 + (i & 3)) * FF * 4, GAUX);
    unsigned bad = 0;
#pragma unroll
    for (int i = 0; i < 8; i++) { gv[i] ^= TN; bad |= or4(gv[i]); }
    if (none((bad & 0xfu) != 0) || spin > SPIN_CAP) break;
    __builtin_amdgcn_s_sleep(4);
  }
  const f32x4 gs = (F4(gv[0]) + F4(gv[1])) + (F4(gv[2]) + F4(gv[3])), us = (F4(gv[4]) + F4(gv[5])) + (F4(gv[6]) + F4(gv[7]));
  u32x2 hp;
#pragma unroll
  for (int i = 0; i < 2; i++) {
    const unsigned gb = cvtpk(gs[2 * i], gs[2 * i + 1]), ub = cvtpk(us[2 * i], us[2 * i + 1]);
    hp[i] = c < 14 ? cvtpk(lo16(gb) * lo16(ub), hi16(gb) * hi16(ub)) : 0u;
  }
  LDSV(u32x2, L_H + g * 128 + c * 8) = hp;
  LDSV(unsigned, F_H) = 1u;
}

__global__ void __launch_bounds__(NTHR, 3) fused(const u16* kc, const u16* vc, const u16* x, const u16* wq, const u16* wg, const u16* wu,
                                                 const u16* wo, const u16* wd, unsigned char* ws, u16* out) {
  __shared__ __attribute__((aligned(16))) unsigned char smem[L_END];
  LAS unsigned char* lds = (LAS unsigned char*)smem;
  const int blk = blockIdx.x, kvh = blk & 7, gi = blk >> 3, b = gi & 3, s = gi >> 2;
  const int tid = threadIdx.x, lane = tid & 63, w = __builtin_amdgcn_readfirstlane(tid >> 6), g = lane >> 4, c = lane & 15;
  STAMP(0);
  const unsigned tstart = (unsigned)__builtin_amdgcn_s_memrealtime();
  asm volatile("" : "+s"(kc), "+s"(vc), "+s"(x), "+s"(wq), "+s"(wg), "+s"(wu), "+s"(wo), "+s"(wd), "+s"(ws), "+s"(out));
  auto rws = RSRC(ws, WS_END);
  const int slot = (kvh * 4 + b) * 8 + s;
  const int kva = (kvh + XO_A) & 7, hl = gi >> 3, hh = kva * 4 + hl, ng = gi & 7;
  if (w < NT) {
    __builtin_amdgcn_s_setprio(3);
    const unsigned ep = (AB & 128) ? 5u : LD32(rws, WS_EPOCH, 0, EAUX);
    if (w == 2) {
      LDSV(unsigned, L_MW + lane * 4) = 0u;
      if (lane < 16) LDSV(unsigned, L_FLAG + lane * 4) = 0u;
      LGKM0();
    }
    if (w != 0) {
      __builtin_amdgcn_s_barrier();
      asm volatile("s_waitcnt vmcnt(0)" ::: "memory");
      LDSV(unsigned char, F_E + w) = 1;
    }
    if (w == 0 || (w == 1 && gi < 28)) {
      const int kvq = (kvh + XO_Q) & 7;
      const int tq = kvh * 28 + gi, mat = tq >= 112 ? 1 : 0, rem = tq - 112 * mat;
      const int kq = (w == 0) ? (gi & 3) : (rem & 3), cb = (w == 0) ? (gi >> 2) : (rem >> 2);
      const int rstride = (w == 0) ? 8192 : FF * 2;
      const u16* wbase = ((w == 0) ? wq + kvq * 512 : (mat ? wu : wg)) + cb * 64 + (size_t)kq * 64 * rstride;
      auto rw = RSRC(wbase, 127 * rstride + 128);
      auto rx = RSRC(x, 4096);
      const int col = cb * 64 + (c & 7) * 8 + (c >> 3) * 4;
      const int poff = (w == 0) ? WS_Q + (((kvq * 4 + g) * 4 + kq) * 512 + col) * 4 : WS_GU + (((mat * 4 + g) * 4 + kq) * FF + col) * 4;
      u32x4 xs[4], xf[2], W[16];
      if (w == 1) {
        if (GUPOS < 0) __builtin_amdgcn_s_sleep(DLY_GU); else lds_wait8(lds, F_G);
      }
#pragma unroll
      for (int i = 0; i < 4; i++) { if (AB & 64) { xs[i] = u32x4{1u, 2u, 3u, (unsigned)lane}; OPQ(xs[i]); } else xs[i] = LD128(rx, (c >> 2) * 1024 + g * 64 + (c & 3) * 16 + i * 256, 0, XAUX); }
#pragma unroll
      for (int i = 0; i < 2; i++) { if (AB & 64) { xf[i] = u32x4{1u, 2u, 3u, (unsigned)lane}; OPQ(xf[i]); } else xf[i] = LD128(rx, (c >> 2) * 1024 + kq * 256 + (c & 1) * 64 + g * 16 + i * 128, 0, XAUX); }
      const int woff = ((c >> 3) * 32 + g * 8) * rstride + (c & 7) * 16;
#pragma unroll
      for (int j = 0; j < 16; j++) {
        if (AB & 512) W[j] = LD128(RSRC((w == 0 ? wq + blk * 8192 : (mat ? wu : wg) + rem * 8192), 16384), lane * 16, j * 1024, W_AUX);
        else W[j] = LD128(rw, woff, ((j >> 3) * 64 + (j & 7)) * rstride, W_AUX);
      }
      SB();
      if (w == 0) __builtin_amdgcn_s_barrier();
      SB();
      STAMP(1);
      float sm = 0.f;
#pragma unroll
      for (int i = 0; i < 4; i++)
#pragma unroll
        for (int j = 0; j < 4; j++) sm = dot2(xs[i][j], 0x3f803f80u, sm);
      sm += dpp<QX1>(sm); sm += dpp<QX2>(sm);
      const float mean = xrow_sum(sm) * (1.f / DM);
      f32x2 ssv = {0.f, 0.f};
#pragma unroll
      for (int i = 0; i < 4; i++)
#pragma unroll
        for (int j = 0; j < 4; j++) { const f32x2 d = f32x2{lo16(xs[i][j]), hi16(xs[i][j])} - mean; ssv += d * d; }
      float ss = ssv[0] + ssv[1];
      ss += dpp<QX1>(ss); ss += dpp<QX2>(ss);
      const float var = rbf(xrow_sum(ss) * (1.f / DM)), mb = rbf(mean);
      const float den = rbf(__builtin_amdgcn_sqrtf(rbf(var + 1e-5f)));
      const float rden = __builtin_amdgcn_rcpf(den);
#pragma unroll
      for (int i = 0; i < 2; i++)
#pragma unroll
        for (int j = 0; j < 4; j++) {
          const unsigned d = cvtpk(lo16(xf[i][j]) - mb, hi16(xf[i][j]) - mb);
          const f32x2 n = {lo16(d), hi16(d)};
          const f32x2 q = n * rden;
          const f32x2 r = (n - q * den) * rden + q;
          xf[i][j] = cvtpk(r[0], r[1]);
        }
      SB();
      STAMP(2);
      f32x4 acc[8];
#pragma unroll
      for (int e = 0; e < 8; e++) acc[e] = f32x4{0.f, 0.f, 0.f, 0.f};
#pragma unroll
      for (int t = 0; t < 2; t++) {
#pragma unroll
        for (int e = 0; e < 8; e++) {
          u32x4 f;
#pragma unroll
          for (int j = 0; j < 4; j++) f[j] = (e & 1) ? perm_hi(W[t * 8 + 2 * j + 1][e >> 1], W[t * 8 + 2 * j][e >> 1]) : perm_lo(W[t * 8 + 2 * j + 1][e >> 1], W[t * 8 + 2 * j][e >> 1]);
          acc[e] = mfma32(xf[t], f, acc[e]);
        }
        SB();
      }
      const unsigned T0 = __builtin_amdgcn_readfirstlane(ep) + 0x01010101u;
      u32x4 o;
#pragma unroll
      for (int i = 0; i < 4; i++) {
        const float a = acc[i][0] + dpp<ROR8>(acc[i][1]), bq = acc[4 + i][1] + dpp<ROR8>(acc[4 + i][0]);
        o[i] = tagm(c < 8 ? a : bq, 0xfu, T0 >> (8 * i));
      }
      STAMP(3);
      if (AB & 4) OPQ(o);
      else if (QAUX == GAUX || w == 0) __builtin_amdgcn_raw_buffer_store_b128(o, rws, poff, 0, QAUX);
      else __builtin_amdgcn_raw_buffer_store_b128(o, rws, poff, 0, GAUX);
      if (w == 0) LDSV(unsigned char, F_E) = 1;
      STAMP(4);
    }
    if (w == 1) LDSV(unsigned, F_GP) = 1u;
    unsigned T;
    if (w == 2) {
      T = __builtin_amdgcn_readfirstlane(ep) + 0x01010101u;
      const u32x4 TN = {T & 0xfu, (T >> 8) & 0xfu, (T >> 16) & 0xfu, (T >> 24) & 0xfu};
      const int goff = WS_Q + (kvh * 4 + b) * 8192 + lane * 16;
      u32x4 q[8];
      lds_wait_eq(lds, F_E, 0x01010101u);
      __builtin_amdgcn_s_sleep(PRE_Q);
      STAMP(3);
      unsigned spin = 0;
      if (AB & 1) { for (int i = 0; i < 8; i++) q[i] = TN; } else
      NOUNROLL for (;; spin++) {
        CBAR();
#pragma unroll
        for (int i = 0; i < 8; i++) q[i] = LD128(rws, goff, i * 1024, QAUX);
        unsigned bad = 0;
#pragma unroll
        for (int i = 0; i < 8; i++) { q[i] ^= TN; bad |= or4(q[i]); }
        if (none((bad & 0xfu) != 0) || spin > SPIN_CAP) break;
        __builtin_amdgcn_s_sleep(4);
      }
      STAMPV(15, spin);
#pragma unroll
      for (int hf = 0; hf < 2; hf++) {
        const f32x4 a = (F4(q[hf]) + F4(q[2 + hf])) + (F4(q[4 + hf]) + F4(q[6 + hf]));
        LDSV(u32x2, L_Q + hf * 512 + lane * 8) = u32x2{cvtpk(a[0], a[1]), cvtpk(a[2], a[3])};
      }
      LDSV(u32x2, F_Q) = u32x2{1u, T};
      STAMP(4);
    }
    if (GUPOS < 0 && w == 3 && !(AB & 2)) {
      lds_wait_eq(lds, F_Q, 1u);
      __builtin_amdgcn_s_sleep(PRE_H2);
      STAMP(3);
      gather_h(lds, rws, __builtin_amdgcn_readfirstlane(ep) + 0x01010101u, hh, g, c);
      STAMP(4);
    }
    if (AB & 8) { if (blk == 0 && w == 2) { lds_wait8(lds, F_PV); __builtin_amdgcn_s_sleep(100); if (lane == 0) __builtin_amdgcn_raw_buffer_store_b32(T, rws, WS_EPOCH, 0, 16); } STAMP_FLUSH(); return; }
    const int bb = c >> 2, q4 = c & 3, sh = g >> 1;
    const int slot0 = (kva * 4 + bb) * 8 + sh * 4;
    int soff = WS_ST + (slot0 + q4) * 128 + hl * 16;
    int aoff = WS_OA + slot0 * OA_STR + hl * 512 + w * 128 + (g & 1) * 64 + q4 * 16;
    int pboff = WS_PB + (hh * 8 + ng) * PB_STR + lane * 16;
    asm volatile("" : "+v"(soff), "+v"(aoff), "+v"(pboff));
    lds_wait_eq(lds, F_Q, 1u);
    T = __builtin_amdgcn_readfirstlane(LDSV(unsigned, F_Q + 4));
    const unsigned ta = T & 0xffu, tb = (T >> 16) & 0xffu;
    unsigned mska = 0xffu;
    OPQ(mska);
    if (w == 0) {
      unsigned lv;
      NOUNROLL for (unsigned spin = 0;; spin++) {
        lv = LDSV(unsigned, L_LW + (lane & 31) * 4);
        if (none(lv == 0u) || spin > (SPIN_CAP << 4)) break;
        __builtin_amdgcn_s_sleep(2);
      }
      float mv = hi16(LDSV(unsigned, L_MW + (lane & 31) * 4));
      float lsum = __uint_as_float(lv);
      lsum += dpp<QX1>(lsum); lsum += dpp<QX2>(lsum); lsum += dpp<HMIR>(lsum);
      mv = fmaxf(mv, dpp<QX1>(mv)); mv = fmaxf(mv, dpp<QX2>(mv)); mv = fmaxf(mv, dpp<HMIR>(mv));
      const u32x4 st = {__float_as_uint(mv), T, __float_as_uint(lsum), T};
      if ((lane & 39) == 0) __builtin_amdgcn_raw_buffer_store_b128(st, rws, WS_ST + slot * 128 + (lane >> 3) * 16, 0, SAS);
    }
    float wt[4];
    {
      u32x4 s0;
      unsigned spin = 0;
      lds_wait8(lds, F_S);
      NOUNROLL for (;; spin++) {
        CBAR();
        s0 = LD128(rws, soff, 0, SAL);
        if (none(s0[1] != T || s0[3] != T) || spin > SPIN_CAP) break;
        __builtin_amdgcn_s_sleep(4);
      }
      STAMPV(14, spin);
      const float m1 = __uint_as_float(s0[0]);
      float MM = fmaxf(m1, dpp<QX1>(m1));
      MM = fmaxf(MM, dpp<QX2>(MM));
      { auto t = __builtin_amdgcn_permlane32_swap(__float_as_uint(MM), __float_as_uint(MM), false, false); MM = fmaxf(__uint_as_float(t[0]), __uint_as_float(t[1])); }
      const float e1 = __builtin_amdgcn_exp2f((m1 - MM) * LOG2E);
      float L = e1 * __uint_as_float(s0[2]);
      L += dpp<QX1>(L); L += dpp<QX2>(L);
      L = half_sum(L);
      const float w1 = e1 * __builtin_amdgcn_rcpf(L);
      wt[0] = dpp<QB0>(w1); wt[1] = dpp<QB1>(w1); wt[2] = dpp<QB2>(w1); wt[3] = dpp<QB3>(w1);
      asm volatile("" : "+v"(wt[0]), "+v"(wt[1]), "+v"(wt[2]), "+v"(wt[3]));
    }
    STAMP(5);
    lds_wait8(lds, WOLATE ? F_PV : F_VI);
    STAMP(6);
    u32x2 WO[8], WD[4];
    {
      auto rwo = RSRC(wo + (size_t)(hh * 128 + w * 32) * DM + ng * 64, 31 * 1024 + 128);
      auto rwd = RSRC(wd + (size_t)(hh * 56 + w * 16) * DM + ng * 64, (w == 3 ? 7 : 15) * 1024 + 128);
      const int rb = (16 * (g & 1) + 2 * (g >> 1)) * 1024 + c * 8;
      unsigned tw = now32() + (w * WTAU) / 40;
#pragma unroll
      for (int j = 0; j < 8; j++) { if (WTAU) pace(tw, WTAU / 10); WO[j] = LD64(rwo, rb + (j & 1) * 1024, (j >> 1) * 4096, WO_AUX); SB(); }
#pragma unroll
      for (int j = 0; j < 4; j++) { if (WTAU) pace(tw, WTAU / 10); WD[j] = LD64(rwd, g * 4096 + c * 8 + j * 1024, 0, WO_AUX); SB(); }
    }
    STAMP(8);
    if (!WOLATE) lds_wait8(lds, F_PV);
    if (PRE_A) __builtin_amdgcn_s_sleep(PRE_A);
    STAMP(9);
    u32x4 a[4];
    f32x4 acc[4];
    {
#pragma unroll
      for (int i = 0; i < 4; i++) a[i] = LD128(rws, aoff, i * OA_STR, OAL);
      SB();
    }
    u32x4 fo[4];
    u32x2 fd[4];
#pragma unroll
    for (int e = 0; e < 4; e++) {
#pragma unroll
      for (int j = 0; j < 4; j++) fo[e][j] = (e & 1) ? perm_hi(WO[2 * j + 1][e >> 1], WO[2 * j][e >> 1]) : perm_lo(WO[2 * j + 1][e >> 1], WO[2 * j][e >> 1]);
#pragma unroll
      for (int j = 0; j < 2; j++) fd[e][j] = (e & 1) ? perm_hi(WD[2 * j + 1][e >> 1], WD[2 * j][e >> 1]) : perm_lo(WD[2 * j + 1][e >> 1], WD[2 * j][e >> 1]);
    }
    asm volatile("" : "+v"(fd[0]), "+v"(fd[1]), "+v"(fd[2]), "+v"(fd[3]), "+v"(fo[0]), "+v"(fo[1]), "+v"(fo[2]), "+v"(fo[3]));
    STAMP(7);
    {
      lds_wait_eq(lds, F_H, 1u);
      const u32x2 hf = LDSV(u32x2, L_H + bb * 128 + w * 32 + g * 8);
#pragma unroll
      for (int e = 0; e < 4; e++) acc[e] = mfma16(hf, fd[e], f32x4{0.f, 0.f, 0.f, 0.f});
      asm volatile("" : "+v"(acc[0]), "+v"(acc[1]), "+v"(acc[2]), "+v"(acc[3]));
      unsigned spin = 0;
      NOUNROLL for (;; spin++) {
        unsigned bad = 0;
#pragma unroll
        for (int i = 0; i < 4; i++) { a[i][0] ^= ta; a[i][2] ^= tb; bad |= a[i][0] | a[i][2]; }
        if (none((bad & 0xffu) != 0)) break;
        if (spin > SPIN_CAP) break;
        __builtin_amdgcn_s_sleep(1);
        CBAR();
#pragma unroll
        for (int i = 0; i < 4; i++) a[i] = LD128(rws, aoff, i * OA_STR, OAL);
      }
      STAMPV(13, spin);
    }
    STAMP(10);
    f32x4 t = F4(a[0]) * wt[0];
#pragma unroll
    for (int i = 1; i < 4; i++) t = __builtin_elementwise_fma(F4(a[i]), f32x4{wt[i], wt[i], wt[i], wt[i]}, t);
    auto p0 = __builtin_amdgcn_permlane32_swap(__float_as_uint(t[0]), __float_as_uint(t[2]), false, false);
    auto p1 = __builtin_amdgcn_permlane32_swap(__float_as_uint(t[1]), __float_as_uint(t[3]), false, false);
    const unsigned own = cvtpk(__uint_as_float(p0[0]) + __uint_as_float(p0[1]), __uint_as_float(p1[0]) + __uint_as_float(p1[1]));
    const u32x4 A = {own, dppu<QB1>(own), dppu<QB2>(own), dppu<QB3>(own)};
#pragma unroll
    for (int e = 0; e < 4; e++) acc[e] = mfma32(A, fo[e], acc[e]);
    LDSV(f32x4, L_TR + w * 1024 + lane * 16) = f32x4{acc[0][0], acc[1][0], acc[2][0], acc[3][0]};
    unsigned prev;
    f32x4 r0, r1, r2, r3;
    {
      const unsigned la = (unsigned)(uintptr_t)(lds + L_TR) + lane * 16, lf = (unsigned)(uintptr_t)(lds + F_T), one = 1u;
      asm volatile("s_mov_b64 exec, 1\n\tds_add_rtn_u32 %0, %6, %7\n\ts_mov_b64 exec, -1\n\t"
                   "ds_read_b128 %1, %5\n\tds_read_b128 %2, %5 offset:1024\n\tds_read_b128 %3, %5 offset:2048\n\tds_read_b128 %4, %5 offset:3072\n\t"
                   "s_waitcnt lgkmcnt(0)"
                   : "=&v"(prev), "=&v"(r0), "=&v"(r1), "=&v"(r2), "=&v"(r3) : "v"(la), "v"(lf), "v"(one) : "memory");
    }
    if (__builtin_amdgcn_readfirstlane(prev) == 3u) {
      const f32x4 z = (r0 + r1) + (r2 + r3);
      const u32x4 o = {tagm(z[0], mska, ta), __float_as_uint(z[1]), tagm(z[2], mska, tb), __float_as_uint(z[3])};
      __builtin_amdgcn_raw_buffer_store_b128(o, rws, pboff, 0, PAS);
    }
    STAMP(11);
    STAMP_FLUSH();
    return;
  }
  const int kv = w - NT;
  const size_t base = ((size_t)(b * 8 + kvh) * SK + s * 512) * DH;
  auto rk = RSRC(kc + base, 131072);
  auto rv = RSRC(vc + base, 131072);
  u32x4 K[16], V[16];
  const int koff = (c >> 2) * 256 + g * 64 + (c & 3) * 16;
  const int voff = lane * 16;
#define IOFF(i) 0
#define SOFF(i) ((((i) & 3) * 8 + kv) * 4096 + ((((i) >> 2) + gi) & 3) * 1024)
  unsigned tgt = tstart + TOFF + TAU / 10 + (kv * TAU) / 80;
#define PACE(i) do { if (TAU && (i) >= N0 && (i) < 32 - NFREE) pace(tgt, (((i) - N0 + 2) * TAU) / 10 - (((i) - N0 + 1) * TAU) / 10); } while (0)
#define WAITN(n) asm volatile("s_waitcnt vmcnt(%0)" ::"n"((n) - 1) : "memory")
#define VLOAD(i) do { PACE(16 + (i)); V[i] = LD128(rv, voff + IOFF(i), SOFF(i), KV_AUX); if ((i) + 1 == GUPOS) LDSV(unsigned char, F_G + kv) = 1; if ((i) + 1 == STPOS) LDSV(unsigned char, F_S + kv) = 1; if ((i) + 1 == WOPOS) LDSV(unsigned char, F_VI + kv) = 1; SB(); } while (0)
#pragma unroll
  for (int i = 0; i < 16; i++) {
    if (i == FIRSTN) __builtin_amdgcn_s_barrier();
    if (i >= WIN) WAITN(WIN);
    PACE(i);
    K[i] = LD128(rk, koff + IOFF(i), SOFF(i), KV_AUX);
    SB();
  }
  STAMP(1);
#pragma unroll
  for (int i = 0; i < QKPOS; i++) { if (i < QKPOS - BOOST) WAITN(WIN); VLOAD(i); }
  if (AB & 256) {
#pragma unroll
    for (int i = QKPOS; i < 16; i++) { WAITN(WIN); VLOAD(i); }
    u32x4 a = K[0];
#pragma unroll
    for (int i = 1; i < 16; i++) a ^= K[i];
#pragma unroll
    for (int i = 0; i < 16; i++) a ^= V[i];
    OPQ(a);
    LDSV(unsigned char, F_PV + kv) = 1;
    lds_wait8(lds, F_PV);
    STAMP(10);
    STAMP_FLUSH();
    return;
  }
  STAMP(2);
  u32x2 fl = LDSV(u32x2, F_Q);
  NOUNROLL for (unsigned spin = 0; fl[0] == 0u && spin < (SPIN_CAP << 4); spin++) { __builtin_amdgcn_s_sleep(1); fl = LDSV(u32x2, F_Q); }
  CBAR();
  const unsigned T = __builtin_amdgcn_readfirstlane(fl[1]);
  const unsigned ta = T & 0xffu, tb = (T >> 16) & 0xffu;
  STAMP(3);
  float S[4];
  {
    const u32x4 qf = LDSV(u32x4, L_Q + (c >> 2) * 256 + g * 64 + (c & 3) * 16);
    const bool a1 = c & 1, a2 = c & 2;
#pragma unroll
    for (int t = 0; t < 4; t++) {
      float y[4];
#pragma unroll
      for (int i = 0; i < 4; i++) {
        const f32x4 d = mfma32(K[t * 4 + i], qf, f32x4{0.f, 0.f, 0.f, 0.f});
        float z = dpp<QB0>(d[0]);
        z += dpp<QB1>(d[1]); OPQ(z);
        z += dpp<QB2>(d[2]); OPQ(z);
        z += dpp<QB3>(d[3]); OPQ(z);
        y[i] = z;
      }
      const float u0 = a1 ? y[1] : y[0], u1 = a1 ? y[3] : y[2];
      S[t] = a2 ? u1 : u0;
    }
  }
  {
#pragma unroll
    for (int i = 0; i < 4; i += 2) {
      const unsigned p = cvtpk(S[i], S[i + 1]);
      const unsigned p2 = cvtpk(lo16(p) * 0.08838834764831845f, hi16(p) * 0.08838834764831845f);
      S[i] = lo16(p2); S[i + 1] = hi16(p2);
    }
    float mw = fmaxf(fmaxf(S[0], S[1]), fmaxf(S[2], S[3]));
    mw = fmaxf(mw, dpp<QX1>(mw)); mw = fmaxf(mw, dpp<QX2>(mw));
    mw = xrow_max(mw);
    LDSV(unsigned, L_MW + (c >> 2) * 32 + kv * 4) = __float_as_uint(mw) | 1u;
  }
  STAMP(4);
  SB();
#pragma unroll
  for (int i = QKPOS; i < SMPOS; i++) { WAITN(WIN); VLOAD(i); }
  STAMP(5);
  u32x4 P[2];
  {
    u32x4 m0, m1;
    NOUNROLL for (unsigned spin = 0;; spin++) {
      m0 = LDSV(u32x4, L_MW + (c >> 2) * 32); m1 = LDSV(u32x4, L_MW + (c >> 2) * 32 + 16);
      const u32x4 mm = m0 & m1;
      if (none((mm[0] & mm[1] & mm[2] & mm[3] & 1u) == 0u) || spin > (SPIN_CAP << 4)) break;
      __builtin_amdgcn_s_sleep(1);
    }
    float M = -INFINITY;
#pragma unroll
    for (int i = 0; i < 4; i++) M = fmaxf(M, fmaxf(hi16(m0[i]), hi16(m1[i])));
    const float mneg = -M * LOG2E;
    float p[4];
#pragma unroll
    for (int i = 0; i < 4; i++) p[i] = __builtin_amdgcn_exp2f(fmaf(S[i], LOG2E, mneg));
    float lsum = (p[0] + p[1]) + (p[2] + p[3]);
    lsum += dpp<QX1>(lsum); lsum += dpp<QX2>(lsum);
    lsum = xrow_sum(lsum);
    LDSV(unsigned, L_LW + (c >> 2) * 32 + kv * 4) = __float_as_uint(lsum) | 1u;
#pragma unroll
    for (int u = 0; u < 2; u++) { const unsigned pk = cvtpk(p[2 * u], p[2 * u + 1]); P[u] = u32x4{pk, dppu<QB1>(pk), dppu<QB2>(pk), dppu<QB3>(pk)}; }
  }
  int oaoff = WS_OA + slot * OA_STR + kv * 512 + (lane & 31) * 16;
  unsigned mska = 0xffu;
  asm volatile("" : "+v"(oaoff), "+v"(mska));
  STAMP(6);
  SB();
#pragma unroll
  for (int i = SMPOS; i < PVPOS; i++) { WAITN(WIN); VLOAD(i); }
  f32x4 O[8];
  {
#pragma unroll
    for (int i = 0; i < 4; i++) {
      u32x4 f0, f1;
#pragma unroll
      for (int j = 0; j < 4; j++) { f0[j] = perm_lo(V[4 + j][i], V[j][i]); f1[j] = perm_hi(V[4 + j][i], V[j][i]); }
      O[2 * i] = mfma32(P[0], f0, f32x4{0.f, 0.f, 0.f, 0.f}); O[2 * i + 1] = mfma32(P[0], f1, f32x4{0.f, 0.f, 0.f, 0.f});
    }
    asm volatile("" : "+v"(O[0]), "+v"(O[1]), "+v"(O[2]), "+v"(O[3]), "+v"(O[4]), "+v"(O[5]), "+v"(O[6]), "+v"(O[7]));
    SB();
#pragma unroll
    for (int i = PVPOS; i < 16; i++) { WAITN(WIN); VLOAD(i); }
    STAMP(11);
    u32x4 F[8];
#pragma unroll
    for (int j = 0; j < 3; j++) {
#pragma unroll
      for (int i = 0; i < 4; i++) { F[2 * i][j] = perm_lo(V[12 + j][i], V[8 + j][i]); F[2 * i + 1][j] = perm_hi(V[12 + j][i], V[8 + j][i]); }
      SB();
    }
    STAMP(7);
#pragma unroll
    for (int i = 0; i < 4; i++) {
      F[2 * i][3] = perm_lo(V[15][i], V[11][i]); F[2 * i + 1][3] = perm_hi(V[15][i], V[11][i]);
      O[2 * i] = mfma32(P[1], F[2 * i], O[2 * i]); O[2 * i + 1] = mfma32(P[1], F[2 * i + 1], O[2 * i + 1]);
    }
  }
  LDSV(f32x4, L_RED + kv * 2048 + lane * 32) = f32x4{O[0][0], O[1][0], O[2][0], O[3][0]};
  LDSV(f32x4, L_RED + kv * 2048 + lane * 32 + 16) = f32x4{O[4][0], O[5][0], O[6][0], O[7][0]};
  LDSV(unsigned char, F_PV + kv) = 1;
  STAMP(8);
  if (kv < 4) {
    const int half = lane >> 5, l5 = lane & 31;
    f32x4 r[4];
    NOUNROLL for (unsigned spin = 0;; spin++) {
      const u32x2 cnt = LDSV(u32x2, F_PV);
#pragma unroll
      for (int i = 0; i < 4; i++) r[i] = LDSV(f32x4, L_RED + (half * 4 + i) * 2048 + kv * 512 + l5 * 16);
      if ((cnt[0] & cnt[1]) == 0x01010101u) break;
      if (spin > (SPIN_CAP << 4)) break;
    }
    STAMP(9);
    const f32x4 a = (r[0] + r[1]) + (r[2] + r[3]);
    const u32x4 o = {tagm(half_sum(a[0]), mska, ta), __float_as_uint(half_sum(a[1])), tagm(half_sum(a[2]), mska, tb), __float_as_uint(half_sum(a[3]))};
    if (half == 0 && !(AB & 16)) __builtin_amdgcn_raw_buffer_store_b128(o, rws, oaoff, 0, OAS);
    if (AB & 16) { u32x4 o2 = o; OPQ(o2); }
    STAMP(10);
  } else if (kv == 4 && !(AB & 8)) {
    const int oi = (gi >> 3) * 512 + kvh * 64 + (gi & 7) * 8 + (c & 1) * 4 + g;
    auto rx = RSRC(x, 4096);
    auto ro = RSRC(out, 4096);
    float xfin = lo16(__builtin_amdgcn_raw_buffer_load_b16(rx, oi * 2, 0, 0));
    int goff = WS_PB + ((lane >> 1) * 8 + kvh) * PB_STR + gi * 32 + (lane & 1) * 16;
    int ooff = c < 2 ? oi * 2 : 0x7ffffff0;
    asm volatile("" : "+v"(xfin), "+v"(goff), "+v"(ooff));
    lds_wait_eq(lds, F_T, 4u);
    if (PRE_B) __builtin_amdgcn_s_sleep(PRE_B);
    STAMP(10);
    u32x4 pb;
    unsigned spin = 0;
    NOUNROLL for (;; spin++) {
      CBAR();
      pb = LD128(rws, goff, 0, PAL);
      pb[0] ^= ta; pb[2] ^= tb;
      if (none(((pb[0] | pb[2]) & 0xffu) != 0)) break;
      if (spin > SPIN_CAP) break;
      __builtin_amdgcn_s_sleep(1);
    }
    STAMPV(15, spin);
    STAMP(11);
    auto s0 = __builtin_amdgcn_permlane32_swap(pb[0], pb[2], false, false);
    auto s1 = __builtin_amdgcn_permlane32_swap(pb[1], pb[3], false, false);
    const float y0 = __uint_as_float(s0[0]) + __uint_as_float(s0[1]), y1 = __uint_as_float(s1[0]) + __uint_as_float(s1[1]);
    auto s2 = __builtin_amdgcn_permlane16_swap(__float_as_uint(y0), __float_as_uint(y1), false, false);
    float z = __uint_as_float(s2[0]) + __uint_as_float(s2[1]);
    z += dpp<ROR8>(z); z += dpp<ROR4>(z); z += dpp<QX2>(z);
    z += xfin;
    __builtin_amdgcn_raw_buffer_store_b16((u16)cvtpk(z, z), ro, ooff, 0, 16);
    if (blk == 0 && lane == 0) __builtin_amdgcn_raw_buffer_store_b32(T, rws, WS_EPOCH, 0, 16);
    STAMP(12);
  } else if (kv == 5 && GUPOS >= 0) {
    lds_wait_eq(lds, F_GP, 1u);
    if (PRE_H) __builtin_amdgcn_s_sleep(PRE_H);
    STAMP(9);
    gather_h(lds, rws, T, hh, g, c);
    STAMP(11);
  }
  STAMP_FLUSH();
}

extern "C" void kernel_launch(void* const* d_in, const int* in_sizes, int n_in, void* d_out, int out_size, void* d_ws, size_t ws_size, hipStream_t stream) {
  fused<<<256, NTHR, 0, stream>>>((const u16*)d_in[1], (const u16*)d_in[2], (const u16*)d_in[0], (const u16*)d_in[3], (const u16*)d_in[6],
                                  (const u16*)d_in[7], (const u16*)d_in[4], (const u16*)d_in[8], (unsigned char*)d_ws, (u16*)d_out);
}
```

```cpp
#include <hip/hip_runtime.h>
#include <stdint.h>

#define WIN 8
#define N0 2
#define TOFF 30
#define TAU1 400
#define I1 8
#define TAU 280
#define QKPOS 3
#define SMPOS 5
#define PVPOS 12
#define STPOS 10
#define WOPOS 16
#define GTAU 20
#define WDEL 0
#define NEARLY 0
#define ETAU 20
#define W0 2
#define DEL 30
#define GOFF 0
#define WTAU 5
#define TQ 400
#define TH 600
#define TA_ADD 140
#define PRE_A 4
#define PRE_B 8
#define CODE_BYTES 10752
#define QAUX 18
#define SAS 16
#define SAL 16
#define XO_Q 1
#define XO_A 4
#define AB 0

typedef unsigned short u16;
typedef int i32x4 __attribute__((ext_vector_type(4)));
typedef int i32x16 __attribute__((ext_vector_type(16)));
typedef unsigned u32x2 __attribute__((ext_vector_type(2)));
typedef unsigned u32x4 __attribute__((ext_vector_type(4)));
typedef float f32x2 __attribute__((ext_vector_type(2)));
typedef float f32x4 __attribute__((ext_vector_type(4)));
typedef short s16x4 __attribute__((ext_vector_type(4)));
typedef short s16x8 __attribute__((ext_vector_type(8)));
typedef __bf16 bf16x2 __attribute__((ext_vector_type(2)));
#define LAS __attribute__((address_space(3)))

__device__ u16 LD16(i32x4 r, int v, int s, int a) __asm("llvm.amdgcn.raw.buffer.load.i16");
__device__ unsigned LD32(i32x4 r, int v, int s, int a) __asm("llvm.amdgcn.raw.buffer.load.i32");
__device__ u32x2 LD64(i32x4 r, int v, int s, int a) __asm("llvm.amdgcn.raw.buffer.load.v2i32");
__device__ u32x4 LD128(i32x4 r, int v, int s, int a) __asm("llvm.amdgcn.raw.buffer.load.v4i32");
__device__ void ST16(u16 d, i32x4 r, int v, int s, int a) __asm("llvm.amdgcn.raw.buffer.store.i16");
__device__ void ST32(unsigned d, i32x4 r, int v, int s, int a) __asm("llvm.amdgcn.raw.buffer.store.i32");
__device__ void ST128(u32x4 d, i32x4 r, int v, int s, int a) __asm("llvm.amdgcn.raw.buffer.store.v4i32");
constexpr int NTL = 2, SC1 = 16;

constexpr int DM = 512, FF = 1792;
constexpr int NT = 4, NTHR = (NT + 8) * 64;
constexpr int OA_STR = 2176, PB_STR = 1152;
constexpr int WS_EPOCH = 0;
constexpr int WS_Q = 4096;
constexpr int WS_GU = WS_Q + 131072;
constexpr int WS_ST = WS_GU + 114688;
constexpr int WS_OA = WS_ST + 32768;
constexpr int WS_PB = WS_OA + 256 * OA_STR;
constexpr int WS_END = WS_PB + 256 * PB_STR;
constexpr int WS_TS = 4 << 20;
constexpr int L_RED = 0;
constexpr int L_TR = L_RED + 16384;
constexpr int L_PR = L_TR + 4096;
constexpr int L_Q = L_PR + 4096;
constexpr int L_H = L_Q + 1024;
constexpr int L_WT = L_H + 512;
constexpr int L_MW = L_WT + 128;
constexpr int L_LW = L_MW + 128;
constexpr int L_FLAG = L_LW + 128;
constexpr int F_Q = L_FLAG, F_HW = L_FLAG + 8, F_E = L_FLAG + 12, F_VI = L_FLAG + 16, F_PV = L_FLAG + 24, F_T = L_FLAG + 32, F_P = L_FLAG + 36, F_S = L_FLAG + 40, F_TV = L_FLAG + 48;
constexpr int L_END = L_FLAG + 64;
constexpr unsigned SPIN_CAP = 1u << 18;
constexpr int OOB = 0x7ffffff0;
constexpr float LOG2E = 1.4426950408889634f;
constexpr int sched(int i) { return TOFF + ((i < I1 ? i - N0 + 1 : I1 - N0) * TAU1 + (i < I1 ? 0 : i - I1 + 1) * TAU) / 10; }

__device__ __forceinline__ float lo16(unsigned u) { return __uint_as_float(u << 16); }
__device__ __forceinline__ float hi16(unsigned u) { return __uint_as_float(u & 0xffff0000u); }
__device__ __forceinline__ unsigned cvtpk(float lo, float hi) { f32x2 f = {lo, hi}; return __builtin_bit_cast(unsigned, __builtin_convertvector(f, bf16x2)); }
__device__ __forceinline__ float rbf(float f) { return lo16(cvtpk(f, f)); }
__device__ __forceinline__ f32x4 mfma32(u32x4 a, u32x4 b, f32x4 c) { return __builtin_amdgcn_mfma_f32_16x16x32_bf16(__builtin_bit_cast(s16x8, a), __builtin_bit_cast(s16x8, b), c, 0, 0, 0); }
__device__ __forceinline__ f32x4 mfma16(u32x2 a, u32x2 b, f32x4 c) { return __builtin_amdgcn_mfma_f32_16x16x16bf16_1k(__builtin_bit_cast(s16x4, a), __builtin_bit_cast(s16x4, b), c, 0, 0, 0); }
template <int CTRL> __device__ __forceinline__ float dpp(float x) { return __builtin_bit_cast(float, __builtin_amdgcn_mov_dpp(__builtin_bit_cast(int, x), CTRL, 0xf, 0xf, true)); }
template <int CTRL> __device__ __forceinline__ unsigned dppu(unsigned x) { return (unsigned)__builtin_amdgcn_mov_dpp((int)x, CTRL, 0xf, 0xf, true); }
constexpr int ROR4 = 0x124, ROR8 = 0x128, HMIR = 0x141, QX1 = 0xB1, QX2 = 0x4E, QB0 = 0x00, QB1 = 0x55, QB2 = 0xAA, QB3 = 0xFF;
__device__ __forceinline__ float xrow_max(float x) {
  auto s = __builtin_amdgcn_permlane16_swap(__float_as_uint(x), __float_as_uint(x), false, false);
  x = fmaxf(__uint_as_float(s[0]), __uint_as_float(s[1]));
  auto t = __builtin_amdgcn_permlane32_swap(__float_as_uint(x), __float_as_uint(x), false, false);
  return fmaxf(__uint_as_float(t[0]), __uint_as_float(t[1]));
}
__device__ __forceinline__ float xrow_sum(float x) {
  auto s = __builtin_amdgcn_permlane16_swap(__float_as_uint(x), __float_as_uint(x), false, false);
  x = __uint_as_float(s[0]) + __uint_as_float(s[1]);
  auto t = __builtin_amdgcn_permlane32_swap(__float_as_uint(x), __float_as_uint(x), false, false);
  return __uint_as_float(t[0]) + __uint_as_float(t[1]);
}
__device__ __forceinline__ float half_sum(float x) {
  auto t = __builtin_amdgcn_permlane32_swap(__float_as_uint(x), __float_as_uint(x), false, false);
  return __uint_as_float(t[0]) + __uint_as_float(t[1]);
}
__device__ __forceinline__ float dot2(unsigned a, unsigned b, float c) { return __builtin_amdgcn_fdot2_f32_bf16(__builtin_bit_cast(bf16x2, a), __builtin_bit_cast(bf16x2, b), c, false); }
__device__ __forceinline__ unsigned tagm(float v, unsigned m, unsigned t) { return (__float_as_uint(v) & ~m) | (t & m); }
__device__ __forceinline__ unsigned perm_lo(unsigned hi, unsigned lo) { return __builtin_amdgcn_perm(hi, lo, 0x05040100u); }
__device__ __forceinline__ unsigned perm_hi(unsigned hi, unsigned lo) { return __builtin_amdgcn_perm(hi, lo, 0x07060302u); }
__device__ __forceinline__ bool none(bool p) { return __builtin_amdgcn_ballot_w64(p) == 0ull; }
#define F4(v) __builtin_bit_cast(f32x4, v)
#define SB() __builtin_amdgcn_sched_barrier(0)
#define CBAR() asm volatile("" ::: "memory")
#define OPQ(x) asm volatile("" : "+v"(x))
#define LDSV(T, off) (*(volatile LAS T*)(lds + (off)))
#define NOUNROLL _Pragma("clang loop unroll(disable)")

__device__ __forceinline__ unsigned now32() { unsigned long long t; asm volatile("s_memrealtime %0\n\ts_waitcnt lgkmcnt(0)" : "=s"(t)::"memory"); return (unsigned)t; }
__device__ __forceinline__ void pace(unsigned& tgt, int step) {
  asm volatile("1:\n\ts_memrealtime vcc\n\ts_waitcnt lgkmcnt(0)\n\ts_sub_u32 vcc_lo, vcc_lo, %0\n\ts_cmp_lt_i32 vcc_lo, 0\n\ts_cbranch_scc1 1b\n\ts_add_u32 %0, %0, %1"
               : "+s"(tgt) : "n"(step) : "memory", "scc", "vcc");
}
__device__ __forceinline__ void apace(unsigned& tgt, int step) {
  unsigned tmp;
  asm volatile("1:\n\ts_memrealtime vcc\n\ts_getreg_b32 %1, hwreg(HW_REG_IB_STS, 0, 4)\n\ts_sub_i32 %1, %1, %3\n\ts_max_i32 %1, %1, 0\n\ts_mul_i32 %1, %1, %4\n\t"
               "s_waitcnt lgkmcnt(0)\n\ts_sub_u32 vcc_lo, vcc_lo, %0\n\ts_cmp_lt_i32 vcc_lo, %1\n\ts_cbranch_scc1 1b\n\ts_add_u32 %0, %0, %2"
               : "+s"(tgt), "=&s"(tmp) : "n"(step), "n"(W0 - 1), "n"(DEL) : "memory", "scc", "vcc");
}
__device__ __forceinline__ void sleep_until(unsigned tgt) {
  asm volatile("1:\n\ts_sleep 1\n\ts_memrealtime vcc\n\ts_waitcnt lgkmcnt(0)\n\ts_sub_u32 vcc_lo, vcc_lo, %0\n\ts_cmp_lt_i32 vcc_lo, 0\n\ts_cbranch_scc1 1b"
               :: "s"(tgt) : "memory", "scc", "vcc");
}
#define STAMP(i)
#define STAMPV(i, val)
#define STAMP_FLUSH()

__device__ __forceinline__ void lds_wait_eq(LAS unsigned char* lds, int off, unsigned n) {
  NOUNROLL for (unsigned spin = 0; LDSV(unsigned, off) != n && spin < (SPIN_CAP << 4); spin++) __builtin_amdgcn_s_sleep(1);
  CBAR();
}
__device__ __forceinline__ void lds_wait8(LAS unsigned char* lds, int off) {
  NOUNROLL for (unsigned spin = 0; spin < (SPIN_CAP << 4); spin++) {
    const u32x2 f = LDSV(u32x2, off);
    if ((f[0] & f[1]) == 0x01010101u) break;
    __builtin_amdgcn_s_sleep(1);
  }
  CBAR();
}
__device__ __forceinline__ bool meet4(LAS unsigned char* lds, int area, int cnt, int w, int lane, f32x4 v, f32x4& sum) {
  LDSV(f32x4, area + w * 1024 + lane * 16) = v;
  unsigned prev;
  f32x4 r0, r1, r2, r3;
  const unsigned la = (unsigned)(uintptr_t)(lds + area) + lane * 16, lf = (unsigned)(uintptr_t)(lds + cnt), one = 1u;
  asm volatile("s_mov_b64 exec, 1\n\tds_add_rtn_u32 %0, %6, %7\n\ts_mov_b64 exec, -1\n\t"
               "ds_read_b128 %1, %5\n\tds_read_b128 %2, %5 offset:1024\n\tds_read_b128 %3, %5 offset:2048\n\tds_read_b128 %4, %5 offset:3072\n\t"
               "s_waitcnt lgkmcnt(0)"
               : "=&v"(prev), "=&v"(r0), "=&v"(r1), "=&v"(r2), "=&v"(r3) : "v"(la), "v"(lf), "v"(one) : "memory");
  sum = (r0 + r1) + (r2 + r3);
  return __builtin_amdgcn_readfirstlane(prev) == 3u;
}

#define DESC(n) const void* p_##n, unsigned n##_bytes, unsigned n##_flags
__global__ void __launch_bounds__(NTHR, 3) fused(DESC(k), DESC(v), DESC(ws), DESC(x), DESC(wq), DESC(wg), DESC(wu), DESC(wo), DESC(wd), DESC(out)) {
  __shared__ __attribute__((aligned(16))) unsigned char smem[L_END];
  auto ka = (const __attribute__((address_space(4))) i32x4*)__builtin_amdgcn_kernarg_segment_ptr();
  i32x16 KA, KB;
  unsigned long long pc0;
  asm volatile("s_load_dwordx16 %0, %3, 0x0\n\ts_load_dwordx16 %1, %3, 0x40\n\ts_getpc_b64 %2" : "=&s"(KA), "=&s"(KB), "=&s"(pc0) : "s"(ka));
  SB();
  LAS unsigned char* lds = (LAS unsigned char*)smem;
  const int blk = blockIdx.x, kvh = blk & 7, gi = blk >> 3, b = gi & 3, s = gi >> 2;
  const int tid = threadIdx.x, lane = tid & 63, w = __builtin_amdgcn_readfirstlane(tid >> 6), g = lane >> 4, c = lane & 15;
  u32x2 pfd = {0u, 0u};
  if (w == 3 && lane < CODE_BYTES / 256) {
    const int off = gi * (CODE_BYTES / 32) + lane * 8;
    asm volatile("global_load_dwordx2 %0, %1, %2" : "=v"(pfd) : "v"(off), "s"(pc0 & ~0xffull));
  }
  if (w == 2) {
    LDSV(unsigned, L_MW + lane * 4) = 0u;
    LDSV(unsigned, L_FLAG + c * 4) = 0u;
  }
  const int kv = w - NT;
  int koff = (c >> 2) * 256 + g * 64 + (c & 3) * 16;
  int kvbase = ((b * 8 + kvh) * 4096 + s * 512) * 256 + kv * 4096;
  int so0 = kvbase + (gi & 3) * 1024;
  OPQ(koff);
  asm volatile("s_waitcnt lgkmcnt(0)\n\ts_barrier" : "+s"(KA), "+s"(KB), "+s"(kvbase), "+s"(so0)::"memory");
  const i32x4 rws = {KA[8], KA[9], KA[10], KA[11]};
  const int slot = (kvh * 4 + b) * 8 + s;
  if (__builtin_expect(w >= NT, 1)) {
    const i32x4 rk = {KA[0], KA[1], KA[2], KA[3]}, rv = {KA[4], KA[5], KA[6], KA[7]};
    u32x4 K[16], V[16];
    const int voff = lane * 16;
#define SOFF(i) (((i) < 4 ? so0 : kvbase + ((((i) >> 2) + gi) & 3) * 1024) + ((i) & 3) * 32768)
#define WAITN(n) asm volatile("s_waitcnt vmcnt(%0)" ::"n"((n) - 1) : "memory")
#define PACE(i) do { if (DEL) apace(tgt, sched((i) + 1) - sched(i)); else pace(tgt, sched((i) + 1) - sched(i)); } while (0)
#define VLOAD(i) do { WAITN(WIN); PACE(16 + (i)); V[i] = LD128(rv, voff, SOFF(i), NTL); \
    if ((i) + 1 == STPOS) LDSV(unsigned char, F_S + kv) = 1; if ((i) + 1 == WOPOS) { LDSV(unsigned, F_TV) = now32(); LDSV(unsigned char, F_VI + kv) = 1; } SB(); } while (0)
#pragma unroll
    for (int i = 0; i < N0; i++) { K[i] = LD128(rk, koff, SOFF(i), NTL); SB(); }
    unsigned tgt = now32() + sched(N0) + (kv * TAU) / 80;
#pragma unroll
    for (int i = N0; i < 16; i++) {
      if (i >= WIN) WAITN(WIN);
      PACE(i);
      K[i] = LD128(rk, koff, SOFF(i), NTL);
      SB();
    }
    STAMP(1);
#pragma unroll
    for (int i = 0; i < QKPOS; i++) VLOAD(i);
    STAMP(2);
    u32x2 fl = LDSV(u32x2, F_Q);
    NOUNROLL for (unsigned spin = 0; fl[0] == 0u && spin < (SPIN_CAP << 4); spin++) { __builtin_amdgcn_s_sleep(1); fl = LDSV(u32x2, F_Q); }
    CBAR();
    const unsigned T = __builtin_amdgcn_readfirstlane(fl[1]);
    const unsigned ta = T & 0xffu, tb = (T >> 16) & 0xffu;
    STAMP(3);
    float S[4];
    {
      const u32x4 qf = LDSV(u32x4, L_Q + koff);
      const bool a1 = c & 1, a2 = c & 2;
#pragma unroll
      for (int t = 0; t < 4; t++) {
        float y[4];
#pragma unroll
        for (int i = 0; i < 4; i++) {
          const f32x4 d = mfma32(K[t * 4 + i], qf, f32x4{0.f, 0.f, 0.f, 0.f});
          float z = dpp<QB0>(d[0]);
          z += dpp<QB1>(d[1]); OPQ(z);
          z += dpp<QB2>(d[2]); OPQ(z);
          z += dpp<QB3>(d[3]); OPQ(z);
          y[i] = z;
        }
        const float u0 = a1 ? y[1] : y[0], u1 = a1 ? y[3] : y[2];
        S[t] = a2 ? u1 : u0;
      }
#pragma unroll
      for (int i = 0; i < 4; i += 2) {
        const unsigned p = cvtpk(S[i], S[i + 1]);
        const unsigned p2 = cvtpk(lo16(p) * 0.08838834764831845f, hi16(p) * 0.08838834764831845f);
        S[i] = lo16(p2); S[i + 1] = hi16(p2);
      }
      float mw = fmaxf(fmaxf(S[0], S[1]), fmaxf(S[2], S[3]));
      mw = fmaxf(mw, dpp<QX1>(mw)); mw = fmaxf(mw, dpp<QX2>(mw));
      mw = xrow_max(mw);
      LDSV(unsigned, L_MW + (c >> 2) * 32 + kv * 4) = __float_as_uint(mw) | 1u;
    }
    STAMP(4);
    SB();
#pragma unroll
    for (int i = QKPOS; i < SMPOS; i++) VLOAD(i);
    STAMP(5);
    u32x4 P[2];
    {
      u32x4 m0, m1;
      NOUNROLL for (unsigned spin = 0;; spin++) {
        m0 = LDSV(u32x4, L_MW + (c >> 2) * 32); m1 = LDSV(u32x4, L_MW + (c >> 2) * 32 + 16);
        const u32x4 mm = m0 & m1;
        if (none((mm[0] & mm[1] & mm[2] & mm[3] & 1u) == 0u) || spin > (SPIN_CAP << 4)) break;
        __builtin_amdgcn_s_sleep(1);
      }
      float M = -INFINITY;
#pragma unroll
      for (int i = 0; i < 4; i++) M = fmaxf(M, fmaxf(hi16(m0[i]), hi16(m1[i])));
      const float mneg = -M * LOG2E;
      float p[4];
#pragma unroll
      for (int i = 0; i < 4; i++) p[i] = __builtin_amdgcn_exp2f(fmaf(S[i], LOG2E, mneg));
      float lsum = (p[0] + p[1]) + (p[2] + p[3]);
      lsum += dpp<QX1>(lsum); lsum += dpp<QX2>(lsum);
      lsum = xrow_sum(lsum);
      LDSV(unsigned, L_LW + (c >> 2) * 32 + kv * 4) = __float_as_uint(lsum) | 1u;
#pragma unroll
      for (int u = 0; u < 2; u++) { const unsigned pk = cvtpk(p[2 * u], p[2 * u + 1]); P[u] = u32x4{pk, dppu<QB1>(pk), dppu<QB2>(pk), dppu<QB3>(pk)}; }
    }
    int oaoff = WS_OA + slot * OA_STR + kv * 512 + (lane & 31) * 16;
    unsigned mska = 0xffu;
    asm volatile("" : "+v"(oaoff), "+v"(mska), "+v"(P[0]), "+v"(P[1]));
    STAMP(6);
    SB();
#pragma unroll
    for (int i = SMPOS; i < PVPOS; i++) VLOAD(i);
    f32x4 O[8];
#pragma unroll
    for (int i = 0; i < 4; i++) {
      u32x4 f0, f1;
#pragma unroll
      for (int j = 0; j < 4; j++) { f0[j] = perm_lo(V[4 + j][i], V[j][i]); f1[j] = perm_hi(V[4 + j][i], V[j][i]); }
      O[2 * i] = mfma32(P[0], f0, f32x4{0.f, 0.f, 0.f, 0.f}); O[2 * i + 1] = mfma32(P[0], f1, f32x4{0.f, 0.f, 0.f, 0.f});
    }
    asm volatile("" : "+v"(O[0]), "+v"(O[1]), "+v"(O[2]), "+v"(O[3]), "+v"(O[4]), "+v"(O[5]), "+v"(O[6]), "+v"(O[7]));
    SB();
#pragma unroll
    for (int i = PVPOS; i < 16; i++) VLOAD(i);
    STAMP(11);
    {
      u32x4 F[8];
#pragma unroll
      for (int j = 0; j < 3; j++) {
#pragma unroll
        for (int i = 0; i < 4; i++) { F[2 * i][j] = perm_lo(V[12 + j][i], V[8 + j][i]); F[2 * i + 1][j] = perm_hi(V[12 + j][i], V[8 + j][i]); }
        SB();
      }
      STAMP(7);
#pragma unroll
      for (int i = 0; i < 4; i++) {
        F[2 * i][3] = perm_lo(V[15][i], V[11][i]); F[2 * i + 1][3] = perm_hi(V[15][i], V[11][i]);
        O[2 * i] = mfma32(P[1], F[2 * i], O[2 * i]); O[2 * i + 1] = mfma32(P[1], F[2 * i + 1], O[2 * i + 1]);
      }
    }
    LDSV(f32x4, L_RED + kv * 2048 + lane * 32) = f32x4{O[0][0], O[1][0], O[2][0], O[3][0]};
    LDSV(f32x4, L_RED + kv * 2048 + lane * 32 + 16) = f32x4{O[4][0], O[5][0], O[6][0], O[7][0]};
    LDSV(unsigned char, F_PV + kv) = 1;
    STAMP(8);
    if (kv < 4) {
      const int half = lane >> 5, l5 = lane & 31;
      f32x4 r[4];
      NOUNROLL for (unsigned spin = 0;; spin++) {
        const u32x2 cnt = LDSV(u32x2, F_PV);
#pragma unroll
        for (int i = 0; i < 4; i++) r[i] = LDSV(f32x4, L_RED + (half * 4 + i) * 2048 + kv * 512 + l5 * 16);
        if ((cnt[0] & cnt[1]) == 0x01010101u) break;
        if (spin > (SPIN_CAP << 4)) break;
      }
      STAMP(9);
      const f32x4 a = (r[0] + r[1]) + (r[2] + r[3]);
      const u32x4 o = {tagm(half_sum(a[0]), mska, ta), __float_as_uint(half_sum(a[1])), tagm(half_sum(a[2]), mska, tb), __float_as_uint(half_sum(a[3]))};
      if (half == 0) ST128(o, rws, oaoff, 0, SC1);
      STAMP(10);
    } else if (kv == 4 && !(AB & 8)) {
      const int oi = (gi >> 3) * 512 + kvh * 64 + (gi & 7) * 8 + (c & 1) * 4 + g;
      const i32x4 rx = {KA[12], KA[13], KA[14], KA[15]}, ro = ka[9];
      float xfin = lo16(LD16(rx, oi * 2, 0, 0));
      int goff = WS_PB + ((lane >> 1) * 8 + kvh) * PB_STR + gi * 32 + (lane & 1) * 16;
      int ooff = c < 2 ? oi * 2 : OOB;
      asm volatile("" : "+v"(xfin), "+v"(goff), "+v"(ooff));
      lds_wait_eq(lds, F_T, 4u);
      if (PRE_B) __builtin_amdgcn_s_sleep(PRE_B);
      STAMP(10);
      u32x4 pb;
      unsigned spin = 0;
      NOUNROLL for (;; spin++) {
        CBAR();
        pb = LD128(rws, goff, 0, SC1);
        pb[0] ^= ta; pb[2] ^= tb;
        if (none(((pb[0] | pb[2]) & 0xffu) != 0)) break;
        if (spin > SPIN_CAP) break;
        __builtin_amdgcn_s_sleep(1);
      }
      STAMPV(15, spin);
      STAMP(9);
      auto s0 = __builtin_amdgcn_permlane32_swap(pb[0], pb[2], false, false);
      auto s1 = __builtin_amdgcn_permlane32_swap(pb[1], pb[3], false, false);
      const float y0 = __uint_as_float(s0[0]) + __uint_as_float(s0[1]), y1 = __uint_as_float(s1[0]) + __uint_as_float(s1[1]);
      auto s2 = __builtin_amdgcn_permlane16_swap(__float_as_uint(y0), __float_as_uint(y1), false, false);
      float z = __uint_as_float(s2[0]) + __uint_as_float(s2[1]);
      z += dpp<ROR8>(z); z += dpp<ROR4>(z); z += dpp<QX2>(z);
      z += xfin;
      ST16((u16)cvtpk(z, z), ro, ooff, 0, SC1);
      if (blk == 0 && lane == 0) ST32(T, rws, WS_EPOCH, 0, SC1);
      STAMP(12);
    }
    STAMP_FLUSH();
    return;
  }
  __builtin_amdgcn_s_setprio(3);
  const unsigned ep = LD32(rws, WS_EPOCH, 0, SC1);
  const int task = gi >> 1, kh = gi & 1;
  const unsigned tstart = (unsigned)__builtin_amdgcn_s_memrealtime();
  if (task < 15) {
    const bool isq = task < 8;
    const i32x4 rx = {KA[12], KA[13], KA[14], KA[15]};
    u32x4 xs[4], xf[1], W[8];
#pragma unroll
    for (int i = 0; i < 4; i++) xs[i] = LD128(rx, (c >> 2) * 1024 + g * 64 + (c & 3) * 16 + i * 256, 0, 0);
    xf[0] = LD128(rx, (c >> 2) * 1024 + (c & 1) * 64 + g * 16, kh * 512 + w * 128, 0);
    const int kvq = (kvh + XO_Q) & 7, idx = kvh * 7 + task - 8, mat = idx >= 28 ? 1 : 0;
    const int cb = isq ? kvq * 8 + task : idx - 28 * mat;
    const int rstride = isq ? 8192 : FF * 2;
    const i32x4 rw = {isq ? KB[0] : mat ? KB[8] : KB[4], isq ? KB[1] : mat ? KB[9] : KB[5], isq ? KB[2] : KB[6], KB[3]};
    const int woff = ((c >> 3) * 32 + g * 8) * rstride + (c & 7) * 16;
    int wso = (kh * 256 + w * 64) * rstride + cb * 128;
    unsigned tg = tstart + GOFF + (w * GTAU) / 4;
#pragma unroll
    for (int j = 0; j < 8; j++) {
      if (!isq) pace(tg, GTAU);
      W[j] = LD128(rw, woff, wso, NTL);
      wso += rstride;
      SB();
    }
    STAMP(1);
    float sm = 0.f;
#pragma unroll
    for (int i = 0; i < 4; i++)
#pragma unroll
      for (int j = 0; j < 4; j++) sm = dot2(xs[i][j], 0x3f803f80u, sm);
    sm += dpp<QX1>(sm); sm += dpp<QX2>(sm);
    const float mean = xrow_sum(sm) * (1.f / DM);
    f32x2 ssv = {0.f, 0.f};
#pragma unroll
    for (int i = 0; i < 4; i++)
#pragma unroll
      for (int j = 0; j < 4; j++) { const f32x2 d = f32x2{lo16(xs[i][j]), hi16(xs[i][j])} - mean; ssv += d * d; }
    float ss = ssv[0] + ssv[1];
    ss += dpp<QX1>(ss); ss += dpp<QX2>(ss);
    const float var = rbf(xrow_sum(ss) * (1.f / DM)), mb = rbf(mean);
    const float den = rbf(__builtin_amdgcn_sqrtf(rbf(var + 1e-5f)));
    const float rden = __builtin_amdgcn_rcpf(den);
#pragma unroll
    for (int i = 0; i < 1; i++)
#pragma unroll
      for (int j = 0; j < 4; j++) {
        const unsigned d = cvtpk(lo16(xf[i][j]) - mb, hi16(xf[i][j]) - mb);
        const f32x2 n = {lo16(d), hi16(d)};
        const f32x2 q = n * rden;
        const f32x2 r = (n - q * den) * rden + q;
        xf[i][j] = cvtpk(r[0], r[1]);
      }
    SB();
    STAMP(2);
    f32x4 acc[8];
#pragma unroll
    for (int e = 0; e < 8; e++) acc[e] = f32x4{0.f, 0.f, 0.f, 0.f};
#pragma unroll
    for (int t = 0; t < 1; t++) {
#pragma unroll
      for (int e = 0; e < 8; e++) {
        u32x4 f;
#pragma unroll
        for (int j = 0; j < 4; j++) f[j] = (e & 1) ? perm_hi(W[t * 8 + 2 * j + 1][e >> 1], W[t * 8 + 2 * j][e >> 1]) : perm_lo(W[t * 8 + 2 * j + 1][e >> 1], W[t * 8 + 2 * j][e >> 1]);
        acc[e] = mfma32(xf[t], f, acc[e]);
      }
      SB();
    }
    f32x4 o, z;
#pragma unroll
    for (int i = 0; i < 4; i++) {
      const float a = acc[i][0] + dpp<ROR8>(acc[i][1]), bq = acc[4 + i][1] + dpp<ROR8>(acc[4 + i][0]);
      o[i] = c < 8 ? a : bq;
    }
    if (meet4(lds, L_PR, F_P, w, lane, o, z)) {
      const unsigned t0 = __builtin_amdgcn_readfirstlane(ep) + 0x01010101u;
      const u32x4 pv = {tagm(z[0], 0xfu, t0), tagm(z[1], 0xfu, t0 >> 8), tagm(z[2], 0xfu, t0 >> 16), tagm(z[3], 0xfu, t0 >> 24)};
      const int col = (c & 7) * 8 + (c >> 3) * 4;
      const int poff = isq ? WS_Q + (((kvq * 4 + g) * 2 + kh) * 512 + task * 64 + col) * 4 : WS_GU + (((mat * 2 + kh) * 4 + g) * FF + cb * 64 + col) * 4;
      if (!(AB & 4)) ST128(pv, rws, poff, 0, QAUX);
    }
    STAMP(3);
  }
  const unsigned T = __builtin_amdgcn_readfirstlane(ep) + 0x01010101u;
  OPQ(pfd);
  LDSV(unsigned char, F_E + w) = 1;
  const int kva = (kvh + XO_A) & 7, hl = gi >> 3, hh = kva * 4 + hl, ng = gi & 7;
  const int bb = c >> 2, q4 = c & 3, sh = g >> 1;
  const int slot0 = (kva * 4 + bb) * 8 + sh * 4;
  if (w >= 2) {
    const int goff = w == 2 ? WS_Q + (kvh * 4 + b) * 4096 + lane * 16 : WS_GU + (g * FF + hh * 56 + (c < 14 ? c : 13) * 4) * 4;
    const int st1 = w == 2 ? 2048 : 4 * FF * 4, st2 = w == 2 ? 1024 : 8 * FF * 4;
    const u32x4 TN = {T & 0xfu, (T >> 8) & 0xfu, (T >> 16) & 0xfu, (T >> 24) & 0xfu};
    u32x4 q[4];
    sleep_until(tstart + (w == 2 ? TQ : TH));
    STAMP(4);
    unsigned spin = 0;
    if (AB & 1) { q[0] = TN; q[1] = TN; q[2] = TN; q[3] = TN; } else
    NOUNROLL for (;; spin++) {
      CBAR();
      q[0] = LD128(rws, goff, 0, QAUX); q[1] = LD128(rws, goff, st1, QAUX); q[2] = LD128(rws, goff, st2, QAUX); q[3] = LD128(rws, goff, st1 + st2, QAUX);
      unsigned bad = 0;
#pragma unroll
      for (int i = 0; i < 4; i++) { q[i] ^= TN; bad |= (q[i][0] | q[i][1]) | (q[i][2] | q[i][3]); }
      if (none((bad & 0xfu) != 0) || spin > SPIN_CAP) break;
      __builtin_amdgcn_s_sleep(8);
    }
    STAMPV(15, spin);
    const f32x4 y0 = F4(q[0]) + F4(q[1]), y1 = F4(q[2]) + F4(q[3]);
    const u32x2 z0 = {cvtpk(y0[0], y0[1]), cvtpk(y0[2], y0[3])}, z1 = {cvtpk(y1[0], y1[1]), cvtpk(y1[2], y1[3])};
    if (w == 2) {
      LDSV(u32x2, L_Q + lane * 8) = z0;
      LDSV(u32x2, L_Q + 512 + lane * 8) = z1;
      lds_wait_eq(lds, F_E, 0x01010101u);
      LDSV(u32x2, F_Q) = u32x2{1u, T};
    } else {
      u32x2 hp;
#pragma unroll
      for (int i = 0; i < 2; i++) hp[i] = c < 14 ? cvtpk(lo16(z0[i]) * lo16(z1[i]), hi16(z0[i]) * hi16(z1[i])) : 0u;
      LDSV(u32x2, L_H + g * 128 + c * 8) = hp;
      LDSV(unsigned char, F_HW) = 1;
    }
    STAMP(5);
  } else if (AB & 8) {
  } else if (w == 0) {
    unsigned lv;
    NOUNROLL for (unsigned spin = 0;; spin++) {
      lv = LDSV(unsigned, L_LW + (lane & 31) * 4);
      if (none(lv == 0u) || spin > (SPIN_CAP << 4)) break;
      __builtin_amdgcn_s_sleep(2);
    }
    float mv = hi16(LDSV(unsigned, L_MW + (lane & 31) * 4));
    float lsum = __uint_as_float(lv);
    lsum += dpp<QX1>(lsum); lsum += dpp<QX2>(lsum); lsum += dpp<HMIR>(lsum);
    mv = fmaxf(mv, dpp<QX1>(mv)); mv = fmaxf(mv, dpp<QX2>(mv)); mv = fmaxf(mv, dpp<HMIR>(mv));
    const u32x4 st = {__float_as_uint(mv), T, __float_as_uint(lsum), T};
    if ((lane & 39) == 0) ST128(st, rws, WS_ST + slot * 128 + (lane >> 3) * 16, 0, SAS);
    STAMP(5);
  } else {
    const int soff = WS_ST + (kva * 32 + (lane & 31)) * 128 + hl * 16;
    u32x4 s0;
    unsigned spin = 0;
    lds_wait8(lds, F_S);
    STAMP(4);
    if (lane < 32) {
      NOUNROLL for (;; spin++) {
        CBAR();
        s0 = LD128(rws, soff, 0, SAL);
        if (none(s0[1] != T || s0[3] != T) || spin > SPIN_CAP) break;
        __builtin_amdgcn_s_sleep(8);
      }
      const float m1 = __uint_as_float(s0[0]);
      float MM = fmaxf(m1, dpp<QX1>(m1));
      MM = fmaxf(MM, dpp<QX2>(MM)); MM = fmaxf(MM, dpp<HMIR>(MM));
      const float e1 = __builtin_amdgcn_exp2f((m1 - MM) * LOG2E);
      float L = e1 * __uint_as_float(s0[2]);
      L += dpp<QX1>(L); L += dpp<QX2>(L); L += dpp<HMIR>(L);
      LDSV(float, L_WT + lane * 4) = e1 * __builtin_amdgcn_rcpf(L);
    }
    LDSV(unsigned char, F_HW + 1) = 1;
    STAMPV(15, spin);
    STAMP(5);
  }
  if (AB & 8) { if (blk == 0 && w == 2) { lds_wait8(lds, F_PV); __builtin_amdgcn_s_sleep(100); if (lane == 0) ST32(T, rws, WS_EPOCH, 0, SC1); } STAMP_FLUSH(); return; }
  int aoff = WS_OA + slot0 * OA_STR + hl * 512 + w * 128 + (g & 1) * 64 + q4 * 16;
  int pboff = WS_PB + (hh * 8 + ng) * PB_STR + lane * 16;
  int wooff = (16 * (g & 1) + 2 * (g >> 1)) * 1024 + c * 8;
  int wdoff = (w == 3 && g >= 2) ? OOB - 4096 : g * 4096 + c * 8;
  const unsigned ta = T & 0xffu, tb = (T >> 16) & 0xffu;
  unsigned mska = 0xffu;
  asm volatile("" : "+v"(aoff), "+v"(pboff), "+v"(wooff), "+v"(wdoff), "+v"(mska));
  const i32x4 rwo = {KB[12], KB[13], KB[14], KB[15]}, rwd = ka[8];
  const int wos = (hh * 128 + w * 32) * 1024 + ng * 128, wds = (hh * 56 + w * 16) * 1024 + ng * 128;
  u32x2 WT[12];
#define WLOAD(j) do { if (AB & 1024) { WT[j] = u32x2{1u, 2u}; OPQ(WT[j]); } else if ((j) < 4) WT[j] = LD64(rwd, wdoff + (j) * 1024, wds, NTL); \
    else WT[j] = LD64(rwo, wooff + ((j) & 1) * 1024, wos + (((j) - 4) >> 1) * 4096, NTL); SB(); } while (0)
  if (NEARLY) {
    unsigned te = now32() + (w * ETAU) / 4;
#pragma unroll
    for (int j = 0; j < NEARLY; j++) { pace(te, ETAU); WLOAD(j); }
  }
  lds_wait8(lds, F_VI);
  const unsigned tvi = __builtin_amdgcn_readfirstlane(LDSV(unsigned, F_TV));
  STAMP(6);
  {
    unsigned tw = tvi + WDEL + (w * WTAU) / 4;
#pragma unroll
    for (int j = NEARLY; j < 12; j++) { if (WTAU || j == NEARLY) pace(tw, WTAU); WLOAD(j); }
  }
  lds_wait_eq(lds, F_HW, 0x0101u);
  const f32x4 wt = LDSV(f32x4, L_WT + bb * 32 + sh * 16);
  const u32x2 hf = LDSV(u32x2, L_H + bb * 128 + w * 32 + g * 8);
  STAMP(8);
  lds_wait8(lds, F_PV);
  if (PRE_A) __builtin_amdgcn_s_sleep(PRE_A);
  { unsigned tA = tvi + TA_ADD; pace(tA, 0); }
  STAMP(9);
  u32x4 a[4];
#pragma unroll
  for (int i = 0; i < 4; i++) a[i] = LD128(rws, aoff, i * OA_STR, SC1);
  SB();
  u32x4 fo[4];
  f32x4 acc[4];
#pragma unroll
  for (int e = 0; e < 4; e++) {
#pragma unroll
    for (int j = 0; j < 4; j++) fo[e][j] = (e & 1) ? perm_hi(WT[4 + 2 * j + 1][e >> 1], WT[4 + 2 * j][e >> 1]) : perm_lo(WT[4 + 2 * j + 1][e >> 1], WT[4 + 2 * j][e >> 1]);
    u32x2 fd;
#pragma unroll
    for (int j = 0; j < 2; j++) fd[j] = (e & 1) ? perm_hi(WT[2 * j + 1][e >> 1], WT[2 * j][e >> 1]) : perm_lo(WT[2 * j + 1][e >> 1], WT[2 * j][e >> 1]);
    acc[e] = mfma16(hf, fd, f32x4{0.f, 0.f, 0.f, 0.f});
  }
  asm volatile("" : "+v"(acc[0]), "+v"(acc[1]), "+v"(acc[2]), "+v"(acc[3]), "+v"(fo[0]), "+v"(fo[1]), "+v"(fo[2]), "+v"(fo[3]));
  STAMP(7);
  {
    unsigned spin = 0;
    NOUNROLL for (;; spin++) {
      unsigned bad = 0;
#pragma unroll
      for (int i = 0; i < 4; i++) { a[i][0] ^= ta; a[i][2] ^= tb; bad |= a[i][0] | a[i][2]; }
      if (none((bad & 0xffu) != 0)) break;
      if (spin > SPIN_CAP) break;
      __builtin_amdgcn_s_sleep(1);
      CBAR();
#pragma unroll
      for (int i = 0; i < 4; i++) a[i] = LD128(rws, aoff, i * OA_STR, SC1);
    }
    STAMPV(13, spin);
  }
  STAMP(10);
  f32x4 t = F4(a[0]) * wt[0];
#pragma unroll
  for (int i = 1; i < 4; i++) t = __builtin_elementwise_fma(F4(a[i]), f32x4{wt[i], wt[i], wt[i], wt[i]}, t);
  auto p0 = __builtin_amdgcn_permlane32_swap(__float_as_uint(t[0]), __float_as_uint(t[2]), false, false);
  auto p1 = __builtin_amdgcn_permlane32_swap(__float_as_uint(t[1]), __float_as_uint(t[3]), false, false);
  const unsigned own = cvtpk(__uint_as_float(p0[0]) + __uint_as_float(p0[1]), __uint_as_float(p1[0]) + __uint_as_float(p1[1]));
  const u32x4 A = {own, dppu<QB1>(own), dppu<QB2>(own), dppu<QB3>(own)};
#pragma unroll
  for (int e = 0; e < 4; e++) acc[e] = mfma32(A, fo[e], acc[e]);
  f32x4 z;
  if (meet4(lds, L_TR, F_T, w, lane, f32x4{acc[0][0], acc[1][0], acc[2][0], acc[3][0]}, z)) {
    const u32x4 o = {tagm(z[0], mska, ta), __float_as_uint(z[1]), tagm(z[2], mska, tb), __float_as_uint(z[3])};
    ST128(o, rws, pboff, 0, SC1);
  }
  STAMP(11);
  STAMP_FLUSH();
}

extern "C" void kernel_launch(void* const* d_in, const int* in_sizes, int n_in, void* d_out, int out_size, void* d_ws, size_t ws_size, hipStream_t stream) {
  const unsigned fl = 0x00020000u;
  fused<<<256, NTHR, 0, stream>>>(d_in[1], 33554432u, fl, d_in[2], 33554432u, fl, d_ws, (unsigned)WS_END, fl, d_in[0], 4096u, fl,
                                  d_in[3], 4194304u, fl, d_in[6], 1835008u, fl, d_in[7], 1835008u, fl, d_in[4], 4194304u, fl,
                                  d_in[8], 1835008u, fl, d_out, 4096u, fl);
}
```

```cpp
#include <hip/hip_runtime.h>
#include <stdint.h>

#define WIN 8
#define N0 2
#define TOFF 30
#define TAU1 400
#define I1 8
#define TAU 265
#define QKPOS 3
#define SMPOS 5
#define PVPOS 12
#define STPOS 10
#define WOPOS 16
#define GTAU 20
#define WDEL 0
#define NEARLY 0
#define ETAU 20
#define W0 2
#define DEL 40
#define GOFF 0
#define WTAU 5
#define TQ 400
#define TH 600
#define TA_ADD 140
#define PRE_A 4
#define PRE_B 8
#define CODE_BYTES 10752
#define QAUX 16
#define SAS 16
#define SAL 16
#define XO_Q 1
#define XO_A 4
#define WSPOS 1
#define WSOFF (WSPOS == 0 ? (size_t)0 : WSPOS == 1 ? ((ws_size - (size_t)WS_END) & ~(size_t)0x3fffff) : ((ws_size / 2) & ~(size_t)0x3fffff))
#define WSOFFK (WSPOS == 0 ? (size_t)0 : WSPOS == 1 ? (size_t)(252 << 20) : (size_t)(128 << 20))
#define AB 0

typedef unsigned short u16;
typedef int i32x4 __attribute__((ext_vector_type(4)));
typedef int i32x16 __attribute__((ext_vector_type(16)));
typedef unsigned u32x2 __attribute__((ext_vector_type(2)));
typedef unsigned u32x4 __attribute__((ext_vector_type(4)));
typedef float f32x2 __attribute__((ext_vector_type(2)));
typedef float f32x4 __attribute__((ext_vector_type(4)));
typedef short s16x4 __attribute__((ext_vector_type(4)));
typedef short s16x8 __attribute__((ext_vector_type(8)));
typedef __bf16 bf16x2 __attribute__((ext_vector_type(2)));
#define LAS __attribute__((address_space(3)))

__device__ u16 LD16(i32x4 r, int v, int s, int a) __asm("llvm.amdgcn.raw.buffer.load.i16");
__device__ unsigned LD32(i32x4 r, int v, int s, int a) __asm("llvm.amdgcn.raw.buffer.load.i32");
__device__ u32x2 LD64(i32x4 r, int v, int s, int a) __asm("llvm.amdgcn.raw.buffer.load.v2i32");
__device__ u32x4 LD128(i32x4 r, int v, int s, int a) __asm("llvm.amdgcn.raw.buffer.load.v4i32");
__device__ void ST16(u16 d, i32x4 r, int v, int s, int a) __asm("llvm.amdgcn.raw.buffer.store.i16");
__device__ void ST32(unsigned d, i32x4 r, int v, int s, int a) __asm("llvm.amdgcn.raw.buffer.store.i32");
__device__ void ST128(u32x4 d, i32x4 r, int v, int s, int a) __asm("llvm.amdgcn.raw.buffer.store.v4i32");
constexpr int NTL = 2, SC1 = 16;

constexpr int DM = 512, FF = 1792;
constexpr int NT = 4, NTHR = (NT + 8) * 64;
constexpr int OA_STR = 2176, PB_STR = 1152;
constexpr int WS_EPOCH = 0;
constexpr int WS_Q = 4096;
constexpr int WS_GU = WS_Q + 131072;
constexpr int WS_ST = WS_GU + 114688;
constexpr int WS_OA = WS_ST + 32768;
constexpr int WS_PB = WS_OA + 256 * OA_STR;
constexpr int WS_END = WS_PB + 256 * PB_STR;
constexpr int WS_TS = 4 << 20;
constexpr int L_RED = 0;
constexpr int L_TR = L_RED + 16384;
constexpr int L_PR = L_TR + 4096;
constexpr int L_Q = L_PR + 4096;
constexpr int L_H = L_Q + 1024;
constexpr int L_WT = L_H + 512;
constexpr int L_MW = L_WT + 128;
constexpr int L_LW = L_MW + 128;
constexpr int L_FLAG = L_LW + 128;
constexpr int F_Q = L_FLAG, F_HW = L_FLAG + 8, F_E = L_FLAG + 12, F_VI = L_FLAG + 16, F_PV = L_FLAG + 24, F_T = L_FLAG + 32, F_P = L_FLAG + 36, F_S = L_FLAG + 40, F_TV = L_FLAG + 48;
constexpr int L_END = L_FLAG + 64;
constexpr unsigned SPIN_CAP = 1u << 18;
constexpr int OOB = 0x7ffffff0;
constexpr float LOG2E = 1.4426950408889634f;
constexpr int sched(int i) { return TOFF + ((i < I1 ? i - N0 + 1 : I1 - N0) * TAU1 + (i < I1 ? 0 : i - I1 + 1) * TAU) / 10; }

__device__ __forceinline__ float lo16(unsigned u) { return __uint_as_float(u << 16); }
__device__ __forceinline__ float hi16(unsigned u) { return __uint_as_float(u & 0xffff0000u); }
__device__ __forceinline__ unsigned cvtpk(float lo, float hi) { f32x2 f = {lo, hi}; return __builtin_bit_cast(unsigned, __builtin_convertvector(f, bf16x2)); }
__device__ __forceinline__ float rbf(float f) { return lo16(cvtpk(f, f)); }
__device__ __forceinline__ f32x4 mfma32(u32x4 a, u32x4 b, f32x4 c) { return __builtin_amdgcn_mfma_f32_16x16x32_bf16(__builtin_bit_cast(s16x8, a), __builtin_bit_cast(s16x8, b), c, 0, 0, 0); }
__device__ __forceinline__ f32x4 mfma16(u32x2 a, u32x2 b, f32x4 c) { return __builtin_amdgcn_mfma_f32_16x16x16bf16_1k(__builtin_bit_cast(s16x4, a), __builtin_bit_cast(s16x4, b), c, 0, 0, 0); }
template <int CTRL> __device__ __forceinline__ float dpp(float x) { return __builtin_bit_cast(float, __builtin_amdgcn_mov_dpp(__builtin_bit_cast(int, x), CTRL, 0xf, 0xf, true)); }
template <int CTRL> __device__ __forceinline__ unsigned dppu(unsigned x) { return (unsigned)__builtin_amdgcn_mov_dpp((int)x, CTRL, 0xf, 0xf, true); }
constexpr int ROR4 = 0x124, ROR8 = 0x128, HMIR = 0x141, QX1 = 0xB1, QX2 = 0x4E, QB0 = 0x00, QB1 = 0x55, QB2 = 0xAA, QB3 = 0xFF;
__device__ __forceinline__ float xrow_max(float x) {
  auto s = __builtin_amdgcn_permlane16_swap(__float_as_uint(x), __float_as_uint(x), false, false);
  x = fmaxf(__uint_as_float(s[0]), __uint_as_float(s[1]));
  auto t = __builtin_amdgcn_permlane32_swap(__float_as_uint(x), __float_as_uint(x), false, false);
  return fmaxf(__uint_as_float(t[0]), __uint_as_float(t[1]));
}
__device__ __forceinline__ float xrow_sum(float x) {
  auto s = __builtin_amdgcn_permlane16_swap(__float_as_uint(x), __float_as_uint(x), false, false);
  x = __uint_as_float(s[0]) + __uint_as_float(s[1]);
  auto t = __builtin_amdgcn_permlane32_swap(__float_as_uint(x), __float_as_uint(x), false, false);
  return __uint_as_float(t[0]) + __uint_as_float(t[1]);
}
__device__ __forceinline__ float half_sum(float x) {
  auto t = __builtin_amdgcn_permlane32_swap(__float_as_uint(x), __float_as_uint(x), false, false);
  return __uint_as_float(t[0]) + __uint_as_float(t[1]);
}
__device__ __forceinline__ float dot2(unsigned a, unsigned b, float c) { return __builtin_amdgcn_fdot2_f32_bf16(__builtin_bit_cast(bf16x2, a), __builtin_bit_cast(bf16x2, b), c, false); }
__device__ __forceinline__ unsigned tagm(float v, unsigned m, unsigned t) { return (__float_as_uint(v) & ~m) | (t & m); }
__device__ __forceinline__ unsigned perm_lo(unsigned hi, unsigned lo) { return __builtin_amdgcn_perm(hi, lo, 0x05040100u); }
__device__ __forceinline__ unsigned perm_hi(unsigned hi, unsigned lo) { return __builtin_amdgcn_perm(hi, lo, 0x07060302u); }
__device__ __forceinline__ bool none(bool p) { return __builtin_amdgcn_ballot_w64(p) == 0ull; }
#define F4(v) __builtin_bit_cast(f32x4, v)
#define SB() __builtin_amdgcn_sched_barrier(0)
#define CBAR() asm volatile("" ::: "memory")
#define OPQ(x) asm volatile("" : "+v"(x))
#define LDSV(T, off) (*(volatile LAS T*)(lds + (off)))
#define NOUNROLL _Pragma("clang loop unroll(disable)")

__device__ __forceinline__ unsigned now32() { unsigned long long t; asm volatile("s_memrealtime %0\n\ts_waitcnt lgkmcnt(0)" : "=s"(t)::"memory"); return (unsigned)t; }
__device__ __forceinline__ void pace(unsigned& tgt, int step) {
  asm volatile("1:\n\ts_memrealtime vcc\n\ts_waitcnt lgkmcnt(0)\n\ts_sub_u32 vcc_lo, vcc_lo, %0\n\ts_cmp_lt_i32 vcc_lo, 0\n\ts_cbranch_scc1 1b\n\ts_add_u32 %0, %0, %1"
               : "+s"(tgt) : "n"(step) : "memory", "scc", "vcc");
}
__device__ __forceinline__ void apace(unsigned& tgt, int step) {
  unsigned tmp;
  asm volatile("1:\n\ts_memrealtime vcc\n\ts_getreg_b32 %1, hwreg(HW_REG_IB_STS, 0, 4)\n\ts_sub_i32 %1, %1, %3\n\ts_max_i32 %1, %1, 0\n\ts_mul_i32 %1, %1, %4\n\t"
               "s_waitcnt lgkmcnt(0)\n\ts_sub_u32 vcc_lo, vcc_lo, %0\n\ts_cmp_lt_i32 vcc_lo, %1\n\ts_cbranch_scc1 1b\n\ts_add_u32 %0, %0, %2"
               : "+s"(tgt), "=&s"(tmp) : "n"(step), "n"(W0 - 1), "n"(DEL) : "memory", "scc", "vcc");
}
__device__ __forceinline__ void sleep_until(unsigned tgt) {
  asm volatile("1:\n\ts_sleep 1\n\ts_memrealtime vcc\n\ts_waitcnt lgkmcnt(0)\n\ts_sub_u32 vcc_lo, vcc_lo, %0\n\ts_cmp_lt_i32 vcc_lo, 0\n\ts_cbranch_scc1 1b"
               :: "s"(tgt) : "memory", "scc", "vcc");
}
#define STAMP(i)
#define STAMPV(i, val)
#define STAMP_FLUSH()

__device__ __forceinline__ void lds_wait_eq(LAS unsigned char* lds, int off, unsigned n) {
  NOUNROLL for (unsigned spin = 0; LDSV(unsigned, off) != n && spin < (SPIN_CAP << 4); spin++) __builtin_amdgcn_s_sleep(1);
  CBAR();
}
__device__ __forceinline__ void lds_wait8(LAS unsigned char* lds, int off) {
  NOUNROLL for (unsigned spin = 0; spin < (SPIN_CAP << 4); spin++) {
    const u32x2 f = LDSV(u32x2, off);
    if ((f[0] & f[1]) == 0x01010101u) break;
    __builtin_amdgcn_s_sleep(1);
  }
  CBAR();
}
__device__ __forceinline__ bool meet4(LAS unsigned char* lds, int area, int cnt, int w, int lane, f32x4 v, f32x4& sum) {
  LDSV(f32x4, area + w * 1024 + lane * 16) = v;
  unsigned prev;
  f32x4 r0, r1, r2, r3;
  const unsigned la = (unsigned)(uintptr_t)(lds + area) + lane * 16, lf = (unsigned)(uintptr_t)(lds + cnt), one = 1u;
  asm volatile("s_mov_b64 exec, 1\n\tds_add_rtn_u32 %0, %6, %7\n\ts_mov_b64 exec, -1\n\t"
               "ds_read_b128 %1, %5\n\tds_read_b128 %2, %5 offset:1024\n\tds_read_b128 %3, %5 offset:2048\n\tds_read_b128 %4, %5 offset:3072\n\t"
               "s_waitcnt lgkmcnt(0)"
               : "=&v"(prev), "=&v"(r0), "=&v"(r1), "=&v"(r2), "=&v"(r3) : "v"(la), "v"(lf), "v"(one) : "memory");
  sum = (r0 + r1) + (r2 + r3);
  return __builtin_amdgcn_readfirstlane(prev) == 3u;
}

#define DESC(n) const void* p_##n, unsigned n##_bytes, unsigned n##_flags
__global__ void __launch_bounds__(NTHR, 3) fused(DESC(k), DESC(v), DESC(ws), DESC(x), DESC(wq), DESC(wg), DESC(wu), DESC(wo), DESC(wd), DESC(out)) {
  __shared__ __attribute__((aligned(16))) unsigned char smem[L_END];
  auto ka = (const __attribute__((address_space(4))) i32x4*)__builtin_amdgcn_kernarg_segment_ptr();
  i32x16 KA, KB;
  unsigned long long pc0;
  asm volatile("s_load_dwordx16 %0, %3, 0x0\n\ts_load_dwordx16 %1, %3, 0x40\n\ts_getpc_b64 %2" : "=&s"(KA), "=&s"(KB), "=&s"(pc0) : "s"(ka));
  SB();
  LAS unsigned char* lds = (LAS unsigned char*)smem;
  const int blk = blockIdx.x, kvh = blk & 7, gi = blk >> 3, b = gi & 3, s = gi >> 2;
  const int tid = threadIdx.x, lane = tid & 63, w = __builtin_amdgcn_readfirstlane(tid >> 6), g = lane >> 4, c = lane & 15;
  u32x2 pfd = {0u, 0u};
  if (w == 3 && lane < CODE_BYTES / 256) {
    const int off = gi * (CODE_BYTES / 32) + lane * 8;
    asm volatile("global_load_dwordx2 %0, %1, %2" : "=v"(pfd) : "v"(off), "s"(pc0 & ~0xffull));
  }
  if (w == 2) {
    LDSV(unsigned, L_MW + lane * 4) = 0u;
    LDSV(unsigned, L_FLAG + c * 4) = 0u;
  }
  const int kv = w - NT;
  int koff = (c >> 2) * 256 + g * 64 + (c & 3) * 16;
  int kvbase = ((b * 8 + kvh) * 4096 + s * 512) * 256 + kv * 4096;
  int so0 = kvbase + (gi & 3) * 1024;
  OPQ(koff);
  asm volatile("s_waitcnt lgkmcnt(0)\n\ts_barrier" : "+s"(KA), "+s"(KB), "+s"(kvbase), "+s"(so0)::"memory");
  const i32x4 rws = {KA[8], KA[9], KA[10], KA[11]};
  const int slot = (kvh * 4 + b) * 8 + s;
  if (__builtin_expect(w >= NT, 1)) {
    const i32x4 rk = {KA[0], KA[1], KA[2], KA[3]}, rv = {KA[4], KA[5], KA[6], KA[7]};
    u32x4 K[16], V[16];
    const int voff = lane * 16;
#define SOFF(i) (((i) < 4 ? so0 : kvbase + ((((i) >> 2) + gi) & 3) * 1024) + ((i) & 3) * 32768)
#define WAITN(n) asm volatile("s_waitcnt vmcnt(%0)" ::"n"((n) - 1) : "memory")
#define PACE(i) do { if (DEL) apace(tgt, sched((i) + 1) - sched(i)); else pace(tgt, sched((i) + 1) - sched(i)); } while (0)
#define VLOAD(i) do { WAITN(WIN); PACE(16 + (i)); V[i] = LD128(rv, voff, SOFF(i), NTL); \
    if ((i) + 1 == STPOS) LDSV(unsigned char, F_S + kv) = 1; if ((i) + 1 == WOPOS) { LDSV(unsigned, F_TV) = now32(); LDSV(unsigned char, F_VI + kv) = 1; } SB(); } while (0)
#pragma unroll
    for (int i = 0; i < N0; i++) { K[i] = LD128(rk, koff, SOFF(i), NTL); SB(); }
    unsigned tgt = now32() + sched(N0) + (kv * TAU) / 80;
#pragma unroll
    for (int i = N0; i < 16; i++) {
      if (i >= WIN) WAITN(WIN);
      PACE(i);
      K[i] = LD128(rk, koff, SOFF(i), NTL);
      SB();
    }
    STAMP(1);
#pragma unroll
    for (int i = 0; i < QKPOS; i++) VLOAD(i);
    STAMP(2);
    u32x2 fl = LDSV(u32x2, F_Q);
    NOUNROLL for (unsigned spin = 0; fl[0] == 0u && spin < (SPIN_CAP << 4); spin++) { __builtin_amdgcn_s_sleep(1); fl = LDSV(u32x2, F_Q); }
    CBAR();
    const unsigned T = __builtin_amdgcn_readfirstlane(fl[1]);
    const unsigned ta = T & 0xffu, tb = (T >> 16) & 0xffu;
    STAMP(3);
    float S[4];
    {
      const u32x4 qf = LDSV(u32x4, L_Q + koff);
      const bool a1 = c & 1, a2 = c & 2;
#pragma unroll
      for (int t = 0; t < 4; t++) {
        float y[4];
#pragma unroll
        for (int i = 0; i < 4; i++) {
          const f32x4 d = mfma32(K[t * 4 + i], qf, f32x4{0.f, 0.f, 0.f, 0.f});
          float z = dpp<QB0>(d[0]);
          z += dpp<QB1>(d[1]); OPQ(z);
          z += dpp<QB2>(d[2]); OPQ(z);
          z += dpp<QB3>(d[3]); OPQ(z);
          y[i] = z;
        }
        const float u0 = a1 ? y[1] : y[0], u1 = a1 ? y[3] : y[2];
        S[t] = a2 ? u1 : u0;
      }
#pragma unroll
      for (int i = 0; i < 4; i += 2) {
        const unsigned p = cvtpk(S[i], S[i + 1]);
        const unsigned p2 = cvtpk(lo16(p) * 0.08838834764831845f, hi16(p) * 0.08838834764831845f);
        S[i] = lo16(p2); S[i + 1] = hi16(p2);
      }
      float mw = fmaxf(fmaxf(S[0], S[1]), fmaxf(S[2], S[3]));
      mw = fmaxf(mw, dpp<QX1>(mw)); mw = fmaxf(mw, dpp<QX2>(mw));
      mw = xrow_max(mw);
      LDSV(unsigned, L_MW + (c >> 2) * 32 + kv * 4) = __float_as_uint(mw) | 1u;
    }
    STAMP(4);
    SB();
#pragma unroll
    for (int i = QKPOS; i < SMPOS; i++) VLOAD(i);
    STAMP(5);
    u32x4 P[2];
    {
      u32x4 m0, m1;
      NOUNROLL for (unsigned spin = 0;; spin++) {
        m0 = LDSV(u32x4, L_MW + (c >> 2) * 32); m1 = LDSV(u32x4, L_MW + (c >> 2) * 32 + 16);
        const u32x4 mm = m0 & m1;
        if (none((mm[0] & mm[1] & mm[2] & mm[3] & 1u) == 0u) || spin > (SPIN_CAP << 4)) break;
        __builtin_amdgcn_s_sleep(1);
      }
      float M = -INFINITY;
#pragma unroll
      for (int i = 0; i < 4; i++) M = fmaxf(M, fmaxf(hi16(m0[i]), hi16(m1[i])));
      const float mneg = -M * LOG2E;
      float p[4];
#pragma unroll
      for (int i = 0; i < 4; i++) p[i] = __builtin_amdgcn_exp2f(fmaf(S[i], LOG2E, mneg));
      float lsum = (p[0] + p[1]) + (p[2] + p[3]);
      lsum += dpp<QX1>(lsum); lsum += dpp<QX2>(lsum);
      lsum = xrow_sum(lsum);
      LDSV(unsigned, L_LW + (c >> 2) * 32 + kv * 4) = __float_as_uint(lsum) | 1u;
#pragma unroll
      for (int u = 0; u < 2; u++) { const unsigned pk = cvtpk(p[2 * u], p[2 * u + 1]); P[u] = u32x4{pk, dppu<QB1>(pk), dppu<QB2>(pk), dppu<QB3>(pk)}; }
    }
    int oaoff = WS_OA + slot * OA_STR + kv * 512 + (lane & 31) * 16;
    unsigned mska = 0xffu;
    asm volatile("" : "+v"(oaoff), "+v"(mska), "+v"(P[0]), "+v"(P[1]));
    STAMP(6);
    SB();
#pragma unroll
    for (int i = SMPOS; i < PVPOS; i++) VLOAD(i);
    f32x4 O[8];
#pragma unroll
    for (int i = 0; i < 4; i++) {
      u32x4 f0, f1;
#pragma unroll
      for (int j = 0; j < 4; j++) { f0[j] = perm_lo(V[4 + j][i], V[j][i]); f1[j] = perm_hi(V[4 + j][i], V[j][i]); }
      O[2 * i] = mfma32(P[0], f0, f32x4{0.f, 0.f, 0.f, 0.f}); O[2 * i + 1] = mfma32(P[0], f1, f32x4{0.f, 0.f, 0.f, 0.f});
    }
    asm volatile("" : "+v"(O[0]), "+v"(O[1]), "+v"(O[2]), "+v"(O[3]), "+v"(O[4]), "+v"(O[5]), "+v"(O[6]), "+v"(O[7]));
    SB();
#pragma unroll
    for (int i = PVPOS; i < 16; i++) VLOAD(i);
    STAMP(11);
    {
      u32x4 F[8];
#pragma unroll
      for (int j = 0; j < 3; j++) {
#pragma unroll
        for (int i = 0; i < 4; i++) { F[2 * i][j] = perm_lo(V[12 + j][i], V[8 + j][i]); F[2 * i + 1][j] = perm_hi(V[12 + j][i], V[8 + j][i]); }
        SB();
      }
      STAMP(7);
#pragma unroll
      for (int i = 0; i < 4; i++) {
        F[2 * i][3] = perm_lo(V[15][i], V[11][i]); F[2 * i + 1][3] = perm_hi(V[15][i], V[11][i]);
        O[2 * i] = mfma32(P[1], F[2 * i], O[2 * i]); O[2 * i + 1] = mfma32(P[1], F[2 * i + 1], O[2 * i + 1]);
      }
    }
    LDSV(f32x4, L_RED + kv * 2048 + lane * 32) = f32x4{O[0][0], O[1][0], O[2][0], O[3][0]};
    LDSV(f32x4, L_RED + kv * 2048 + lane * 32 + 16) = f32x4{O[4][0], O[5][0], O[6][0], O[7][0]};
    LDSV(unsigned char, F_PV + kv) = 1;
    STAMP(8);
    if (kv < 4) {
      const int half = lane >> 5, l5 = lane & 31;
      f32x4 r[4];
      NOUNROLL for (unsigned spin = 0;; spin++) {
        const u32x2 cnt = LDSV(u32x2, F_PV);
#pragma unroll
        for (int i = 0; i < 4; i++) r[i] = LDSV(f32x4, L_RED + (half * 4 + i) * 2048 + kv * 512 + l5 * 16);
        if ((cnt[0] & cnt[1]) == 0x01010101u) break;
        if (spin > (SPIN_CAP << 4)) break;
      }
      STAMP(9);
      const f32x4 a = (r[0] + r[1]) + (r[2] + r[3]);
      const u32x4 o = {tagm(half_sum(a[0]), mska, ta), __float_as_uint(half_sum(a[1])), tagm(half_sum(a[2]), mska, tb), __float_as_uint(half_sum(a[3]))};
      if (half == 0) ST128(o, rws, oaoff, 0, SC1);
      STAMP(10);
    } else if (kv == 4 && !(AB & 8)) {
      const int oi = (gi >> 3) * 512 + kvh * 64 + (gi & 7) * 8 + (c & 1) * 4 + g;
      const i32x4 rx = {KA[12], KA[13], KA[14], KA[15]}, ro = ka[9];
      float xfin = lo16(LD16(rx, oi * 2, 0, 0));
      int goff = WS_PB + ((lane >> 1) * 8 + kvh) * PB_STR + gi * 32 + (lane & 1) * 16;
      int ooff = c < 2 ? oi * 2 : OOB;
      asm volatile("" : "+v"(xfin), "+v"(goff), "+v"(ooff));
      lds_wait_eq(lds, F_T, 4u);
      if (PRE_B) __builtin_amdgcn_s_sleep(PRE_B);
      STAMP(10);
      u32x4 pb;
      unsigned spin = 0;
      NOUNROLL for (;; spin++) {
        CBAR();
        pb = LD128(rws, goff, 0, SC1);
        pb[0] ^= ta; pb[2] ^= tb;
        if (none(((pb[0] | pb[2]) & 0xffu) != 0)) break;
        if (spin > SPIN_CAP) break;
        __builtin_amdgcn_s_sleep(1);
      }
      STAMPV(15, spin);
      STAMP(9);
      auto s0 = __builtin_amdgcn_permlane32_swap(pb[0], pb[2], false, false);
      auto s1 = __builtin_amdgcn_permlane32_swap(pb[1], pb[3], false, false);
      const float y0 = __uint_as_float(s0[0]) + __uint_as_float(s0[1]), y1 = __uint_as_float(s1[0]) + __uint_as_float(s1[1]);
      auto s2 = __builtin_amdgcn_permlane16_swap(__float_as_uint(y0), __float_as_uint(y1), false, false);
      float z = __uint_as_float(s2[0]) + __uint_as_float(s2[1]);
      z += dpp<ROR8>(z); z += dpp<ROR4>(z); z += dpp<QX2>(z);
      z += xfin;
      ST16((u16)cvtpk(z, z), ro, ooff, 0, SC1);
      if (blk == 0 && lane == 0) ST32(T, rws, WS_EPOCH, 0, SC1);
      STAMP(12);
    }
    STAMP_FLUSH();
    return;
  }
  __builtin_amdgcn_s_setprio(3);
  const unsigned ep = LD32(rws, WS_EPOCH, 0, SC1);
  const int task = gi >> 1, kh = gi & 1;
  const unsigned tstart = (unsigned)__builtin_amdgcn_s_memrealtime();
  if (task < 15) {
    const bool isq = task < 8;
    const i32x4 rx = {KA[12], KA[13], KA[14], KA[15]};
    u32x4 xs[4], xf[1], W[8];
#pragma unroll
    for (int i = 0; i < 4; i++) xs[i] = LD128(rx, (c >> 2) * 1024 + g * 64 + (c & 3) * 16 + i * 256, 0, 0);
    xf[0] = LD128(rx, (c >> 2) * 1024 + (c & 1) * 64 + g * 16, kh * 512 + w * 128, 0);
    const int kvq = (kvh + XO_Q) & 7, idx = kvh * 7 + task - 8, mat = idx >= 28 ? 1 : 0;
    const int cb = isq ? kvq * 8 + task : idx - 28 * mat;
    const int rstride = isq ? 8192 : FF * 2;
    const i32x4 rw = {isq ? KB[0] : mat ? KB[8] : KB[4], isq ? KB[1] : mat ? KB[9] : KB[5], isq ? KB[2] : KB[6], KB[3]};
    const int woff = ((c >> 3) * 32 + g * 8) * rstride + (c & 7) * 16;
    int wso = (kh * 256 + w * 64) * rstride + cb * 128;
    unsigned tg = tstart + GOFF + (w * GTAU) / 4;
#pragma unroll
    for (int j = 0; j < 8; j++) {
      if (!isq) pace(tg, GTAU);
      W[j] = LD128(rw, woff, wso, NTL);
      wso += rstride;
      SB();
    }
    STAMP(1);
    float sm = 0.f;
#pragma unroll
    for (int i = 0; i < 4; i++)
#pragma unroll
      for (int j = 0; j < 4; j++) sm = dot2(xs[i][j], 0x3f803f80u, sm);
    sm += dpp<QX1>(sm); sm += dpp<QX2>(sm);
    const float mean = xrow_sum(sm) * (1.f / DM);
    f32x2 ssv = {0.f, 0.f};
#pragma unroll
    for (int i = 0; i < 4; i++)
#pragma unroll
      for (int j = 0; j < 4; j++) { const f32x2 d = f32x2{lo16(xs[i][j]), hi16(xs[i][j])} - mean; ssv += d * d; }
    float ss = ssv[0] + ssv[1];
    ss += dpp<QX1>(ss); ss += dpp<QX2>(ss);
    const float var = rbf(xrow_sum(ss) * (1.f / DM)), mb = rbf(mean);
    const float den = rbf(__builtin_amdgcn_sqrtf(rbf(var + 1e-5f)));
    const float rden = __builtin_amdgcn_rcpf(den);
#pragma unroll
    for (int i = 0; i < 1; i++)
#pragma unroll
      for (int j = 0; j < 4; j++) {
        const unsigned d = cvtpk(lo16(xf[i][j]) - mb, hi16(xf[i][j]) - mb);
        const f32x2 n = {lo16(d), hi16(d)};
        const f32x2 q = n * rden;
        const f32x2 r = (n - q * den) * rden + q;
        xf[i][j] = cvtpk(r[0], r[1]);
      }
    SB();
    STAMP(2);
    f32x4 acc[8];
#pragma unroll
    for (int e = 0; e < 8; e++) acc[e] = f32x4{0.f, 0.f, 0.f, 0.f};
#pragma unroll
    for (int t = 0; t < 1; t++) {
#pragma unroll
      for (int e = 0; e < 8; e++) {
        u32x4 f;
#pragma unroll
        for (int j = 0; j < 4; j++) f[j] = (e & 1) ? perm_hi(W[t * 8 + 2 * j + 1][e >> 1], W[t * 8 + 2 * j][e >> 1]) : perm_lo(W[t * 8 + 2 * j + 1][e >> 1], W[t * 8 + 2 * j][e >> 1]);
        acc[e] = mfma32(xf[t], f, acc[e]);
      }
      SB();
    }
    f32x4 o, z;
#pragma unroll
    for (int i = 0; i < 4; i++) {
      const float a = acc[i][0] + dpp<ROR8>(acc[i][1]), bq = acc[4 + i][1] + dpp<ROR8>(acc[4 + i][0]);
      o[i] = c < 8 ? a : bq;
    }
    if (meet4(lds, L_PR, F_P, w, lane, o, z)) {
      const unsigned t0 = __builtin_amdgcn_readfirstlane(ep) + 0x01010101u;
      const u32x4 pv = {tagm(z[0], 0xfu, t0), tagm(z[1], 0xfu, t0 >> 8), tagm(z[2], 0xfu, t0 >> 16), tagm(z[3], 0xfu, t0 >> 24)};
      const int col = (c & 7) * 8 + (c >> 3) * 4;
      const int poff = isq ? WS_Q + (((kvq * 4 + g) * 2 + kh) * 512 + task * 64 + col) * 4 : WS_GU + (((mat * 2 + kh) * 4 + g) * FF + cb * 64 + col) * 4;
      if (!(AB & 4)) ST128(pv, rws, poff, 0, QAUX);
    }
    STAMP(3);
  }
  const unsigned T = __builtin_amdgcn_readfirstlane(ep) + 0x01010101u;
  OPQ(pfd);
  LDSV(unsigned char, F_E + w) = 1;
  const int kva = (kvh + XO_A) & 7, hl = gi >> 3, hh = kva * 4 + hl, ng = gi & 7;
  const int bb = c >> 2, q4 = c & 3, sh = g >> 1;
  const int slot0 = (kva * 4 + bb) * 8 + sh * 4;
  if (w >= 2) {
    const int goff = w == 2 ? WS_Q + (kvh * 4 + b) * 4096 + lane * 16 : WS_GU + (g * FF + hh * 56 + (c < 14 ? c : 13) * 4) * 4;
    const int st1 = w == 2 ? 2048 : 4 * FF * 4, st2 = w == 2 ? 1024 : 8 * FF * 4;
    const u32x4 TN = {T & 0xfu, (T >> 8) & 0xfu, (T >> 16) & 0xfu, (T >> 24) & 0xfu};
    u32x4 q[4];
    sleep_until(tstart + (w == 2 ? TQ : TH));
    STAMP(4);
    unsigned spin = 0;
    if (AB & 1) { q[0] = TN; q[1] = TN; q[2] = TN; q[3] = TN; } else
    NOUNROLL for (;; spin++) {
      CBAR();
      q[0] = LD128(rws, goff, 0, QAUX); q[1] = LD128(rws, goff, st1, QAUX); q[2] = LD128(rws, goff, st2, QAUX); q[3] = LD128(rws, goff, st1 + st2, QAUX);
      unsigned bad = 0;
#pragma unroll
      for (int i = 0; i < 4; i++) { q[i] ^= TN; bad |= (q[i][0] | q[i][1]) | (q[i][2] | q[i][3]); }
      if (none((bad & 0xfu) != 0) || spin > SPIN_CAP) break;
      __builtin_amdgcn_s_sleep(8);
    }
    STAMPV(15, spin);
    const f32x4 y0 = F4(q[0]) + F4(q[1]), y1 = F4(q[2]) + F4(q[3]);
    const u32x2 z0 = {cvtpk(y0[0], y0[1]), cvtpk(y0[2], y0[3])}, z1 = {cvtpk(y1[0], y1[1]), cvtpk(y1[2], y1[3])};
    if (w == 2) {
      LDSV(u32x2, L_Q + lane * 8) = z0;
      LDSV(u32x2, L_Q + 512 + lane * 8) = z1;
      lds_wait_eq(lds, F_E, 0x01010101u);
      LDSV(u32x2, F_Q) = u32x2{1u, T};
    } else {
      u32x2 hp;
#pragma unroll
      for (int i = 0; i < 2; i++) hp[i] = c < 14 ? cvtpk(lo16(z0[i]) * lo16(z1[i]), hi16(z0[i]) * hi16(z1[i])) : 0u;
      LDSV(u32x2, L_H + g * 128 + c * 8) = hp;
      LDSV(unsigned char, F_HW) = 1;
    }
    STAMP(5);
  } else if (AB & 8) {
  } else if (w == 0) {
    unsigned lv;
    NOUNROLL for (unsigned spin = 0;; spin++) {
      lv = LDSV(unsigned, L_LW + (lane & 31) * 4);
      if (none(lv == 0u) || spin > (SPIN_CAP << 4)) break;
      __builtin_amdgcn_s_sleep(2);
    }
    float mv = hi16(LDSV(unsigned, L_MW + (lane & 31) * 4));
    float lsum = __uint_as_float(lv);
    lsum += dpp<QX1>(lsum); lsum += dpp<QX2>(lsum); lsum += dpp<HMIR>(lsum);
    mv = fmaxf(mv, dpp<QX1>(mv)); mv = fmaxf(mv, dpp<QX2>(mv)); mv = fmaxf(mv, dpp<HMIR>(mv));
    const u32x4 st = {__float_as_uint(mv), T, __float_as_uint(lsum), T};
    if ((lane & 39) == 0) ST128(st, rws, WS_ST + slot * 128 + (lane >> 3) * 16, 0, SAS);
    STAMP(5);
  } else {
    const int soff = WS_ST + (kva * 32 + (lane & 31)) * 128 + hl * 16;
    u32x4 s0;
    unsigned spin = 0;
    lds_wait8(lds, F_S);
    STAMP(4);
    if (lane < 32) {
      NOUNROLL for (;; spin++) {
        CBAR();
        s0 = LD128(rws, soff, 0, SAL);
        if (none(s0[1] != T || s0[3] != T) || spin > SPIN_CAP) break;
        __builtin_amdgcn_s_sleep(8);
      }
      const float m1 = __uint_as_float(s0[0]);
      float MM = fmaxf(m1, dpp<QX1>(m1));
      MM = fmaxf(MM, dpp<QX2>(MM)); MM = fmaxf(MM, dpp<HMIR>(MM));
      const float e1 = __builtin_amdgcn_exp2f((m1 - MM) * LOG2E);
      float L = e1 * __uint_as_float(s0[2]);
      L += dpp<QX1>(L); L += dpp<QX2>(L); L += dpp<HMIR>(L);
      LDSV(float, L_WT + lane * 4) = e1 * __builtin_amdgcn_rcpf(L);
    }
    LDSV(unsigned char, F_HW + 1) = 1;
    STAMPV(15, spin);
    STAMP(5);
  }
  if (AB & 8) { if (blk == 0 && w == 2) { lds_wait8(lds, F_PV); __builtin_amdgcn_s_sleep(100); if (lane == 0) ST32(T, rws, WS_EPOCH, 0, SC1); } STAMP_FLUSH(); return; }
  int aoff = WS_OA + slot0 * OA_STR + hl * 512 + w * 128 + (g & 1) * 64 + q4 * 16;
  int pboff = WS_PB + (hh * 8 + ng) * PB_STR + lane * 16;
  int wooff = (16 * (g & 1) + 2 * (g >> 1)) * 1024 + c * 8;
  int wdoff = (w == 3 && g >= 2) ? OOB - 4096 : g * 4096 + c * 8;
  const unsigned ta = T & 0xffu, tb = (T >> 16) & 0xffu;
  unsigned mska = 0xffu;
  asm volatile("" : "+v"(aoff), "+v"(pboff), "+v"(wooff), "+v"(wdoff), "+v"(mska));
  const i32x4 rwo = {KB[12], KB[13], KB[14], KB[15]}, rwd = ka[8];
  const int wos = (hh * 128 + w * 32) * 1024 + ng * 128, wds = (hh * 56 + w * 16) * 1024 + ng * 128;
  u32x2 WT[12];
#define WLOAD(j) do { if (AB & 1024) { WT[j] = u32x2{1u, 2u}; OPQ(WT[j]); } else if ((j) < 4) WT[j] = LD64(rwd, wdoff + (j) * 1024, wds, NTL); \
    else WT[j] = LD64(rwo, wooff + ((j) & 1) * 1024, wos + (((j) - 4) >> 1) * 4096, NTL); SB(); } while (0)
  if (NEARLY) {
    unsigned te = now32() + (w * ETAU) / 4;
#pragma unroll
    for (int j = 0; j < NEARLY; j++) { pace(te, ETAU); WLOAD(j); }
  }
  lds_wait8(lds, F_VI);
  const unsigned tvi = __builtin_amdgcn_readfirstlane(LDSV(unsigned, F_TV));
  STAMP(6);
  {
    unsigned tw = tvi + WDEL + (w * WTAU) / 4;
#pragma unroll
    for (int j = NEARLY; j < 12; j++) { if (WTAU || j == NEARLY) pace(tw, WTAU); WLOAD(j); }
  }
  lds_wait_eq(lds, F_HW, 0x0101u);
  const f32x4 wt = LDSV(f32x4, L_WT + bb * 32 + sh * 16);
  const u32x2 hf = LDSV(u32x2, L_H + bb * 128 + w * 32 + g * 8);
  STAMP(8);
  lds_wait8(lds, F_PV);
  if (PRE_A) __builtin_amdgcn_s_sleep(PRE_A);
  { unsigned tA = tvi + TA_ADD; pace(tA, 0); }
  STAMP(9);
  u32x4 a[4];
#pragma unroll
  for (int i = 0; i < 4; i++) a[i] = LD128(rws, aoff, i * OA_STR, SC1);
  SB();
  u32x4 fo[4];
  f32x4 acc[4];
#pragma unroll
  for (int e = 0; e < 4; e++) {
#pragma unroll
    for (int j = 0; j < 4; j++) fo[e][j] = (e & 1) ? perm_hi(WT[4 + 2 * j + 1][e >> 1], WT[4 + 2 * j][e >> 1]) : perm_lo(WT[4 + 2 * j + 1][e >> 1], WT[4 + 2 * j][e >> 1]);
    u32x2 fd;
#pragma unroll
    for (int j = 0; j < 2; j++) fd[j] = (e & 1) ? perm_hi(WT[2 * j + 1][e >> 1], WT[2 * j][e >> 1]) : perm_lo(WT[2 * j + 1][e >> 1], WT[2 * j][e >> 1]);
    acc[e] = mfma16(hf, fd, f32x4{0.f, 0.f, 0.f, 0.f});
  }
  asm volatile("" : "+v"(acc[0]), "+v"(acc[1]), "+v"(acc[2]), "+v"(acc[3]), "+v"(fo[0]), "+v"(fo[1]), "+v"(fo[2]), "+v"(fo[3]));
  STAMP(7);
  {
    unsigned spin = 0;
    NOUNROLL for (;; spin++) {
      unsigned bad = 0;
#pragma unroll
      for (int i = 0; i < 4; i++) { a[i][0] ^= ta; a[i][2] ^= tb; bad |= a[i][0] | a[i][2]; }
      if (none((bad & 0xffu) != 0)) break;
      if (spin > SPIN_CAP) break;
      __builtin_amdgcn_s_sleep(1);
      CBAR();
#pragma unroll
      for (int i = 0; i < 4; i++) a[i] = LD128(rws, aoff, i * OA_STR, SC1);
    }
    STAMPV(13, spin);
  }
  STAMP(10);
  f32x4 t = F4(a[0]) * wt[0];
#pragma unroll
  for (int i = 1; i < 4; i++) t = __builtin_elementwise_fma(F4(a[i]), f32x4{wt[i], wt[i], wt[i], wt[i]}, t);
  auto p0 = __builtin_amdgcn_permlane32_swap(__float_as_uint(t[0]), __float_as_uint(t[2]), false, false);
  auto p1 = __builtin_amdgcn_permlane32_swap(__float_as_uint(t[1]), __float_as_uint(t[3]), false, false);
  const unsigned own = cvtpk(__uint_as_float(p0[0]) + __uint_as_float(p0[1]), __uint_as_float(p1[0]) + __uint_as_float(p1[1]));
  const u32x4 A = {own, dppu<QB1>(own), dppu<QB2>(own), dppu<QB3>(own)};
#pragma unroll
  for (int e = 0; e < 4; e++) acc[e] = mfma32(A, fo[e], acc[e]);
  f32x4 z;
  if (meet4(lds, L_TR, F_T, w, lane, f32x4{acc[0][0], acc[1][0], acc[2][0], acc[3][0]}, z)) {
    const u32x4 o = {tagm(z[0], mska, ta), __float_as_uint(z[1]), tagm(z[2], mska, tb), __float_as_uint(z[3])};
    ST128(o, rws, pboff, 0, SC1);
  }
  STAMP(11);
  STAMP_FLUSH();
}

extern "C" void kernel_launch(void* const* d_in, const int* in_sizes, int n_in, void* d_out, int out_size, void* d_ws, size_t ws_size, hipStream_t stream) {
  const unsigned fl = 0x00020000u;
  fused<<<256, NTHR, 0, stream>>>(d_in[1], 33554432u, fl, d_in[2], 33554432u, fl, (char*)d_ws + WSOFF, (unsigned)WS_END, fl, d_in[0], 4096u, fl,
                                  d_in[3], 4194304u, fl, d_in[6], 1835008u, fl, d_in[7], 1835008u, fl, d_in[4], 4194304u, fl,
                                  d_in[8], 1835008u, fl, d_out, 4096u, fl);
}
```

```cpp
#include <hip/hip_runtime.h>
#include <stdint.h>

#define WIN 8
#define N0 2
#define TOFF 30
#define TAU1 400
#define I1 8
#define TAU 265
#define QKPOS 5
#define SMPOS 8
#define PVPOS 12
#define STPOS 10
#define WOPOS 16
#define GTAU 30
#define WDEL 0
#define NEARLY 0
#define ETAU 20
#define QGPOS 12
#define HGPOS 22
#define TLBW 0
#define REBASE 55
#define EARLYT 1
#define W0 2
#define DEL 40
#define GOFF 40
#define WTAU 5
#define TQ 400
#define TH 600
#define TA_ADD 120
#define PRE_A 4
#define PRE_B 8
#define CODE_BYTES 10752
#define QAUX 18
#define QAL QAUX
#define SAS 16
#define SAL 16
#define XO_Q 1
#define XO_A 4
#define AB 0

typedef unsigned short u16;
typedef int i32x4 __attribute__((ext_vector_type(4)));
typedef int i32x16 __attribute__((ext_vector_type(16)));
typedef unsigned u32x2 __attribute__((ext_vector_type(2)));
typedef unsigned u32x4 __attribute__((ext_vector_type(4)));
typedef float f32x2 __attribute__((ext_vector_type(2)));
typedef float f32x4 __attribute__((ext_vector_type(4)));
typedef short s16x4 __attribute__((ext_vector_type(4)));
typedef short s16x8 __attribute__((ext_vector_type(8)));
typedef __bf16 bf16x2 __attribute__((ext_vector_type(2)));
#define LAS __attribute__((address_space(3)))

__device__ u16 LD16(i32x4 r, int v, int s, int a) __asm("llvm.amdgcn.raw.buffer.load.i16");
__device__ unsigned LD32(i32x4 r, int v, int s, int a) __asm("llvm.amdgcn.raw.buffer.load.i32");
__device__ u32x2 LD64(i32x4 r, int v, int s, int a) __asm("llvm.amdgcn.raw.buffer.load.v2i32");
__device__ u32x4 LD128(i32x4 r, int v, int s, int a) __asm("llvm.amdgcn.raw.buffer.load.v4i32");
__device__ void ST16(u16 d, i32x4 r, int v, int s, int a) __asm("llvm.amdgcn.raw.buffer.store.i16");
__device__ void ST32(unsigned d, i32x4 r, int v, int s, int a) __asm("llvm.amdgcn.raw.buffer.store.i32");
__device__ void ST128(u32x4 d, i32x4 r, int v, int s, int a) __asm("llvm.amdgcn.raw.buffer.store.v4i32");
constexpr int NTL = 2, SC1 = 16;

constexpr int DM = 512, FF = 1792;
constexpr int NT = 4, NTHR = (NT + 8) * 64;
constexpr int OA_STR = 2176, PB_STR = 1152;
constexpr int WS_EPOCH = 0;
constexpr int WS_Q = 4096;
constexpr int WS_GU = WS_Q + 131072;
constexpr int WS_ST = WS_GU + 114688;
constexpr int WS_OA = WS_ST + 32768;
constexpr int WS_PB = WS_OA + 256 * OA_STR;
constexpr int WS_END = WS_PB + 256 * PB_STR;
constexpr int WS_TS = 4 << 20;
constexpr int L_RED = 0;
constexpr int L_TR = L_RED + 16384;
constexpr int L_PR = L_TR + 4096;
constexpr int L_Q = L_PR + 4096;
constexpr int L_H = L_Q + 1024;
constexpr int L_WT = L_H + 512;
constexpr int L_MW = L_WT + 128;
constexpr int L_LW = L_MW + 128;
constexpr int L_FLAG = L_LW + 128;
constexpr int F_Q = L_FLAG, F_HW = L_FLAG + 8, F_E = L_FLAG + 12, F_VI = L_FLAG + 16, F_PV = L_FLAG + 24, F_T = L_FLAG + 32, F_P = L_FLAG + 36, F_S = L_FLAG + 40, F_TV = L_FLAG + 48, F_W = L_FLAG + 52, F_G = L_FLAG + 56;
constexpr int L_END = L_FLAG + 64;
constexpr unsigned SPIN_CAP = 1u << 18;
constexpr int OOB = 0x7ffffff0;
constexpr float LOG2E = 1.4426950408889634f;
constexpr int sched(int i) { return TOFF + ((i < I1 ? i - N0 + 1 : I1 - N0) * TAU1 + (i < I1 ? 0 : i - I1 + 1) * TAU) / 10; }

__device__ __forceinline__ float lo16(unsigned u) { return __uint_as_float(u << 16); }
__device__ __forceinline__ float hi16(unsigned u) { return __uint_as_float(u & 0xffff0000u); }
__device__ __forceinline__ unsigned cvtpk(float lo, float hi) { f32x2 f = {lo, hi}; return __builtin_bit_cast(unsigned, __builtin_convertvector(f, bf16x2)); }
__device__ __forceinline__ float rbf(float f) { return lo16(cvtpk(f, f)); }
__device__ __forceinline__ f32x4 mfma32(u32x4 a, u32x4 b, f32x4 c) { return __builtin_amdgcn_mfma_f32_16x16x32_bf16(__builtin_bit_cast(s16x8, a), __builtin_bit_cast(s16x8, b), c, 0, 0, 0); }
__device__ __forceinline__ f32x4 mfma16(u32x2 a, u32x2 b, f32x4 c) { return __builtin_amdgcn_mfma_f32_16x16x16bf16_1k(__builtin_bit_cast(s16x4, a), __builtin_bit_cast(s16x4, b), c, 0, 0, 0); }
template <int CTRL> __device__ __forceinline__ float dpp(float x) { return __builtin_bit_cast(float, __builtin_amdgcn_mov_dpp(__builtin_bit_cast(int, x), CTRL, 0xf, 0xf, true)); }
template <int CTRL> __device__ __forceinline__ unsigned dppu(unsigned x) { return (unsigned)__builtin_amdgcn_mov_dpp((int)x, CTRL, 0xf, 0xf, true); }
constexpr int ROR4 = 0x124, ROR8 = 0x128, HMIR = 0x141, QX1 = 0xB1, QX2 = 0x4E, QB0 = 0x00, QB1 = 0x55, QB2 = 0xAA, QB3 = 0xFF;
__device__ __forceinline__ float xrow_max(float x) {
  auto s = __builtin_amdgcn_permlane16_swap(__float_as_uint(x), __float_as_uint(x), false, false);
  x = fmaxf(__uint_as_float(s[0]), __uint_as_float(s[1]));
  auto t = __builtin_amdgcn_permlane32_swap(__float_as_uint(x), __float_as_uint(x), false, false);
  return fmaxf(__uint_as_float(t[0]), __uint_as_float(t[1]));
}
__device__ __forceinline__ float xrow_sum(float x) {
  auto s = __builtin_amdgcn_permlane16_swap(__float_as_uint(x), __float_as_uint(x), false, false);
  x = __uint_as_float(s[0]) + __uint_as_float(s[1]);
  auto t = __builtin_amdgcn_permlane32_swap(__float_as_uint(x), __float_as_uint(x), false, false);
  return __uint_as_float(t[0]) + __uint_as_float(t[1]);
}
__device__ __forceinline__ float half_sum(float x) {
  auto t = __builtin_amdgcn_permlane32_swap(__float_as_uint(x), __float_as_uint(x), false, false);
  return __uint_as_float(t[0]) + __uint_as_float(t[1]);
}
__device__ __forceinline__ float dot2(unsigned a, unsigned b, float c) { return __builtin_amdgcn_fdot2_f32_bf16(__builtin_bit_cast(bf16x2, a), __builtin_bit_cast(bf16x2, b), c, false); }
__device__ __forceinline__ unsigned tagm(float v, unsigned m, unsigned t) { return (__float_as_uint(v) & ~m) | (t & m); }
__device__ __forceinline__ unsigned perm_lo(unsigned hi, unsigned lo) { return __builtin_amdgcn_perm(hi, lo, 0x05040100u); }
__device__ __forceinline__ unsigned perm_hi(unsigned hi, unsigned lo) { return __builtin_amdgcn_perm(hi, lo, 0x07060302u); }
__device__ __forceinline__ bool none(bool p) { return __builtin_amdgcn_ballot_w64(p) == 0ull; }
#define F4(v) __builtin_bit_cast(f32x4, v)
#define SB() __builtin_amdgcn_sched_barrier(0)
#define CBAR() asm volatile("" ::: "memory")
#define OPQ(x) asm volatile("" : "+v"(x))
#define LDSV(T, off) (*(volatile LAS T*)(lds + (off)))
#define NOUNROLL _Pragma("clang loop unroll(disable)")

__device__ __forceinline__ unsigned now32() { unsigned long long t; asm volatile("s_memrealtime %0\n\ts_waitcnt lgkmcnt(0)" : "=s"(t)::"memory"); return (unsigned)t; }
__device__ __forceinline__ void pace(unsigned& tgt, int step) {
  asm volatile("1:\n\ts_memrealtime vcc\n\ts_waitcnt lgkmcnt(0)\n\ts_sub_u32 vcc_lo, vcc_lo, %0\n\ts_cmp_lt_i32 vcc_lo, 0\n\ts_cbranch_scc1 1b\n\ts_add_u32 %0, %0, %1"
               : "+s"(tgt) : "n"(step) : "memory", "scc", "vcc");
}
__device__ __forceinline__ void apace(unsigned& tgt, int step) {
  unsigned tmp;
  asm volatile("1:\n\ts_memrealtime vcc\n\ts_getreg_b32 %1, hwreg(HW_REG_IB_STS, 0, 4)\n\ts_sub_i32 %1, %1, %3\n\ts_max_i32 %1, %1, 0\n\ts_mul_i32 %1, %1, %4\n\t"
               "s_waitcnt lgkmcnt(0)\n\ts_sub_u32 vcc_lo, vcc_lo, %0\n\ts_cmp_lt_i32 vcc_lo, %1\n\ts_cbranch_scc1 1b\n\ts_add_u32 %0, %0, %2"
               : "+s"(tgt), "=&s"(tmp) : "n"(step), "n"(W0 - 1), "n"(DEL) : "memory", "scc", "vcc");
}
__device__ __forceinline__ void sleep_until(unsigned tgt) {
  asm volatile("1:\n\ts_sleep 1\n\ts_memrealtime vcc\n\ts_waitcnt lgkmcnt(0)\n\ts_sub_u32 vcc_lo, vcc_lo, %0\n\ts_cmp_lt_i32 vcc_lo, 0\n\ts_cbranch_scc1 1b"
               :: "s"(tgt) : "memory", "scc", "vcc");
}
#define STAMP(i)
#define STAMPV(i, val)
#define STAMP_FLUSH()

__device__ __forceinline__ void lds_wait_eq(LAS unsigned char* lds, int off, unsigned n) {
  NOUNROLL for (unsigned spin = 0; LDSV(unsigned, off) != n && spin < (SPIN_CAP << 4); spin++) __builtin_amdgcn_s_sleep(1);
  CBAR();
}
__device__ __forceinline__ void lds_wait8(LAS unsigned char* lds, int off) {
  NOUNROLL for (unsigned spin = 0; spin < (SPIN_CAP << 4); spin++) {
    const u32x2 f = LDSV(u32x2, off);
    if ((f[0] & f[1]) == 0x01010101u) break;
    __builtin_amdgcn_s_sleep(1);
  }
  CBAR();
}
__device__ __forceinline__ bool meet4(LAS unsigned char* lds, int area, int cnt, int w, int lane, f32x4 v, f32x4& sum) {
  LDSV(f32x4, area + w * 1024 + lane * 16) = v;
  unsigned prev;
  f32x4 r0, r1, r2, r3;
  const unsigned la = (unsigned)(uintptr_t)(lds + area) + lane * 16, lf = (unsigned)(uintptr_t)(lds + cnt), one = 1u;
  asm volatile("s_mov_b64 exec, 1\n\tds_add_rtn_u32 %0, %6, %7\n\ts_mov_b64 exec, -1\n\t"
               "ds_read_b128 %1, %5\n\tds_read_b128 %2, %5 offset:1024\n\tds_read_b128 %3, %5 offset:2048\n\tds_read_b128 %4, %5 offset:3072\n\t"
               "s_waitcnt lgkmcnt(0)"
               : "=&v"(prev), "=&v"(r0), "=&v"(r1), "=&v"(r2), "=&v"(r3) : "v"(la), "v"(lf), "v"(one) : "memory");
  sum = (r0 + r1) + (r2 + r3);
  return __builtin_amdgcn_readfirstlane(prev) == 3u;
}

#define DESC(n) const void* p_##n, unsigned n##_bytes, unsigned n##_flags
__global__ void __launch_bounds__(NTHR, 3) fused(DESC(k), DESC(v), DESC(ws), DESC(x), DESC(wq), DESC(wg), DESC(wu), DESC(wo), DESC(wd), DESC(out)) {
  __shared__ __attribute__((aligned(16))) unsigned char smem[L_END];
  auto ka = (const __attribute__((address_space(4))) i32x4*)__builtin_amdgcn_kernarg_segment_ptr();
  i32x16 KA, KB;
  unsigned long long pc0;
  asm volatile("s_load_dwordx16 %0, %3, 0x0\n\ts_load_dwordx16 %1, %3, 0x40\n\ts_getpc_b64 %2" : "=&s"(KA), "=&s"(KB), "=&s"(pc0) : "s"(ka));
  SB();
  LAS unsigned char* lds = (LAS unsigned char*)smem;
  const int blk = blockIdx.x, kvh = blk & 7, gi = blk >> 3, b = gi & 3, s = gi >> 2;
  const int tid = threadIdx.x, lane = tid & 63, w = __builtin_amdgcn_readfirstlane(tid >> 6), g = lane >> 4, c = lane & 15;
  u32x2 pfd = {0u, 0u};
  if (w == 3 && lane < CODE_BYTES / 256) {
    const int off = gi * (CODE_BYTES / 32) + lane * 8;
    asm volatile("global_load_dwordx2 %0, %1, %2" : "=v"(pfd) : "v"(off), "s"(pc0 & ~0xffull));
  }
  if (w == 2) {
    LDSV(unsigned, L_MW + lane * 4) = 0u;
    LDSV(unsigned, L_FLAG + c * 4) = 0u;
  }
  const int kv = w - NT;
  int koff = (c >> 2) * 256 + g * 64 + (c & 3) * 16;
  int kvbase = ((b * 8 + kvh) * 4096 + s * 512) * 256 + kv * 4096;
  int so0 = kvbase + (gi & 3) * 1024;
  OPQ(koff);
  asm volatile("s_waitcnt lgkmcnt(0)\n\ts_barrier" : "+s"(KA), "+s"(KB), "+s"(kvbase), "+s"(so0)::"memory");
  const i32x4 rws = {KA[8], KA[9], KA[10], KA[11]};
  if (TLBW & 2) {
    if (w == 1) {
      unsigned d0, d1, d2, d3, d4, d5;
      asm volatile("s_buffer_load_dword %0, %6, 0x0\n\ts_buffer_load_dword %1, %7, 0x0\n\ts_buffer_load_dword %2, %8, %12\n\ts_buffer_load_dword %3, %9, %13\n\t"
                   "s_buffer_load_dword %4, %10, %13\n\ts_buffer_load_dword %5, %11, %14\n\ts_waitcnt lgkmcnt(0)"
                   : "=&s"(d0), "=&s"(d1), "=&s"(d2), "=&s"(d3), "=&s"(d4), "=&s"(d5)
                   : "s"(rws), "s"(i32x4{KA[12], KA[13], KA[14], KA[15]}), "s"(i32x4{KB[0], KB[1], KB[2], KB[3]}), "s"(i32x4{KB[4], KB[5], KB[6], KB[7]}),
                     "s"(i32x4{KB[8], KB[9], KB[10], KB[11]}), "s"(i32x4{KA[4], KA[5], KA[6], KA[7]}), "s"(gi * 131072), "s"(gi * 57344), "s"(kvbase + 3 * 4096));
    }
  }
  const int slot = (kvh * 4 + b) * 8 + s;
  if (__builtin_expect(w >= NT, 1)) {
    const i32x4 rk = {KA[0], KA[1], KA[2], KA[3]}, rv = {KA[4], KA[5], KA[6], KA[7]};
    u32x4 K[16], V[16];
    const int voff = lane * 16;
#define SOFF(i) (((i) < 4 ? so0 : kvbase + ((((i) >> 2) + gi) & 3) * 1024) + ((i) & 3) * 32768)
#define WAITN(n) asm volatile("s_waitcnt vmcnt(%0)" ::"n"((n) - 1) : "memory")
#define PACE(i) do { if (DEL) apace(tgt, sched((i) + 1) - sched(i)); else pace(tgt, sched((i) + 1) - sched(i)); } while (0)
#define VLOAD(i) do { WAITN(WIN); PACE(16 + (i)); V[i] = LD128(rv, voff, SOFF(i), NTL); \
    if ((i) + 17 == HGPOS && kv == 7) LDSV(unsigned char, F_G + 1) = 1; if ((i) + 1 == WOPOS) { LDSV(unsigned, F_TV) = now32(); LDSV(unsigned char, F_VI + kv) = 1; } SB(); } while (0)
    unsigned long long t64;
    if (EARLYT) asm volatile("s_memrealtime %0" : "=s"(t64));
#pragma unroll
    for (int i = 0; i < N0; i++) { K[i] = LD128(rk, koff, SOFF(i), NTL); SB(); }
    if (EARLYT) asm volatile("s_waitcnt lgkmcnt(0)" : "+s"(t64));
    unsigned tgt = (EARLYT ? (unsigned)t64 : now32()) + sched(N0) + (kv * TAU) / 80;
    if (REBASE) { WAITN(N0); const unsigned t1 = now32() + REBASE - 100 + (kv * TAU) / 80; if ((int)(t1 - tgt) > 0) tgt = t1; }
#pragma unroll
    for (int i = N0; i < 16; i++) {
      if (i >= WIN) WAITN(WIN);
      PACE(i);
      K[i] = LD128(rk, koff, SOFF(i), NTL);
      if (i + 1 == QGPOS && kv == 7) LDSV(unsigned char, F_G) = 1;
      SB();
    }
    STAMP(1);
#pragma unroll
    for (int i = 0; i < QKPOS; i++) VLOAD(i);
    STAMP(2);
    u32x2 fl = LDSV(u32x2, F_Q);
    NOUNROLL for (unsigned spin = 0; fl[0] == 0u && spin < (SPIN_CAP << 4); spin++) { __builtin_amdgcn_s_sleep(1); fl = LDSV(u32x2, F_Q); }
    CBAR();
    const unsigned T = __builtin_amdgcn_readfirstlane(fl[1]);
    const unsigned ta = T & 0xffu, tb = (T >> 16) & 0xffu;
    STAMP(3);
    float S[4];
    {
      const u32x4 qf = LDSV(u32x4, L_Q + koff);
      const bool a1 = c & 1, a2 = c & 2;
#pragma unroll
      for (int t = 0; t < 4; t++) {
        float y[4];
#pragma unroll
        for (int i = 0; i < 4; i++) {
          const f32x4 d = mfma32(K[t * 4 + i], qf, f32x4{0.f, 0.f, 0.f, 0.f});
          float z = dpp<QB0>(d[0]);
          z += dpp<QB1>(d[1]); OPQ(z);
          z += dpp<QB2>(d[2]); OPQ(z);
          z += dpp<QB3>(d[3]); OPQ(z);
          y[i] = z;
        }
        const float u0 = a1 ? y[1] : y[0], u1 = a1 ? y[3] : y[2];
        S[t] = a2 ? u1 : u0;
      }
#pragma unroll
      for (int i = 0; i < 4; i += 2) {
        const unsigned p = cvtpk(S[i], S[i + 1]);
        const unsigned p2 = cvtpk(lo16(p) * 0.08838834764831845f, hi16(p) * 0.08838834764831845f);
        S[i] = lo16(p2); S[i + 1] = hi16(p2);
      }
      float mw = fmaxf(fmaxf(S[0], S[1]), fmaxf(S[2], S[3]));
      mw = fmaxf(mw, dpp<QX1>(mw)); mw = fmaxf(mw, dpp<QX2>(mw));
      mw = xrow_max(mw);
      LDSV(unsigned, L_MW + (c >> 2) * 32 + kv * 4) = __float_as_uint(mw) | 1u;
    }
    STAMP(4);
    SB();
#pragma unroll
    for (int i = QKPOS; i < SMPOS; i++) VLOAD(i);
    STAMP(5);
    u32x4 P[2];
    {
      u32x4 m0, m1;
      NOUNROLL for (unsigned spin = 0;; spin++) {
        m0 = LDSV(u32x4, L_MW + (c >> 2) * 32); m1 = LDSV(u32x4, L_MW + (c >> 2) * 32 + 16);
        const u32x4 mm = m0 & m1;
        if (none((mm[0] & mm[1] & mm[2] & mm[3] & 1u) == 0u) || spin > (SPIN_CAP << 4)) break;
        __builtin_amdgcn_s_sleep(1);
      }
      float M = -INFINITY;
#pragma unroll
      for (int i = 0; i < 4; i++) M = fmaxf(M, fmaxf(hi16(m0[i]), hi16(m1[i])));
      const float mneg = -M * LOG2E;
      float p[4];
#pragma unroll
      for (int i = 0; i < 4; i++) p[i] = __builtin_amdgcn_exp2f(fmaf(S[i], LOG2E, mneg));
      float lsum = (p[0] + p[1]) + (p[2] + p[3]);
      lsum += dpp<QX1>(lsum); lsum += dpp<QX2>(lsum);
      lsum = xrow_sum(lsum);
      LDSV(unsigned, L_LW + (c >> 2) * 32 + kv * 4) = __float_as_uint(lsum) | 1u;
#pragma unroll
      for (int u = 0; u < 2; u++) { const unsigned pk = cvtpk(p[2 * u], p[2 * u + 1]); P[u] = u32x4{pk, dppu<QB1>(pk), dppu<QB2>(pk), dppu<QB3>(pk)}; }
    }
    int oaoff = WS_OA + slot * OA_STR + kv * 512 + (lane & 31) * 16;
    unsigned mska = 0xffu;
    asm volatile("" : "+v"(oaoff), "+v"(mska), "+v"(P[0]), "+v"(P[1]));
    STAMP(6);
    SB();
#pragma unroll
    for (int i = SMPOS; i < PVPOS; i++) VLOAD(i);
    f32x4 O[8];
#pragma unroll
    for (int i = 0; i < 4; i++) {
      u32x4 f0, f1;
#pragma unroll
      for (int j = 0; j < 4; j++) { f0[j] = perm_lo(V[4 + j][i], V[j][i]); f1[j] = perm_hi(V[4 + j][i], V[j][i]); }
      O[2 * i] = mfma32(P[0], f0, f32x4{0.f, 0.f, 0.f, 0.f}); O[2 * i + 1] = mfma32(P[0], f1, f32x4{0.f, 0.f, 0.f, 0.f});
    }
    asm volatile("" : "+v"(O[0]), "+v"(O[1]), "+v"(O[2]), "+v"(O[3]), "+v"(O[4]), "+v"(O[5]), "+v"(O[6]), "+v"(O[7]));
    SB();
#pragma unroll
    for (int i = PVPOS; i < 16; i++) VLOAD(i);
    STAMP(11);
    {
      u32x4 F[8];
#pragma unroll
      for (int j = 0; j < 3; j++) {
#pragma unroll
        for (int i = 0; i < 4; i++) { F[2 * i][j] = perm_lo(V[12 + j][i], V[8 + j][i]); F[2 * i + 1][j] = perm_hi(V[12 + j][i], V[8 + j][i]); }
        SB();
      }
      STAMP(7);
#pragma unroll
      for (int i = 0; i < 4; i++) {
        F[2 * i][3] = perm_lo(V[15][i], V[11][i]); F[2 * i + 1][3] = perm_hi(V[15][i], V[11][i]);
        O[2 * i] = mfma32(P[1], F[2 * i], O[2 * i]); O[2 * i + 1] = mfma32(P[1], F[2 * i + 1], O[2 * i + 1]);
      }
    }
    LDSV(f32x4, L_RED + kv * 2048 + lane * 32) = f32x4{O[0][0], O[1][0], O[2][0], O[3][0]};
    LDSV(f32x4, L_RED + kv * 2048 + lane * 32 + 16) = f32x4{O[4][0], O[5][0], O[6][0], O[7][0]};
    LDSV(unsigned char, F_PV + kv) = 1;
    STAMP(8);
    if (kv < 4) {
      const int half = lane >> 5, l5 = lane & 31;
      f32x4 r[4];
      NOUNROLL for (unsigned spin = 0;; spin++) {
        const u32x2 cnt = LDSV(u32x2, F_PV);
#pragma unroll
        for (int i = 0; i < 4; i++) r[i] = LDSV(f32x4, L_RED + (half * 4 + i) * 2048 + kv * 512 + l5 * 16);
        if ((cnt[0] & cnt[1]) == 0x01010101u) break;
        if (spin > (SPIN_CAP << 4)) break;
      }
      STAMP(9);
      const f32x4 a = (r[0] + r[1]) + (r[2] + r[3]);
      const u32x4 o = {tagm(half_sum(a[0]), mska, ta), __float_as_uint(half_sum(a[1])), tagm(half_sum(a[2]), mska, tb), __float_as_uint(half_sum(a[3]))};
      if (half == 0) ST128(o, rws, oaoff, 0, SC1);
      STAMP(10);
    } else if (kv == 4 && !(AB & 8)) {
      const int oi = (gi >> 3) * 512 + kvh * 64 + (gi & 7) * 8 + (c & 1) * 4 + g;
      const i32x4 rx = {KA[12], KA[13], KA[14], KA[15]}, ro = ka[9];
      float xfin = lo16(LD16(rx, oi * 2, 0, 0));
      int goff = WS_PB + ((lane >> 1) * 8 + kvh) * PB_STR + gi * 32 + (lane & 1) * 16;
      int ooff = c < 2 ? oi * 2 : OOB;
      asm volatile("" : "+v"(xfin), "+v"(goff), "+v"(ooff));
      lds_wait_eq(lds, F_T, 4u);
      if (PRE_B) __builtin_amdgcn_s_sleep(PRE_B);
      STAMP(10);
      u32x4 pb;
      unsigned spin = 0;
      NOUNROLL for (;; spin++) {
        CBAR();
        pb = LD128(rws, goff, 0, SC1);
        pb[0] ^= ta; pb[2] ^= tb;
        if (none(((pb[0] | pb[2]) & 0xffu) != 0)) break;
        if (spin > SPIN_CAP) break;
        __builtin_amdgcn_s_sleep(1);
      }
      STAMPV(15, spin);
      STAMP(9);
      auto s0 = __builtin_amdgcn_permlane32_swap(pb[0], pb[2], false, false);
      auto s1 = __builtin_amdgcn_permlane32_swap(pb[1], pb[3], false, false);
      const float y0 = __uint_as_float(s0[0]) + __uint_as_float(s0[1]), y1 = __uint_as_float(s1[0]) + __uint_as_float(s1[1]);
      auto s2 = __builtin_amdgcn_permlane16_swap(__float_as_uint(y0), __float_as_uint(y1), false, false);
      float z = __uint_as_float(s2[0]) + __uint_as_float(s2[1]);
      z += dpp<ROR8>(z); z += dpp<ROR4>(z); z += dpp<QX2>(z);
      z += xfin;
      ST16((u16)cvtpk(z, z), ro, ooff, 0, SC1);
      if (blk == 0 && lane == 0) ST32(T, rws, WS_EPOCH, 0, SC1);
      STAMP(12);
    } else if (kv == 5 && !(AB & 8)) {
      const int soff = WS_ST + (((kvh + XO_A) & 7) * 32 + (lane & 31)) * 128 + (gi >> 3) * 16;
      u32x4 s0;
      unsigned spin = 0;
      if (lane < 32) {
        NOUNROLL for (;; spin++) {
          CBAR();
          s0 = LD128(rws, soff, 0, SAL);
          if (none(s0[1] != T || s0[3] != T) || spin > SPIN_CAP) break;
          __builtin_amdgcn_s_sleep(4);
        }
        const float m1 = __uint_as_float(s0[0]);
        float MM = fmaxf(m1, dpp<QX1>(m1));
        MM = fmaxf(MM, dpp<QX2>(MM)); MM = fmaxf(MM, dpp<HMIR>(MM));
        const float e1 = __builtin_amdgcn_exp2f((m1 - MM) * LOG2E);
        float L = e1 * __uint_as_float(s0[2]);
        L += dpp<QX1>(L); L += dpp<QX2>(L); L += dpp<HMIR>(L);
        LDSV(float, L_WT + lane * 4) = e1 * __builtin_amdgcn_rcpf(L);
      }
      LDSV(unsigned, F_W) = 1u;
      STAMPV(15, spin);
      STAMP(10);
    }
    STAMP_FLUSH();
    return;
  }
  __builtin_amdgcn_s_setprio(3);
  const unsigned ep = LD32(rws, WS_EPOCH, 0, SC1);
  const int kva = (kvh + XO_A) & 7, hl = gi >> 3, hh = kva * 4 + hl, ng = gi & 7;
  const i32x4 rwo = {KB[12], KB[13], KB[14], KB[15]}, rwd = ka[8];
  const int wos = (hh * 128 + w * 32) * 1024 + ng * 128, wds = (hh * 56 + w * 16) * 1024 + ng * 128;
  const int task = gi >> 1, kh = gi & 1;
  const unsigned tstart = (unsigned)__builtin_amdgcn_s_memrealtime();
  if (task < 15) {
    const bool isq = task < 8;
    const i32x4 rx = {KA[12], KA[13], KA[14], KA[15]};
    u32x4 xs[4], xf[1], W[8];
#pragma unroll
    for (int i = 0; i < 4; i++) xs[i] = LD128(rx, (c >> 2) * 1024 + g * 64 + (c & 3) * 16 + i * 256, 0, 0);
    xf[0] = LD128(rx, (c >> 2) * 1024 + (c & 1) * 64 + g * 16, kh * 512 + w * 128, 0);
    const int kvq = (kvh + XO_Q) & 7, idx = kvh * 7 + task - 8, mat = idx >= 28 ? 1 : 0;
    const int cb = isq ? kvq * 8 + task : idx - 28 * mat;
    const int rstride = isq ? 8192 : FF * 2;
    const i32x4 rw = {isq ? KB[0] : mat ? KB[8] : KB[4], isq ? KB[1] : mat ? KB[9] : KB[5], isq ? KB[2] : KB[6], KB[3]};
    const int woff = ((c >> 3) * 32 + g * 8) * rstride + (c & 7) * 16;
    int wso = (kh * 256 + w * 64) * rstride + cb * 128;
    unsigned tg = tstart + GOFF + (w * GTAU) / 4;
#pragma unroll
    for (int j = 0; j < 8; j++) {
      if (!isq) pace(tg, GTAU);
      W[j] = LD128(rw, woff, wso, NTL);
      wso += rstride;
      SB();
    }
    STAMP(1);
    float sm = 0.f;
#pragma unroll
    for (int i = 0; i < 4; i++)
#pragma unroll
      for (int j = 0; j < 4; j++) sm = dot2(xs[i][j], 0x3f803f80u, sm);
    sm += dpp<QX1>(sm); sm += dpp<QX2>(sm);
    const float mean = xrow_sum(sm) * (1.f / DM);
    f32x2 ssv = {0.f, 0.f};
#pragma unroll
    for (int i = 0; i < 4; i++)
#pragma unroll
      for (int j = 0; j < 4; j++) { const f32x2 d = f32x2{lo16(xs[i][j]), hi16(xs[i][j])} - mean; ssv += d * d; }
    float ss = ssv[0] + ssv[1];
    ss += dpp<QX1>(ss); ss += dpp<QX2>(ss);
    const float var = rbf(xrow_sum(ss) * (1.f / DM)), mb = rbf(mean);
    const float den = rbf(__builtin_amdgcn_sqrtf(rbf(var + 1e-5f)));
    const float rden = __builtin_amdgcn_rcpf(den);
#pragma unroll
    for (int i = 0; i < 1; i++)
#pragma unroll
      for (int j = 0; j < 4; j++) {
        const unsigned d = cvtpk(lo16(xf[i][j]) - mb, hi16(xf[i][j]) - mb);
        const f32x2 n = {lo16(d), hi16(d)};
        const f32x2 q = n * rden;
        const f32x2 r = (n - q * den) * rden + q;
        xf[i][j] = cvtpk(r[0], r[1]);
      }
    SB();
    STAMP(2);
    f32x4 acc[8];
#pragma unroll
    for (int e = 0; e < 8; e++) acc[e] = f32x4{0.f, 0.f, 0.f, 0.f};
#pragma unroll
    for (int t = 0; t < 1; t++) {
#pragma unroll
      for (int e = 0; e < 8; e++) {
        u32x4 f;
#pragma unroll
        for (int j = 0; j < 4; j++) f[j] = (e & 1) ? perm_hi(W[t * 8 + 2 * j + 1][e >> 1], W[t * 8 + 2 * j][e >> 1]) : perm_lo(W[t * 8 + 2 * j + 1][e >> 1], W[t * 8 + 2 * j][e >> 1]);
        acc[e] = mfma32(xf[t], f, acc[e]);
      }
      SB();
    }
    f32x4 o, z;
#pragma unroll
    for (int i = 0; i < 4; i++) {
      const float a = acc[i][0] + dpp<ROR8>(acc[i][1]), bq = acc[4 + i][1] + dpp<ROR8>(acc[4 + i][0]);
      o[i] = c < 8 ? a : bq;
    }
    if (meet4(lds, L_PR, F_P, w, lane, o, z)) {
      const unsigned t0 = __builtin_amdgcn_readfirstlane(ep) + 0x01010101u;
      const u32x4 pv = {tagm(z[0], 0xfu, t0), tagm(z[1], 0xfu, t0 >> 8), tagm(z[2], 0xfu, t0 >> 16), tagm(z[3], 0xfu, t0 >> 24)};
      const int col = (c & 7) * 8 + (c >> 3) * 4;
      const int poff = isq ? WS_Q + (((kvq * 4 + g) * 2 + kh) * 512 + task * 64 + col) * 4 : WS_GU + (((mat * 2 + kh) * 4 + g) * FF + cb * 64 + col) * 4;
      if (!(AB & 4)) ST128(pv, rws, poff, 0, QAUX);
    }
    STAMP(3);
  }
  const unsigned T = __builtin_amdgcn_readfirstlane(ep) + 0x01010101u;
  OPQ(pfd);
  LDSV(unsigned char, F_E + w) = 1;
  if ((TLBW & 1) && w == 1) {
    const i32x4 rv = {KA[4], KA[5], KA[6], KA[7]}, ro = ka[9];
    unsigned d0, d1, d2, d3;
    asm volatile("s_buffer_load_dword %0, %4, %8\n\ts_buffer_load_dword %1, %5, %9\n\ts_buffer_load_dword %2, %6, %10\n\ts_buffer_load_dword %3, %7, 0x0\n\ts_waitcnt lgkmcnt(0)"
                 : "=&s"(d0), "=&s"(d1), "=&s"(d2), "=&s"(d3) : "s"(rv), "s"(rwo), "s"(rwd), "s"(ro), "s"(kvbase + 3 * 4096), "s"(wos), "s"(wds));
  }
  const int bb = c >> 2, q4 = c & 3, sh = g >> 1;
  const int slot0 = (kva * 4 + bb) * 8 + sh * 4;
  if (w >= 2) {
    const int goff = w == 2 ? WS_Q + (kvh * 4 + b) * 4096 + lane * 16 : WS_GU + (g * FF + hh * 56 + (c < 14 ? c : 13) * 4) * 4;
    const int st1 = w == 2 ? 2048 : 4 * FF * 4, st2 = w == 2 ? 1024 : 8 * FF * 4;
    const u32x4 TN = {T & 0xfu, (T >> 8) & 0xfu, (T >> 16) & 0xfu, (T >> 24) & 0xfu};
    u32x4 q[4];
    NOUNROLL for (unsigned sp = 0; LDSV(unsigned char, F_G + w - 2) == 0 && sp < (SPIN_CAP << 4); sp++) __builtin_amdgcn_s_sleep(2);
    STAMP(4);
    unsigned spin = 0;
    if (AB & 1) { q[0] = TN; q[1] = TN; q[2] = TN; q[3] = TN; } else
    NOUNROLL for (;; spin++) {
      CBAR();
      q[0] = LD128(rws, goff, 0, QAL); q[1] = LD128(rws, goff, st1, QAL); q[2] = LD128(rws, goff, st2, QAL); q[3] = LD128(rws, goff, st1 + st2, QAL);
      unsigned bad = 0;
#pragma unroll
      for (int i = 0; i < 4; i++) { q[i] ^= TN; bad |= (q[i][0] | q[i][1]) | (q[i][2] | q[i][3]); }
      if (none((bad & 0xfu) != 0) || spin > SPIN_CAP) break;
      __builtin_amdgcn_s_sleep(8);
    }
    STAMPV(15, spin);
    const f32x4 y0 = F4(q[0]) + F4(q[1]), y1 = F4(q[2]) + F4(q[3]);
    const u32x2 z0 = {cvtpk(y0[0], y0[1]), cvtpk(y0[2], y0[3])}, z1 = {cvtpk(y1[0], y1[1]), cvtpk(y1[2], y1[3])};
    if (w == 2) {
      LDSV(u32x2, L_Q + lane * 8) = z0;
      LDSV(u32x2, L_Q + 512 + lane * 8) = z1;
      lds_wait_eq(lds, F_E, 0x01010101u);
      LDSV(u32x2, F_Q) = u32x2{1u, T};
    } else {
      u32x2 hp;
#pragma unroll
      for (int i = 0; i < 2; i++) hp[i] = c < 14 ? cvtpk(lo16(z0[i]) * lo16(z1[i]), hi16(z0[i]) * hi16(z1[i])) : 0u;
      LDSV(u32x2, L_H + g * 128 + c * 8) = hp;
      LDSV(unsigned char, F_HW) = 1;
    }
    STAMP(5);
  } else if (AB & 8) {
  } else if (w == 0) {
    unsigned lv;
    NOUNROLL for (unsigned spin = 0;; spin++) {
      lv = LDSV(unsigned, L_LW + (lane & 31) * 4);
      if (none(lv == 0u) || spin > (SPIN_CAP << 4)) break;
      __builtin_amdgcn_s_sleep(2);
    }
    float mv = hi16(LDSV(unsigned, L_MW + (lane & 31) * 4));
    float lsum = __uint_as_float(lv);
    lsum += dpp<QX1>(lsum); lsum += dpp<QX2>(lsum); lsum += dpp<HMIR>(lsum);
    mv = fmaxf(mv, dpp<QX1>(mv)); mv = fmaxf(mv, dpp<QX2>(mv)); mv = fmaxf(mv, dpp<HMIR>(mv));
    const u32x4 st = {__float_as_uint(mv), T, __float_as_uint(lsum), T};
    if ((lane & 39) == 0) ST128(st, rws, WS_ST + slot * 128 + (lane >> 3) * 16, 0, SAS);
    STAMP(5);
  }
  if (AB & 8) { if (blk == 0 && w == 2) { lds_wait8(lds, F_PV); __builtin_amdgcn_s_sleep(100); if (lane == 0) ST32(T, rws, WS_EPOCH, 0, SC1); } STAMP_FLUSH(); return; }
  int aoff = WS_OA + slot0 * OA_STR + hl * 512 + w * 128 + (g & 1) * 64 + q4 * 16;
  int pboff = WS_PB + (hh * 8 + ng) * PB_STR + lane * 16;
  int wooff = (16 * (g & 1) + 2 * (g >> 1)) * 1024 + c * 8;
  int wdoff = (w == 3 && g >= 2) ? OOB - 4096 : g * 4096 + c * 8;
  const unsigned ta = T & 0xffu, tb = (T >> 16) & 0xffu;
  unsigned mska = 0xffu;
  asm volatile("" : "+v"(aoff), "+v"(pboff), "+v"(wooff), "+v"(wdoff), "+v"(mska));
  u32x2 WT[12];
#define WLOAD(j) do { if (AB & 1024) { WT[j] = u32x2{1u, 2u}; OPQ(WT[j]); } else if ((j) < 4) WT[j] = LD64(rwd, wdoff + (j) * 1024, wds, NTL); \
    else WT[j] = LD64(rwo, wooff + ((j) & 1) * 1024, wos + (((j) - 4) >> 1) * 4096, NTL); SB(); } while (0)
  if (NEARLY) {
    unsigned te = now32() + (w * ETAU) / 4;
#pragma unroll
    for (int j = 0; j < NEARLY; j++) { pace(te, ETAU); WLOAD(j); }
  }
  lds_wait8(lds, F_VI);
  const unsigned tvi = __builtin_amdgcn_readfirstlane(LDSV(unsigned, F_TV));
  STAMP(6);
  {
    unsigned tw = tvi + WDEL + (w * WTAU) / 4;
#pragma unroll
    for (int j = NEARLY; j < 12; j++) { if (WTAU || j == NEARLY) pace(tw, WTAU); WLOAD(j); }
  }
  lds_wait_eq(lds, F_HW, 1u);
  const u32x2 hf = LDSV(u32x2, L_H + bb * 128 + w * 32 + g * 8);
  STAMP(8);
  lds_wait8(lds, F_PV);
  if (PRE_A) __builtin_amdgcn_s_sleep(PRE_A);
  { unsigned tA = tvi + TA_ADD; pace(tA, 0); }
  STAMP(9);
  u32x4 a[4];
#pragma unroll
  for (int i = 0; i < 4; i++) a[i] = LD128(rws, aoff, i * OA_STR, SC1);
  SB();
  u32x4 fo[4];
  f32x4 acc[4];
#pragma unroll
  for (int e = 0; e < 4; e++) {
#pragma unroll
    for (int j = 0; j < 4; j++) fo[e][j] = (e & 1) ? perm_hi(WT[4 + 2 * j + 1][e >> 1], WT[4 + 2 * j][e >> 1]) : perm_lo(WT[4 + 2 * j + 1][e >> 1], WT[4 + 2 * j][e >> 1]);
    u32x2 fd;
#pragma unroll
    for (int j = 0; j < 2; j++) fd[j] = (e & 1) ? perm_hi(WT[2 * j + 1][e >> 1], WT[2 * j][e >> 1]) : perm_lo(WT[2 * j + 1][e >> 1], WT[2 * j][e >> 1]);
    acc[e] = mfma16(hf, fd, f32x4{0.f, 0.f, 0.f, 0.f});
  }
  asm volatile("" : "+v"(acc[0]), "+v"(acc[1]), "+v"(acc[2]), "+v"(acc[3]), "+v"(fo[0]), "+v"(fo[1]), "+v"(fo[2]), "+v"(fo[3]));
  lds_wait_eq(lds, F_W, 1u);
  f32x4 wt = LDSV(f32x4, L_WT + bb * 32 + sh * 16);
  OPQ(wt);
  STAMP(7);
  {
    unsigned spin = 0;
    NOUNROLL for (;; spin++) {
      unsigned bad = 0;
#pragma unroll
      for (int i = 0; i < 4; i++) { a[i][0] ^= ta; a[i][2] ^= tb; bad |= a[i][0] | a[i][2]; }
      if (none((bad & 0xffu) != 0)) break;
      if (spin > SPIN_CAP) break;
      __builtin_amdgcn_s_sleep(1);
      CBAR();
#pragma unroll
      for (int i = 0; i < 4; i++) a[i] = LD128(rws, aoff, i * OA_STR, SC1);
    }
    STAMPV(13, spin);
  }
  STAMP(10);
  f32x4 t = F4(a[0]) * wt[0];
#pragma unroll
  for (int i = 1; i < 4; i++) t = __builtin_elementwise_fma(F4(a[i]), f32x4{wt[i], wt[i], wt[i], wt[i]}, t);
  auto p0 = __builtin_amdgcn_permlane32_swap(__float_as_uint(t[0]), __float_as_uint(t[2]), false, false);
  auto p1 = __builtin_amdgcn_permlane32_swap(__float_as_uint(t[1]), __float_as_uint(t[3]), false, false);
  const unsigned own = cvtpk(__uint_as_float(p0[0]) + __uint_as_float(p0[1]), __uint_as_float(p1[0]) + __uint_as_float(p1[1]));
  const u32x4 A = {own, dppu<QB1>(own), dppu<QB2>(own), dppu<QB3>(own)};
#pragma unroll
  for (int e = 0; e < 4; e++) acc[e] = mfma32(A, fo[e], acc[e]);
  f32x4 z;
  if (meet4(lds, L_TR, F_T, w, lane, f32x4{acc[0][0], acc[1][0], acc[2][0], acc[3][0]}, z)) {
    const u32x4 o = {tagm(z[0], mska, ta), __float_as_uint(z[1]), tagm(z[2], mska, tb), __float_as_uint(z[3])};
    ST128(o, rws, pboff, 0, SC1);
  }
  STAMP(11);
  STAMP_FLUSH();
}

extern "C" void kernel_launch(void* const* d_in, const int* in_sizes, int n_in, void* d_out, int out_size, void* d_ws, size_t ws_size, hipStream_t stream) {
  const unsigned fl = 0x00020000u;
  fused<<<256, NTHR, 0, stream>>>(d_in[1], 33554432u, fl, d_in[2], 33554432u, fl, d_ws, (unsigned)WS_END, fl, d_in[0], 4096u, fl,
                                  d_in[3], 4194304u, fl, d_in[6], 1835008u, fl, d_in[7], 1835008u, fl, d_in[4], 4194304u, fl,
                                  d_in[8], 1835008u, fl, d_out, 4096u, fl);
}
```

```cpp
#include <hip/hip_runtime.h>
#include <stdint.h>

#define KAUX 2
#define VAUX 2
#define WAUX 0
#define OAUX 0
typedef unsigned short u16;
typedef int i32x4 __attribute__((ext_vector_type(4)));
typedef int i32x16 __attribute__((ext_vector_type(16)));
typedef unsigned u32x2 __attribute__((ext_vector_type(2)));
typedef unsigned u32x4 __attribute__((ext_vector_type(4)));
typedef float f32x2 __attribute__((ext_vector_type(2)));
typedef float f32x4 __attribute__((ext_vector_type(4)));
typedef short s16x4 __attribute__((ext_vector_type(4)));
typedef short s16x8 __attribute__((ext_vector_type(8)));
typedef __bf16 bf16x2 __attribute__((ext_vector_type(2)));
#define LAS __attribute__((address_space(3)))

__device__ u16 LD16(i32x4 r, int v, int s, int a) __asm("llvm.amdgcn.raw.buffer.load.i16");
__device__ unsigned LD32(i32x4 r, int v, int s, int a) __asm("llvm.amdgcn.raw.buffer.load.i32");
__device__ u32x2 LD64(i32x4 r, int v, int s, int a) __asm("llvm.amdgcn.raw.buffer.load.v2i32");
__device__ u32x4 LD128(i32x4 r, int v, int s, int a) __asm("llvm.amdgcn.raw.buffer.load.v4i32");
__device__ void ST16(u16 d, i32x4 r, int v, int s, int a) __asm("llvm.amdgcn.raw.buffer.store.i16");
__device__ void ST32(unsigned d, i32x4 r, int v, int s, int a) __asm("llvm.amdgcn.raw.buffer.store.i32");
__device__ void ST64(u32x2 d, i32x4 r, int v, int s, int a) __asm("llvm.amdgcn.raw.buffer.store.v2i32");
__device__ void ST128(u32x4 d, i32x4 r, int v, int s, int a) __asm("llvm.amdgcn.raw.buffer.store.v4i32");
constexpr int NTL = 2, SC1 = 16, SC1NT = 18;

constexpr int DM = 512, FF = 1792;
constexpr int NT = 4, NTHR = (NT + 8) * 64;

constexpr int WIN = 8;
constexpr int N0 = 2;
constexpr int TOFF = 30, TAU1 = 400, I1 = 8, TAU = 265;
constexpr int W0 = 2, DEL = 40;
constexpr int REBASE = -45;
constexpr int QGPOS = 12, HGPOS = 21;
constexpr int QKPOS = 4, SMPOS = 7, ORGPOS = 10, PVPOS = 13;
constexpr int GOFF = 40, GTAU = 30;
constexpr int WDEL = 15, WTAU = 5;
constexpr int TA_ADD = 140;
constexpr int CODE_BYTES = 12288;
constexpr int sched(int i) { return TOFF + ((i < I1 ? i - N0 + 1 : I1 - N0) * TAU1 + (i < I1 ? 0 : i - I1 + 1) * TAU) / 10; }

constexpr int WS_EPOCH = 0;
constexpr int WS_PF = 1024;
constexpr int WS_Q = 4096;
constexpr int WS_GU = WS_Q + 131072;
constexpr int WS_ST = WS_GU + 114688;
constexpr int WS_PR = 294912;
constexpr int WS_OA = WS_PR + 16384;
constexpr int WS_PB = WS_OA + 64 * 16384;
constexpr int WS_END = WS_PB + 32 * 16384;
static_assert(WS_ST + 32768 <= WS_PR && WS_PR % 16384 == 0, "map");
constexpr int L_RED = 0;
constexpr int L_TR = L_RED + 20480;
constexpr int L_PR = L_TR + 4096;
constexpr int L_Q = L_PR + 4096;
constexpr int L_H = L_Q + 1024;
constexpr int L_WT = L_H + 512;
constexpr int L_MW = L_WT + 128;
constexpr int L_LW = L_MW + 128;
constexpr int L_FLAG = L_LW + 128;
constexpr int F_Q = L_FLAG, F_H = L_FLAG + 8, F_E = L_FLAG + 12, F_VI = L_FLAG + 16, F_PV = L_FLAG + 24, F_T = L_FLAG + 32, F_P = L_FLAG + 36,
              F_TV = L_FLAG + 48, F_W = L_FLAG + 52, F_G = L_FLAG + 56, F_PF = L_FLAG + 60;
constexpr int L_END = L_FLAG + 64;
constexpr unsigned CAP_G = 1u << 18, CAP_L = 1u << 22;
constexpr int OOB = 0x7ffffff0;
constexpr float LOG2E = 1.4426950408889634f;

__device__ __forceinline__ float lo16(unsigned u) { return __uint_as_float(u << 16); }
__device__ __forceinline__ float hi16(unsigned u) { return __uint_as_float(u & 0xffff0000u); }
__device__ __forceinline__ unsigned cvtpk(float lo, float hi) { f32x2 f = {lo, hi}; return __builtin_bit_cast(unsigned, __builtin_convertvector(f, bf16x2)); }
__device__ __forceinline__ float rbf(float f) { return lo16(cvtpk(f, f)); }
__device__ __forceinline__ f32x4 mfma32(u32x4 a, u32x4 b, f32x4 c) { return __builtin_amdgcn_mfma_f32_16x16x32_bf16(__builtin_bit_cast(s16x8, a), __builtin_bit_cast(s16x8, b), c, 0, 0, 0); }
__device__ __forceinline__ f32x4 mfma16(u32x2 a, u32x2 b, f32x4 c) { return __builtin_amdgcn_mfma_f32_16x16x16bf16_1k(__builtin_bit_cast(s16x4, a), __builtin_bit_cast(s16x4, b), c, 0, 0, 0); }
template <int CTRL> __device__ __forceinline__ float dpp(float x) { return __builtin_bit_cast(float, __builtin_amdgcn_mov_dpp(__builtin_bit_cast(int, x), CTRL, 0xf, 0xf, true)); }
template <int CTRL> __device__ __forceinline__ unsigned dppu(unsigned x) { return (unsigned)__builtin_amdgcn_mov_dpp((int)x, CTRL, 0xf, 0xf, true); }
constexpr int ROR8 = 0x128, HMIR = 0x141, QX1 = 0xB1, QX2 = 0x4E, QB0 = 0x00, QB1 = 0x55, QB2 = 0xAA, QB3 = 0xFF;
__device__ __forceinline__ float fold32(float a, float b) {
  auto t = __builtin_amdgcn_permlane32_swap(__float_as_uint(a), __float_as_uint(b), false, false);
  return __uint_as_float(t[0]) + __uint_as_float(t[1]);
}
__device__ __forceinline__ float fold16(float a, float b) {
  auto t = __builtin_amdgcn_permlane16_swap(__float_as_uint(a), __float_as_uint(b), false, false);
  return __uint_as_float(t[0]) + __uint_as_float(t[1]);
}
__device__ __forceinline__ float xrow_sum(float x) { x = fold16(x, x); return fold32(x, x); }
__device__ __forceinline__ float xrow_max(float x) {
  auto s = __builtin_amdgcn_permlane16_swap(__float_as_uint(x), __float_as_uint(x), false, false);
  x = fmaxf(__uint_as_float(s[0]), __uint_as_float(s[1]));
  auto t = __builtin_amdgcn_permlane32_swap(__float_as_uint(x), __float_as_uint(x), false, false);
  return fmaxf(__uint_as_float(t[0]), __uint_as_float(t[1]));
}
__device__ __forceinline__ float dot2(unsigned a, unsigned b, float c) { return __builtin_amdgcn_fdot2_f32_bf16(__builtin_bit_cast(bf16x2, a), __builtin_bit_cast(bf16x2, b), c, false); }
__device__ __forceinline__ unsigned tagm(float v, unsigned m, unsigned t) { return (__float_as_uint(v) & ~m) | (t & m); }
__device__ __forceinline__ unsigned kpair(unsigned r0, unsigned r1, int sel) { return __builtin_amdgcn_perm(r1, r0, sel ? 0x07060302u : 0x05040100u); }
__device__ __forceinline__ bool none(bool p) { return __builtin_amdgcn_ballot_w64(p) == 0ull; }
#define F4(v) __builtin_bit_cast(f32x4, v)
#define SB() __builtin_amdgcn_sched_barrier(0)
#define CBAR() asm volatile("" ::: "memory")
#define OPQ(x) asm volatile("" : "+v"(x))
#define LDSV(T, off) (*(volatile LAS T*)(lds + (off)))
#define NOUNROLL _Pragma("clang loop unroll(disable)")
#define GIVEUP(spin, cap) asm volatile("s_add_u32 %0, %0, 1\n\ts_cmp_lt_u32 %0, %1\n\ts_cbranch_scc1 1f\n\ts_endpgm\n1:" : "+s"(spin) : "n"(cap) : "scc")

__device__ __forceinline__ unsigned now32() { unsigned long long t; asm volatile("s_memrealtime %0\n\ts_waitcnt lgkmcnt(0)" : "=s"(t)::"memory"); return (unsigned)t; }
__device__ __forceinline__ void pace(unsigned& tgt, int step) {
  asm volatile("1:\n\ts_memrealtime vcc\n\ts_waitcnt lgkmcnt(0)\n\ts_sub_u32 vcc_lo, vcc_lo, %0\n\ts_cmp_lt_i32 vcc_lo, 0\n\ts_cbranch_scc1 1b\n\ts_add_u32 %0, %0, %1"
               : "+s"(tgt) : "n"(step) : "memory", "scc", "vcc");
}
__device__ __forceinline__ void apace(unsigned& tgt, int step) {
  unsigned tmp;
  asm volatile("1:\n\ts_memrealtime vcc\n\ts_getreg_b32 %1, hwreg(HW_REG_IB_STS, 0, 4)\n\ts_sub_i32 %1, %1, %3\n\ts_max_i32 %1, %1, 0\n\ts_mul_i32 %1, %1, %4\n\t"
               "s_waitcnt lgkmcnt(0)\n\ts_sub_u32 vcc_lo, vcc_lo, %0\n\ts_cmp_lt_i32 vcc_lo, %1\n\ts_cbranch_scc1 1b\n\ts_add_u32 %0, %0, %2"
               : "+s"(tgt), "=&s"(tmp) : "n"(step), "n"(W0 - 1), "n"(DEL) : "memory", "scc", "vcc");
}
__device__ __forceinline__ void lds_wait_eq(LAS unsigned char* lds, int off, unsigned n) {
  unsigned spin = 0;
  NOUNROLL while (LDSV(unsigned, off) != n) { GIVEUP(spin, CAP_L); __builtin_amdgcn_s_sleep(1); }
  CBAR();
}
__device__ __forceinline__ void lds_wait_byte(LAS unsigned char* lds, int off) {
  unsigned spin = 0;
  NOUNROLL while (LDSV(unsigned char, off) == 0) { GIVEUP(spin, CAP_L); __builtin_amdgcn_s_sleep(2); }
  CBAR();
}
__device__ __forceinline__ void lds_wait8(LAS unsigned char* lds, int off) {
  unsigned spin = 0;
  NOUNROLL for (;;) {
    const u32x2 f = LDSV(u32x2, off);
    if ((f[0] & f[1]) == 0x01010101u) break;
    GIVEUP(spin, CAP_L);
    __builtin_amdgcn_s_sleep(1);
  }
  CBAR();
}
struct Meet { unsigned wr, rd, cnt, one; };
__device__ __forceinline__ Meet meet_prep(LAS unsigned char* lds, int area, int cnt, int w, int lane) {
  Meet m = {(unsigned)(uintptr_t)(lds + area) + w * 1024 + lane * 16, (unsigned)(uintptr_t)(lds + area) + lane * 16, (unsigned)(uintptr_t)(lds + cnt), 1u};
  asm volatile("" : "+v"(m.wr), "+v"(m.rd), "+v"(m.cnt), "+v"(m.one));
  return m;
}
__device__ __forceinline__ bool meet4(const Meet& m, f32x4 v, f32x4& sum) {
  *(volatile LAS f32x4*)(uintptr_t)m.wr = v;
  unsigned prev;
  f32x4 r0, r1, r2, r3;
  asm volatile("s_mov_b64 exec, 1\n\tds_add_rtn_u32 %0, %6, %7\n\ts_mov_b64 exec, -1\n\t"
               "ds_read_b128 %1, %5\n\tds_read_b128 %2, %5 offset:1024\n\tds_read_b128 %3, %5 offset:2048\n\tds_read_b128 %4, %5 offset:3072\n\t"
               "s_waitcnt lgkmcnt(0)"
               : "=&v"(prev), "=&v"(r0), "=&v"(r1), "=&v"(r2), "=&v"(r3) : "v"(m.rd), "v"(m.cnt), "v"(m.one) : "memory");
  sum = (r0 + r1) + (r2 + r3);
  return __builtin_amdgcn_readfirstlane(prev) == 3u;
}
__device__ __forceinline__ unsigned wait_place(LAS unsigned char* lds) {
  unsigned p = LDSV(unsigned, F_PF), spin = 0;
  NOUNROLL while (p == 0u) { GIVEUP(spin, CAP_L); __builtin_amdgcn_s_sleep(1); p = LDSV(unsigned, F_PF); }
  return __builtin_amdgcn_readfirstlane(p);
}
__device__ __forceinline__ int partner(unsigned place, int g) {
  const unsigned m = (((place >> g) & 1u) ? place : ~place) & 0xffu;
  return (g + 1 + __builtin_ctz(((m | (m << 8)) >> (g + 1)) & 0xffu)) & 7;
}

#define DESC(n) const void* p_##n, unsigned n##_bytes, unsigned n##_flags
__global__ void __launch_bounds__(NTHR, 3) fused_decode(DESC(k), DESC(v), DESC(ws), DESC(x), DESC(wq), DESC(wg), DESC(wu), DESC(wo), DESC(wd), DESC(out)) {
  __shared__ __attribute__((aligned(16))) unsigned char smem[L_END];
  auto ka = (const __attribute__((address_space(4))) i32x4*)__builtin_amdgcn_kernarg_segment_ptr();
  i32x16 KA, KB;
  unsigned long long pc0;
  asm volatile("s_load_dwordx16 %0, %3, 0x0\n\ts_load_dwordx16 %1, %3, 0x40\n\ts_getpc_b64 %2" : "=&s"(KA), "=&s"(KB), "=&s"(pc0) : "s"(ka));
  SB();
  LAS unsigned char* lds = (LAS unsigned char*)smem;
  const int blk = blockIdx.x, kvh = blk & 7, gi = blk >> 3, b = gi & 3, s = gi >> 2;
  const int tid = threadIdx.x, lane = tid & 63, w = __builtin_amdgcn_readfirstlane(tid >> 6), g = lane >> 4, c = lane & 15;
  u32x2 pfd = {0u, 0u};
  if (w == 3 && lane < CODE_BYTES / 256) {
    const int off = gi * (CODE_BYTES / 32) + lane * 8;
    asm volatile("global_load_dwordx2 %0, %1, %2" : "=v"(pfd) : "v"(off), "s"(pc0 & ~0xffull));
  }
  if (w == 2) {
    LDSV(unsigned, L_MW + lane * 4) = 0u;
    LDSV(unsigned, L_FLAG + c * 4) = 0u;
  }
  const int kv = w - NT;
  int koff = (c >> 2) * 256 + g * 64 + (c & 3) * 16;
  int kvbase = ((b * 8 + kvh) * 4096 + s * 512) * 256 + kv * 4096;
  int so0 = kvbase + (gi & 3) * 1024;
  OPQ(koff);
  asm volatile("s_waitcnt lgkmcnt(0)\n\ts_barrier" : "+s"(KA), "+s"(KB), "+s"(kvbase), "+s"(so0)::"memory");
  const i32x4 rws = {KA[8], KA[9], KA[10], KA[11]}, rx = {KA[12], KA[13], KA[14], KA[15]};
  const int slot = (kvh * 4 + b) * 8 + s;

  if (__builtin_expect(w >= NT, 1)) {
    const i32x4 rk = {KA[0], KA[1], KA[2], KA[3]}, rv = {KA[4], KA[5], KA[6], KA[7]};
    u32x4 K[16], V[16];
    const int voff = lane * 16;
#define SOFF(i) (((i) < 4 ? so0 : kvbase + ((((i) >> 2) + gi) & 3) * 1024) + ((i) & 3) * 32768)
#define WAITN(n) asm volatile("s_waitcnt vmcnt(%0)" ::"n"((n) - 1) : "memory")
#define PACE(i) apace(tgt, sched((i) + 1) - sched(i))
#define VLOAD(i) do { WAITN(WIN); PACE(16 + (i)); V[i] = LD128(rv, voff, SOFF(i), VAUX); \
    if ((i) + 17 == HGPOS && kv == 7) LDSV(unsigned char, F_G + 1) = 1; \
    if ((i) == 15) { LDSV(unsigned, F_TV) = now32(); LDSV(unsigned char, F_VI + kv) = 1; } SB(); } while (0)
    unsigned long long t64;
    asm volatile("s_memrealtime %0" : "=s"(t64));
#pragma unroll
    for (int i = 0; i < N0; i++) { K[i] = LD128(rk, koff, SOFF(i), KAUX); SB(); }
    asm volatile("s_waitcnt lgkmcnt(0)" : "+s"(t64));
    unsigned tgt = (unsigned)t64 + sched(N0) + (kv * TAU) / 80;
    WAITN(N0);
    { const unsigned t1 = now32() + REBASE + (kv * TAU) / 80; if ((int)(t1 - tgt) > 0) tgt = t1; }
#pragma unroll
    for (int i = N0; i < 16; i++) {
      if (i >= WIN) WAITN(WIN);
      PACE(i);
      K[i] = LD128(rk, koff, SOFF(i), KAUX);
      if (i + 1 == QGPOS && kv == 7) LDSV(unsigned char, F_G) = 1;
      SB();
    }
#pragma unroll
    for (int i = 0; i < QKPOS; i++) VLOAD(i);
    u32x2 fl = LDSV(u32x2, F_Q);
    { unsigned spin = 0; NOUNROLL while (fl[0] == 0u) { GIVEUP(spin, CAP_L); __builtin_amdgcn_s_sleep(1); fl = LDSV(u32x2, F_Q); } }
    CBAR();
    const unsigned T = __builtin_amdgcn_readfirstlane(fl[1]);
    float S[4];
    {
      const u32x4 qf = LDSV(u32x4, L_Q + koff);
      const bool a1 = c & 1, a2 = c & 2;
#pragma unroll
      for (int t = 0; t < 4; t++) {
        float y[4];
#pragma unroll
        for (int i = 0; i < 4; i++) {
          const f32x4 d = mfma32(K[t * 4 + i], qf, f32x4{0.f, 0.f, 0.f, 0.f});
          float z = dpp<QB0>(d[0]);
          z += dpp<QB1>(d[1]); OPQ(z);
          z += dpp<QB2>(d[2]); OPQ(z);
          z += dpp<QB3>(d[3]); OPQ(z);
          y[i] = z;
        }
        const float u0 = a1 ? y[1] : y[0], u1 = a1 ? y[3] : y[2];
        S[t] = a2 ? u1 : u0;
      }
#pragma unroll
      for (int i = 0; i < 4; i += 2) {
        const unsigned p = cvtpk(S[i], S[i + 1]);
        const unsigned p2 = cvtpk(lo16(p) * 0.08838834764831845f, hi16(p) * 0.08838834764831845f);
        S[i] = lo16(p2); S[i + 1] = hi16(p2);
      }
      float mw = fmaxf(fmaxf(S[0], S[1]), fmaxf(S[2], S[3]));
      mw = fmaxf(mw, dpp<QX1>(mw)); mw = fmaxf(mw, dpp<QX2>(mw));
      mw = xrow_max(mw);
      LDSV(unsigned, L_MW + (c >> 2) * 32 + kv * 4) = __float_as_uint(mw) | 1u;
    }
    SB();
#pragma unroll
    for (int i = QKPOS; i < SMPOS; i++) VLOAD(i);
    u32x4 P[2];
    {
      u32x4 m0, m1;
      unsigned spin = 0;
      NOUNROLL for (;;) {
        m0 = LDSV(u32x4, L_MW + (c >> 2) * 32); m1 = LDSV(u32x4, L_MW + (c >> 2) * 32 + 16);
        const u32x4 mm = m0 & m1;
        if (none((mm[0] & mm[1] & mm[2] & mm[3] & 1u) == 0u)) break;
        GIVEUP(spin, CAP_L);
        __builtin_amdgcn_s_sleep(1);
      }
      float M = -INFINITY;
#pragma unroll
      for (int i = 0; i < 4; i++) M = fmaxf(M, fmaxf(hi16(m0[i]), hi16(m1[i])));
      float p[4];
#pragma unroll
      for (int i = 0; i < 4; i++) p[i] = __builtin_amdgcn_exp2f((S[i] - M) * LOG2E);
      float lsum = (p[0] + p[1]) + (p[2] + p[3]);
      lsum += dpp<QX1>(lsum); lsum += dpp<QX2>(lsum);
      lsum = xrow_sum(lsum);
      LDSV(unsigned, L_LW + (c >> 2) * 32 + kv * 4) = __float_as_uint(lsum) | 1u;
#pragma unroll
      for (int u = 0; u < 2; u++) { const unsigned pk = cvtpk(p[2 * u], p[2 * u + 1]); P[u] = u32x4{pk, dppu<QB1>(pk), dppu<QB2>(pk), dppu<QB3>(pk)}; }
    }
    unsigned mska = 0xffu;
    asm volatile("" : "+v"(mska), "+v"(P[0]), "+v"(P[1]));
    SB();
#pragma unroll
    for (int i = SMPOS; i < PVPOS; i++) {
      if (i == ORGPOS) tgt += ((int)__builtin_amdgcn_readfirstlane(LDSV(unsigned, F_PF)) >> 16) - 6;
      VLOAD(i);
    }
    f32x4 O[8];
#pragma unroll
    for (int e = 0; e < 8; e++) {
      u32x4 f;
#pragma unroll
      for (int j = 0; j < 4; j++) f[j] = kpair(V[j][e >> 1], V[4 + j][e >> 1], e & 1);
      O[e] = mfma32(P[0], f, f32x4{0.f, 0.f, 0.f, 0.f});
    }
    asm volatile("" : "+v"(O[0]), "+v"(O[1]), "+v"(O[2]), "+v"(O[3]), "+v"(O[4]), "+v"(O[5]), "+v"(O[6]), "+v"(O[7]));
    SB();
    const unsigned place = wait_place(lds);
    const int side = ((place >> kvh) & 1) * 8192;
    int oaoff = WS_OA + (slot >> 2) * 16384 + side + (slot & 3) * 2048 + kv * 512 + ((lane >> 3) & 3) * 128 + (lane >> 5) * 64 + (lane & 7) * 8;
    OPQ(oaoff);
    const int soff = WS_ST + (partner(place, kvh) * 32 + (lane & 31)) * 128 + (gi >> 3) * 16;
    u32x4 s0 = {0u, ~T, 0u, 0u};
    if (kv == 5 && lane < 32) s0 = LD128(rws, soff, 0, SC1);
    SB();
    int redw = kv * 2560 + lane * 4, redw2 = redw + 1280, redr = (lane >> 5) * 10240 + ((lane & 31) >> 2) * 320 + kv * 64 + (lane & 3) * 16;
    unsigned tab = T >> ((lane & 3) * 8);
    asm volatile("" : "+v"(redw), "+v"(redw2), "+v"(redr), "+v"(tab));
#pragma unroll
    for (int i = PVPOS; i < 16; i++) VLOAD(i);
    {
      u32x4 F[8];
#pragma unroll
      for (int j = 0; j < 3; j++) {
#pragma unroll
        for (int e = 0; e < 8; e++) F[e][j] = kpair(V[8 + j][e >> 1], V[12 + j][e >> 1], e & 1);
        SB();
      }
#pragma unroll
      for (int i = 0; i < 4; i++) {
        F[2 * i][3] = kpair(V[11][i], V[15][i], 0); F[2 * i + 1][3] = kpair(V[11][i], V[15][i], 1);
        O[2 * i] = mfma32(P[1], F[2 * i], O[2 * i]); O[2 * i + 1] = mfma32(P[1], F[2 * i + 1], O[2 * i + 1]);
        SB();
      }
    }
#pragma unroll
    for (int e = 0; e < 4; e++) *(LAS float*)(lds + L_RED + redw + e * 320) = O[e][0];
#pragma unroll
    for (int e = 0; e < 4; e++) *(LAS float*)(lds + L_RED + redw2 + e * 320) = O[4 + e][0];
    CBAR();
    LDSV(unsigned char, F_PV + kv) = 1;
    if (kv < 4) {
      f32x4 r[4];
      unsigned spin = 0;
      NOUNROLL for (;;) {
        const u32x2 cnt = LDSV(u32x2, F_PV);
#pragma unroll
        for (int i = 0; i < 4; i++) r[i] = LDSV(f32x4, L_RED + redr + i * 2560);
        if ((cnt[0] & cnt[1]) == 0x01010101u) break;
        GIVEUP(spin, CAP_L);
      }
      const f32x4 a = (r[0] + r[1]) + (r[2] + r[3]);
      ST64(u32x2{tagm(fold32(a[0], a[2]), mska, tab), __float_as_uint(fold32(a[1], a[3]))}, rws, oaoff, 0, SC1);
    }
    if (kv == 5) {
      if (lane < 32) {
        unsigned spin = 0;
        NOUNROLL while (!none(((s0[1] ^ T) | (s0[3] ^ T)) != 0u)) {
          GIVEUP(spin, CAP_G);
          __builtin_amdgcn_s_sleep(2);
          CBAR();
          s0 = LD128(rws, soff, 0, SC1);
        }
        const float m1 = __uint_as_float(s0[0]);
        float MM = fmaxf(m1, dpp<QX1>(m1));
        MM = fmaxf(MM, dpp<QX2>(MM)); MM = fmaxf(MM, dpp<HMIR>(MM));
        const float e1 = __builtin_amdgcn_exp2f((m1 - MM) * LOG2E);
        float L = e1 * __uint_as_float(s0[2]);
        L += dpp<QX1>(L); L += dpp<QX2>(L); L += dpp<HMIR>(L);
        LDSV(float, L_WT + lane * 4) = e1 * __builtin_amdgcn_rcpf(L);
      }
      LDSV(unsigned, F_W) = 1u;
    }
    if (kv >= 6 && kvh == (int)((place >> 9) & 7u)) {
      const int lh = kv - 6, pg = lane >> 3;
      const int oi = (gi >> 3) * 512 + (gi & 7) * 64 + lh * 32 + (c & 7) * 4 + g;
      const i32x4 ro = ka[9];
      float xfin = lo16(LD16(rx, oi * 2, 0, 0));
      int go[4];
#pragma unroll
      for (int i = 0; i < 4; i++) go[i] = WS_PB + ((gi & 7) * 4 + i) * 16384 + side + pg * 1024 + (((gi >> 3) * 2 + lh + 2 * i + (pg >> 2)) & 7) * 128 + (lane & 7) * 16;
      int ooff = c < 8 ? oi * 2 : OOB;
      unsigned ta = (T >> ((lane & 1) * 16)) & 0xffu, tb = (T >> ((lane & 1) * 16 + 8)) & 0xffu;
      asm volatile("" : "+v"(xfin), "+v"(go[0]), "+v"(go[1]), "+v"(go[2]), "+v"(go[3]), "+v"(ooff), "+v"(ta), "+v"(tb));
      lds_wait_eq(lds, F_T, 4u);
      __builtin_amdgcn_s_sleep(18);
      u32x4 pb[4];
      unsigned spin = 0;
      NOUNROLL for (;;) {
        CBAR();
#pragma unroll
        for (int i = 0; i < 4; i++) pb[i] = LD128(rws, go[i], 0, SC1);
        unsigned bad = 0;
#pragma unroll
        for (int i = 0; i < 4; i++) { pb[i][0] ^= ta; pb[i][2] ^= tb; bad |= pb[i][0] | pb[i][2]; }
        if (none((bad & 0xffu) != 0)) break;
        GIVEUP(spin, CAP_G);
        __builtin_amdgcn_s_sleep(3);
      }
      const f32x4 v = (F4(pb[0]) + F4(pb[1])) + (F4(pb[2]) + F4(pb[3]));
      float z = fold16(fold32(v[0], v[2]), fold32(v[1], v[3]));
      z += dpp<ROR8>(z);
      z += xfin;
      ST16((u16)cvtpk(z, z), ro, ooff, 0, SC1);
      if (gi == 0 && lh == 0 && lane == 0) ST32(T, rws, WS_EPOCH, 0, SC1);
    }
    return;
  }

  __builtin_amdgcn_s_setprio(3);
  const unsigned ep = LD32(rws, WS_EPOCH, 0, SC1);
  const int hl = gi >> 3, ng = gi & 7;
  const int task = gi >> 1, kh = gi & 1;
  const unsigned tstart = (unsigned)__builtin_amdgcn_s_memrealtime();
  if (task < 15) {
    const bool isq = task < 8;
    u32x4 xs[4], xf, W[8];
#pragma unroll
    for (int i = 0; i < 4; i++) xs[i] = LD128(rx, (c >> 2) * 1024 + g * 64 + (c & 3) * 16 + i * 256, 0, 0);
    xf = LD128(rx, (c >> 2) * 1024 + (c & 1) * 64 + g * 16, kh * 512 + w * 128, 0);
    const int kvq = (kvh + 1) & 7, idx = kvh * 7 + task - 8, mat = idx >= 28 ? 1 : 0;
    const int cb = isq ? kvq * 8 + task : idx - 28 * mat;
    const int rstride = isq ? 8192 : FF * 2;
    const i32x4 rw = {isq ? KB[0] : mat ? KB[8] : KB[4], isq ? KB[1] : mat ? KB[9] : KB[5], isq ? KB[2] : KB[6], KB[3]};
    const int woff = ((c >> 3) * 32 + g * 8) * rstride + (c & 7) * 16;
    int wso = (kh * 256 + w * 64) * rstride + cb * 128;
    unsigned tg = tstart + GOFF + (w * GTAU) / 4;
#pragma unroll
    for (int j = 0; j < 8; j++) {
      if (!isq) pace(tg, GTAU);
      W[j] = LD128(rw, woff, wso, WAUX);
      wso += rstride;
      SB();
    }
    float sm = 0.f;
#pragma unroll
    for (int i = 0; i < 4; i++)
#pragma unroll
      for (int j = 0; j < 4; j++) sm = dot2(xs[i][j], 0x3f803f80u, sm);
    sm += dpp<QX1>(sm); sm += dpp<QX2>(sm);
    const float mean = xrow_sum(sm) * (1.f / DM);
    f32x2 ssv = {0.f, 0.f};
#pragma unroll
    for (int i = 0; i < 4; i++)
#pragma unroll
      for (int j = 0; j < 4; j++) { const f32x2 d = f32x2{lo16(xs[i][j]), hi16(xs[i][j])} - mean; ssv += d * d; }
    float ss = ssv[0] + ssv[1];
    ss += dpp<QX1>(ss); ss += dpp<QX2>(ss);
    const float var = rbf(xrow_sum(ss) * (1.f / DM)), mb = rbf(mean);
    const float den = rbf(__builtin_amdgcn_sqrtf(rbf(var + 1e-5f)));
    const float rden = __builtin_amdgcn_rcpf(den);
#pragma unroll
    for (int j = 0; j < 4; j++) {
      const unsigned d = cvtpk(lo16(xf[j]) - mb, hi16(xf[j]) - mb);
      const f32x2 n = {lo16(d), hi16(d)};
      const f32x2 q = n * rden;
      const f32x2 r = (n - q * den) * rden + q;
      xf[j] = cvtpk(r[0], r[1]);
    }
    SB();
    f32x4 acc[8];
#pragma unroll
    for (int e = 0; e < 8; e++) {
      u32x4 f;
#pragma unroll
      for (int j = 0; j < 4; j++) f[j] = kpair(W[2 * j][e >> 1], W[2 * j + 1][e >> 1], e & 1);
      acc[e] = mfma32(xf, f, f32x4{0.f, 0.f, 0.f, 0.f});
    }
    SB();
    f32x4 o, z;
#pragma unroll
    for (int i = 0; i < 4; i++) {
      const float a = acc[i][0] + dpp<ROR8>(acc[i][1]), bq = acc[4 + i][1] + dpp<ROR8>(acc[4 + i][0]);
      o[i] = c < 8 ? a : bq;
    }
    if (meet4(meet_prep(lds, L_PR, F_P, w, lane), o, z)) {
      const unsigned t0 = (__builtin_amdgcn_readfirstlane(ep) + 0x01010101u) >> ((c >> 3) * 16);
      const u32x4 pv = {tagm(z[0], 0xfu, t0), tagm(z[1], 0xfu, t0 >> 4), tagm(z[2], 0xfu, t0 >> 8), tagm(z[3], 0xfu, t0 >> 12)};
      const int col = (c & 7) * 8 + (c >> 3) * 4;
      const int poff = isq ? WS_Q + (((kvq * 4 + g) * 2 + kh) * 512 + task * 64 + col) * 4 : WS_GU + (((mat * 2 + kh) * 4 + g) * FF + cb * 64 + col) * 4;
      ST128(pv, rws, poff, 0, SC1NT);
    }
  }
  const unsigned T = __builtin_amdgcn_readfirstlane(ep) + 0x01010101u;
  asm volatile("" : "+v"(pfd) : "s"(T) : "memory");
  LDSV(unsigned char, F_E + w) = 1;
  const int bb = c >> 2, q4 = c & 3, sh = g >> 1;
  if (w >= 2) {
    int hh3 = 0;
    if (w == 3) hh3 = partner(wait_place(lds), kvh) * 4 + hl;
    const int goff = w == 2 ? WS_Q + (kvh * 4 + b) * 4096 + lane * 16 : WS_GU + (g * FF + hh3 * 56 + (c < 14 ? c : 13) * 4) * 4;
    const int st1 = w == 2 ? 2048 : 4 * FF * 4, st2 = w == 2 ? 1024 : 8 * FF * 4;
    const unsigned tn = T >> (goff & 16);
    const u32x4 TN = {tn & 0xfu, (tn >> 4) & 0xfu, (tn >> 8) & 0xfu, (tn >> 12) & 0xfu};
    u32x4 q[4];
    lds_wait_byte(lds, F_G);
    unsigned spin = 0;
    NOUNROLL for (;;) {
      CBAR();
      q[0] = LD128(rws, goff, 0, SC1NT); q[1] = LD128(rws, goff, st1, SC1NT); q[2] = LD128(rws, goff, st2, SC1NT); q[3] = LD128(rws, goff, st1 + st2, SC1NT);
      unsigned bad = 0;
#pragma unroll
      for (int i = 0; i < 4; i++) { q[i] ^= TN; bad |= (q[i][0] | q[i][1]) | (q[i][2] | q[i][3]); }
      if (none((bad & 0xfu) != 0)) break;
      GIVEUP(spin, CAP_G);
      __builtin_amdgcn_s_sleep(8);
    }
    const f32x4 y0 = F4(q[0]) + F4(q[1]), y1 = F4(q[2]) + F4(q[3]);
    const u32x2 z0 = {cvtpk(y0[0], y0[1]), cvtpk(y0[2], y0[3])}, z1 = {cvtpk(y1[0], y1[1]), cvtpk(y1[2], y1[3])};
    if (w == 2) {
      LDSV(u32x2, L_Q + lane * 8) = z0;
      LDSV(u32x2, L_Q + 512 + lane * 8) = z1;
      lds_wait_eq(lds, F_E, 0x01010101u);
      LDSV(u32x2, F_Q) = u32x2{1u, T};
    } else {
      u32x2 hp;
#pragma unroll
      for (int i = 0; i < 2; i++) hp[i] = c < 14 ? cvtpk(lo16(z0[i]) * lo16(z1[i]), hi16(z0[i]) * hi16(z1[i])) : 0u;
      LDSV(u32x2, L_H + g * 128 + c * 8) = hp;
      LDSV(unsigned, F_H) = 1u;
    }
  } else if (w == 0) {
    unsigned lv, spin = 0;
    NOUNROLL for (;;) {
      lv = LDSV(unsigned, L_LW + (lane & 31) * 4);
      if (none(lv == 0u)) break;
      GIVEUP(spin, CAP_L);
      __builtin_amdgcn_s_sleep(2);
    }
    float mv = hi16(LDSV(unsigned, L_MW + (lane & 31) * 4));
    float lsum = __uint_as_float(lv);
    lsum += dpp<QX1>(lsum); lsum += dpp<QX2>(lsum); lsum += dpp<HMIR>(lsum);
    mv = fmaxf(mv, dpp<QX1>(mv)); mv = fmaxf(mv, dpp<QX2>(mv)); mv = fmaxf(mv, dpp<HMIR>(mv));
    const u32x4 st = {__float_as_uint(mv), T, __float_as_uint(lsum), T};
    if ((lane & 39) == 0) ST128(st, rws, WS_ST + slot * 128 + (lane >> 3) * 16, 0, SC1);
  } else {
    if (gi == 31) {
      if (lane < 16) ST32(T, rws, WS_PR + (lane >> 3) * 8192 + kvh * 1024 + (lane & 7) * 128, 0, SC1);
      asm volatile("s_waitcnt vmcnt(0)" ::: "memory");
      unsigned best0 = ~0u, best1 = ~0u;
      NOUNROLL for (int i = 0; i < 8; i++) {
        unsigned long long c0, c1; unsigned d;
        asm volatile("s_memtime %0\n\ts_waitcnt lgkmcnt(0)\n\ts_buffer_load_dword %2, %3, %4\n\ts_waitcnt lgkmcnt(0)\n\ts_memtime %1\n\ts_waitcnt lgkmcnt(0)"
                     : "=&s"(c0), "=&s"(c1), "=&s"(d) : "s"(rws), "s"(WS_PR + (i & 1) * 8192 + kvh * 1024 + (i >> 1) * 128) : "memory");
        const unsigned dt = (unsigned)c1 - (unsigned)c0;
        if (i >= 2) { if (i & 1) best1 = min(best1, dt); else best0 = min(best0, dt); }
      }
      const unsigned xcc = __builtin_amdgcn_s_getreg(20 | (3 << 11)) & 7u;
      if (lane == 0) ST64(u32x2{T, (tstart << 4) | (xcc << 1) | (best1 < best0 ? 1u : 0u)}, rws, WS_PF + kvh * 8, 0, SC1);
    }
    lds_wait_byte(lds, F_G + 1);
    if (lane < 8) {
      u32x2 pf;
      unsigned spin = 0;
      NOUNROLL for (;;) {
        CBAR();
        pf = LD64(rws, WS_PF + lane * 8, 0, SC1);
        if (none(pf[0] != T)) break;
        GIVEUP(spin, CAP_G);
        __builtin_amdgcn_s_sleep(8);
      }
      unsigned kx = ((pf[1] & 14u) << 2) | lane;
      kx = min(kx, dppu<QX1>(kx)); kx = min(kx, dppu<QX2>(kx)); kx = min(kx, dppu<HMIR>(kx));
      unsigned dl = (unsigned)min(max(((int)(pf[1] - (tstart << 4))) >> 4, -100), 100);
      dl += dppu<QX1>(dl); dl += dppu<QX2>(dl); dl += dppu<HMIR>(dl);
      LDSV(unsigned, F_PF) = (unsigned)__builtin_amdgcn_ballot_w64(pf[1] & 1u) | 0x100u | ((kx & 7u) << 9) | ((unsigned)((int)dl >> 3) << 16);
    }
  }
  const unsigned place = wait_place(lds);
  const int kva = partner(place, kvh), hh = kva * 4 + hl;
  const i32x4 rwo = {KB[12], KB[13], KB[14], KB[15]}, rwd = ka[8];
  const int wos = (hh * 128 + w * 2) * 1024 + ng * 128, wds = (hh * 56 + w * 16) * 1024 + ng * 128;
  int aoff = WS_OA + ((kva * 4 + bb) * 2 + sh) * 16384 + ((place >> kvh) & 1) * 8192 + hl * 512 + w * 128 + (g & 1) * 64 + q4 * 16;
  int pboff = WS_PB + (ng * 4 + (hh >> 3)) * 16384 + ((place >> ((place >> 9) & 7)) & 1) * 8192 + (hh & 7) * 1024 + ((lane * 16 + (hh >> 2) * 128) & 1023);
  int wooff = g * 16384 + c * 8;
  int wdoff = (w == 3 && g >= 2) ? OOB - 4096 : g * 4096 + c * 8;
  unsigned ta = (T >> ((lane & 1) * 16)) & 0xffu, tb = (T >> ((lane & 1) * 16 + 8)) & 0xffu, mska = 0xffu;
  asm volatile("" : "+v"(ta), "+v"(tb));
  asm volatile("" : "+v"(aoff), "+v"(pboff), "+v"(wooff), "+v"(wdoff), "+v"(mska));
  lds_wait8(lds, F_VI);
  const unsigned tvi = __builtin_amdgcn_readfirstlane(LDSV(unsigned, F_TV));
  u32x2 WD[4], WO[8];
  {
    unsigned tw = tvi + WDEL + (w * WTAU) / 4;
#pragma unroll
    for (int j = 0; j < 4; j++) { pace(tw, WTAU); WD[j] = LD64(rwd, wdoff + j * 1024, wds, OAUX); SB(); }
#pragma unroll
    for (int j = 0; j < 8; j++) { pace(tw, WTAU); WO[j] = LD64(rwo, wooff + (j >> 2) * 1024, wos + (j & 1) * 8192 + ((j >> 1) & 1) * 65536, OAUX); SB(); }
  }
  lds_wait_eq(lds, F_H, 1u);
  const u32x2 hf = LDSV(u32x2, L_H + bb * 128 + w * 32 + g * 8);
  lds_wait8(lds, F_PV);
  __builtin_amdgcn_s_sleep(4);
  { unsigned tA = tvi + TA_ADD; pace(tA, 0); }
  u32x4 a[4];
#pragma unroll
  for (int i = 0; i < 4; i++) a[i] = LD128(rws, aoff, i * 2048, SC1);
  SB();
  u32x4 fo[4];
  f32x4 acc[4];
#pragma unroll
  for (int e = 0; e < 4; e++) {
#pragma unroll
    for (int j = 0; j < 4; j++) fo[e][j] = kpair(WO[2 * j][e >> 1], WO[2 * j + 1][e >> 1], e & 1);
    acc[e] = mfma16(hf, u32x2{kpair(WD[0][e >> 1], WD[1][e >> 1], e & 1), kpair(WD[2][e >> 1], WD[3][e >> 1], e & 1)}, f32x4{0.f, 0.f, 0.f, 0.f});
  }
  asm volatile("" : "+v"(acc[0]), "+v"(acc[1]), "+v"(acc[2]), "+v"(acc[3]), "+v"(fo[0]), "+v"(fo[1]), "+v"(fo[2]), "+v"(fo[3]));
  const Meet mt = meet_prep(lds, L_TR, F_T, w, lane);
  lds_wait_eq(lds, F_W, 1u);
  f32x4 wt = LDSV(f32x4, L_WT + bb * 32 + sh * 16);
  OPQ(wt);
  {
    unsigned spin = 0;
    NOUNROLL for (;;) {
      unsigned bad = 0;
#pragma unroll
      for (int i = 0; i < 4; i++) { a[i][0] ^= ta; a[i][2] ^= tb; bad |= a[i][0] | a[i][2]; }
      if (none((bad & 0xffu) != 0)) break;
      GIVEUP(spin, CAP_G);
      __builtin_amdgcn_s_sleep(2);
      CBAR();
#pragma unroll
      for (int i = 0; i < 4; i++) a[i] = LD128(rws, aoff, i * 2048, SC1);
    }
  }
  f32x4 t = F4(a[0]) * wt[0];
#pragma unroll
  for (int i = 1; i < 4; i++) t = __builtin_elementwise_fma(F4(a[i]), f32x4{wt[i], wt[i], wt[i], wt[i]}, t);
  const unsigned own = cvtpk(fold32(t[0], t[2]), fold32(t[1], t[3]));
  const u32x4 A = {own, dppu<QB1>(own), dppu<QB2>(own), dppu<QB3>(own)};
#pragma unroll
  for (int e = 0; e < 4; e++) acc[e] = mfma32(A, fo[e], acc[e]);
  SB();
  f32x4 z;
  if (meet4(mt, f32x4{acc[0][0], acc[1][0], acc[2][0], acc[3][0]}, z)) {
    const u32x4 o = {tagm(z[0], mska, ta), __float_as_uint(z[1]), tagm(z[2], mska, tb), __float_as_uint(z[3])};
    ST128(o, rws, pboff, 0, SC1);
  }
}

extern "C" void kernel_launch(void* const* d_in, const int* in_sizes, int n_in, void* d_out, int out_size, void* d_ws, size_t ws_size, hipStream_t stream) {
  const unsigned fl = 0x00020000u;
  fused_decode<<<256, NTHR, 0, stream>>>(d_in[1], 33554432u, fl, d_in[2], 33554432u, fl, d_ws, (unsigned)WS_END, fl, d_in[0], 4096u, fl,
                                         d_in[3], 4194304u, fl, d_in[6], 1835008u, fl, d_in[7], 1835008u, fl, d_in[4], 4194304u, fl,
                                         d_in[8], 1835008u, fl, d_out, 4096u, fl);
}
```

```cpp
#include <hip/hip_runtime.h>
#include <stdint.h>

#ifndef _WIN
#define _WIN 8
#endif
#ifndef _N0
#define _N0 2
#endif
#ifndef _TOFF
#define _TOFF 30
#endif
#ifndef _TAU1
#define _TAU1 400
#endif
#ifndef _I1
#define _I1 8
#endif
#ifndef _TAU
#define _TAU 265
#endif
#ifndef _W0
#define _W0 2
#endif
#ifndef _DEL
#define _DEL 40
#endif
#ifndef _REBASE
#define _REBASE -45
#endif
#ifndef _QGPOS
#define _QGPOS 12
#endif
#ifndef _HGPOS
#define _HGPOS 21
#endif
#ifndef _QKPOS
#define _QKPOS 4
#endif
#ifndef _SMPOS
#define _SMPOS 7
#endif
#ifndef _ORGPOS
#define _ORGPOS 10
#endif
#ifndef _PVPOS
#define _PVPOS 14
#endif
#ifndef _GOFF
#define _GOFF 40
#endif
#ifndef _GTAU
#define _GTAU 30
#endif
#ifndef _WDEL
#define _WDEL 8
#endif
#ifndef _WTAU
#define _WTAU 6
#endif
#ifndef _TA_ADD
#define _TA_ADD 125
#endif
#define QS 16
#define QL 16
#define PREB 18
#define SLA 1
#define SLB 1
#define KAUX 2
#define VAUX 2
#define WAUX 2
#define OAUX 2
typedef unsigned short u16;
typedef int i32x4 __attribute__((ext_vector_type(4)));
typedef int i32x16 __attribute__((ext_vector_type(16)));
typedef unsigned u32x2 __attribute__((ext_vector_type(2)));
typedef unsigned u32x4 __attribute__((ext_vector_type(4)));
typedef float f32x2 __attribute__((ext_vector_type(2)));
typedef float f32x4 __attribute__((ext_vector_type(4)));
typedef short s16x4 __attribute__((ext_vector_type(4)));
typedef short s16x8 __attribute__((ext_vector_type(8)));
typedef __bf16 bf16x2 __attribute__((ext_vector_type(2)));
#define LAS __attribute__((address_space(3)))

__device__ u16 LD16(i32x4 r, int v, int s, int a) __asm("llvm.amdgcn.raw.buffer.load.i16");
__device__ unsigned LD32(i32x4 r, int v, int s, int a) __asm("llvm.amdgcn.raw.buffer.load.i32");
__device__ u32x2 LD64(i32x4 r, int v, int s, int a) __asm("llvm.amdgcn.raw.buffer.load.v2i32");
__device__ u32x4 LD128(i32x4 r, int v, int s, int a) __asm("llvm.amdgcn.raw.buffer.load.v4i32");
__device__ void ST16(u16 d, i32x4 r, int v, int s, int a) __asm("llvm.amdgcn.raw.buffer.store.i16");
__device__ void ST32(unsigned d, i32x4 r, int v, int s, int a) __asm("llvm.amdgcn.raw.buffer.store.i32");
__device__ void ST64(u32x2 d, i32x4 r, int v, int s, int a) __asm("llvm.amdgcn.raw.buffer.store.v2i32");
__device__ void ST128(u32x4 d, i32x4 r, int v, int s, int a) __asm("llvm.amdgcn.raw.buffer.store.v4i32");
constexpr int NTL = 2, SC1 = 16, SC1NT = 18;

constexpr int DM = 512, FF = 1792;
constexpr int NT = 4, NTHR = (NT + 8) * 64;

constexpr int WIN = _WIN;
constexpr int N0 = _N0;
constexpr int TOFF = _TOFF, TAU1 = _TAU1, I1 = _I1, TAU = _TAU;
constexpr int W0 = _W0, DEL = _DEL;
constexpr int REBASE = _REBASE;
constexpr int QGPOS = _QGPOS, HGPOS = _HGPOS;
constexpr int QKPOS = _QKPOS, SMPOS = _SMPOS, ORGPOS = _ORGPOS, PVPOS = _PVPOS;
constexpr int GOFF = _GOFF, GTAU = _GTAU;
constexpr int WDEL = _WDEL, WTAU = _WTAU;
constexpr int TA_ADD = _TA_ADD;
constexpr int CODE_BYTES = 12288;
constexpr int sched(int i) { return TOFF + ((i < I1 ? i - N0 + 1 : I1 - N0) * TAU1 + (i < I1 ? 0 : i - I1 + 1) * TAU) / 10; }

constexpr int WS_EPOCH = 0;
constexpr int WS_PF = 1024;
constexpr int WS_Q = 4096;
constexpr int WS_GU = WS_Q + 131072;
constexpr int WS_ST = WS_GU + 114688;
constexpr int WS_PR = 294912;
constexpr int WS_OA = WS_PR + 16384;
constexpr int WS_PB = WS_OA + 64 * 16384;
constexpr int WS_END = WS_PB + 32 * 16384;
static_assert(WS_ST + 32768 <= WS_PR && WS_PR % 16384 == 0, "map");
constexpr int L_RED = 0;
constexpr int L_TR = L_RED + 20480;
constexpr int L_PR = L_TR + 4096;
constexpr int L_Q = L_PR + 4096;
constexpr int L_H = L_Q + 1024;
constexpr int L_WT = L_H + 512;
constexpr int L_MW = L_WT + 128;
constexpr int L_LW = L_MW + 128;
constexpr int L_FLAG = L_LW + 128;
constexpr int F_Q = L_FLAG, F_H = L_FLAG + 8, F_E = L_FLAG + 12, F_VI = L_FLAG + 16, F_PV = L_FLAG + 24, F_T = L_FLAG + 32, F_P = L_FLAG + 36,
              F_TV = L_FLAG + 48, F_W = L_FLAG + 52, F_G = L_FLAG + 56, F_PF = L_FLAG + 60;
constexpr int L_END = L_FLAG + 64;
constexpr unsigned CAP_G = 1u << 18, CAP_L = 1u << 22;
constexpr int OOB = 0x7ffffff0;
constexpr float LOG2E = 1.4426950408889634f;

__device__ __forceinline__ float lo16(unsigned u) { return __uint_as_float(u << 16); }
__device__ __forceinline__ float hi16(unsigned u) { return __uint_as_float(u & 0xffff0000u); }
__device__ __forceinline__ unsigned cvtpk(float lo, float hi) { f32x2 f = {lo, hi}; return __builtin_bit_cast(unsigned, __builtin_convertvector(f, bf16x2)); }
__device__ __forceinline__ float rbf(float f) { return lo16(cvtpk(f, f)); }
__device__ __forceinline__ f32x4 mfma32(u32x4 a, u32x4 b, f32x4 c) { return __builtin_amdgcn_mfma_f32_16x16x32_bf16(__builtin_bit_cast(s16x8, a), __builtin_bit_cast(s16x8, b), c, 0, 0, 0); }
__device__ __forceinline__ f32x4 mfma16(u32x2 a, u32x2 b, f32x4 c) { return __builtin_amdgcn_mfma_f32_16x16x16bf16_1k(__builtin_bit_cast(s16x4, a), __builtin_bit_cast(s16x4, b), c, 0, 0, 0); }
template <int CTRL> __device__ __forceinline__ float dpp(float x) { return __builtin_bit_cast(float, __builtin_amdgcn_mov_dpp(__builtin_bit_cast(int, x), CTRL, 0xf, 0xf, true)); }
template <int CTRL> __device__ __forceinline__ unsigned dppu(unsigned x) { return (unsigned)__builtin_amdgcn_mov_dpp((int)x, CTRL, 0xf, 0xf, true); }
constexpr int ROR8 = 0x128, HMIR = 0x141, QX1 = 0xB1, QX2 = 0x4E, QB0 = 0x00, QB1 = 0x55, QB2 = 0xAA, QB3 = 0xFF;
__device__ __forceinline__ float fold32(float a, float b) {
  auto t = __builtin_amdgcn_permlane32_swap(__float_as_uint(a), __float_as_uint(b), false, false);
  return __uint_as_float(t[0]) + __uint_as_float(t[1]);
}
__device__ __forceinline__ float fold16(float a, float b) {
  auto t = __builtin_amdgcn_permlane16_swap(__float_as_uint(a), __float_as_uint(b), false, false);
  return __uint_as_float(t[0]) + __uint_as_float(t[1]);
}
__device__ __forceinline__ float xrow_sum(float x) { x = fold16(x, x); return fold32(x, x); }
__device__ __forceinline__ float xrow_max(float x) {
  auto s = __builtin_amdgcn_permlane16_swap(__float_as_uint(x), __float_as_uint(x), false, false);
  x = fmaxf(__uint_as_float(s[0]), __uint_as_float(s[1]));
  auto t = __builtin_amdgcn_permlane32_swap(__float_as_uint(x), __float_as_uint(x), false, false);
  return fmaxf(__uint_as_float(t[0]), __uint_as_float(t[1]));
}
__device__ __forceinline__ float dot2(unsigned a, unsigned b, float c) { return __builtin_amdgcn_fdot2_f32_bf16(__builtin_bit_cast(bf16x2, a), __builtin_bit_cast(bf16x2, b), c, false); }
__device__ __forceinline__ unsigned tagm(float v, unsigned m, unsigned t) { return (__float_as_uint(v) & ~m) | (t & m); }
__device__ __forceinline__ unsigned kpair(unsigned r0, unsigned r1, int sel) { return __builtin_amdgcn_perm(r1, r0, sel ? 0x07060302u : 0x05040100u); }
__device__ __forceinline__ bool none(bool p) { return __builtin_amdgcn_ballot_w64(p) == 0ull; }
#define F4(v) __builtin_bit_cast(f32x4, v)
#define SB() __builtin_amdgcn_sched_barrier(0)
#define CBAR() asm volatile("" ::: "memory")
#define OPQ(x) asm volatile("" : "+v"(x))
#define LDSV(T, off) (*(volatile LAS T*)(lds + (off)))
#define NOUNROLL _Pragma("clang loop unroll(disable)")
#define GIVEUP(spin, cap) asm volatile("s_add_u32 %0, %0, 1\n\ts_cmp_lt_u32 %0, %1\n\ts_cbranch_scc1 1f\n\ts_endpgm\n1:" : "+s"(spin) : "n"(cap) : "scc")

__device__ __forceinline__ unsigned now32() { unsigned long long t; asm volatile("s_memrealtime %0\n\ts_waitcnt lgkmcnt(0)" : "=s"(t)::"memory"); return (unsigned)t; }
__device__ __forceinline__ void pace(unsigned& tgt, int step) {
  asm volatile("1:\n\ts_memrealtime vcc\n\ts_waitcnt lgkmcnt(0)\n\ts_sub_u32 vcc_lo, vcc_lo, %0\n\ts_cmp_lt_i32 vcc_lo, 0\n\ts_cbranch_scc1 1b\n\ts_add_u32 %0, %0, %1"
               : "+s"(tgt) : "n"(step) : "memory", "scc", "vcc");
}
__device__ __forceinline__ void apace(unsigned& tgt, int step) {
  unsigned tmp;
  asm volatile("1:\n\ts_memrealtime vcc\n\ts_getreg_b32 %1, hwreg(HW_REG_IB_STS, 0, 4)\n\ts_sub_i32 %1, %1, %3\n\ts_max_i32 %1, %1, 0\n\ts_mul_i32 %1, %1, %4\n\t"
               "s_waitcnt lgkmcnt(0)\n\ts_sub_u32 vcc_lo, vcc_lo, %0\n\ts_cmp_lt_i32 vcc_lo, %1\n\ts_cbranch_scc1 1b\n\ts_add_u32 %0, %0, %2"
               : "+s"(tgt), "=&s"(tmp) : "n"(step), "n"(W0 - 1), "n"(DEL) : "memory", "scc", "vcc");
}
__device__ __forceinline__ void lds_wait_eq(LAS unsigned char* lds, int off, unsigned n) {
  unsigned spin = 0;
  NOUNROLL while (LDSV(unsigned, off) != n) { GIVEUP(spin, CAP_L); __builtin_amdgcn_s_sleep(1); }
  CBAR();
}
__device__ __forceinline__ void lds_wait_byte(LAS unsigned char* lds, int off) {
  unsigned spin = 0;
  NOUNROLL while (LDSV(unsigned char, off) == 0) { GIVEUP(spin, CAP_L); __builtin_amdgcn_s_sleep(2); }
  CBAR();
}
__device__ __forceinline__ void lds_wait8(LAS unsigned char* lds, int off) {
  unsigned spin = 0;
  NOUNROLL for (;;) {
    const u32x2 f = LDSV(u32x2, off);
    if ((f[0] & f[1]) == 0x01010101u) break;
    GIVEUP(spin, CAP_L);
    __builtin_amdgcn_s_sleep(1);
  }
  CBAR();
}
struct Meet { unsigned wr, rd, cnt, one; };
__device__ __forceinline__ Meet meet_prep(LAS unsigned char* lds, int area, int cnt, int w, int lane) {
  Meet m = {(unsigned)(uintptr_t)(lds + area) + w * 1024 + lane * 16, (unsigned)(uintptr_t)(lds + area) + lane * 16, (unsigned)(uintptr_t)(lds + cnt), 1u};
  asm volatile("" : "+v"(m.wr), "+v"(m.rd), "+v"(m.cnt), "+v"(m.one));
  return m;
}
__device__ __forceinline__ bool meet4(const Meet& m, f32x4 v, f32x4& sum) {
  *(volatile LAS f32x4*)(uintptr_t)m.wr = v;
  unsigned prev;
  f32x4 r0, r1, r2, r3;
  asm volatile("s_mov_b64 exec, 1\n\tds_add_rtn_u32 %0, %6, %7\n\ts_mov_b64 exec, -1\n\t"
               "ds_read_b128 %1, %5\n\tds_read_b128 %2, %5 offset:1024\n\tds_read_b128 %3, %5 offset:2048\n\tds_read_b128 %4, %5 offset:3072\n\t"
               "s_waitcnt lgkmcnt(0)"
               : "=&v"(prev), "=&v"(r0), "=&v"(r1), "=&v"(r2), "=&v"(r3) : "v"(m.rd), "v"(m.cnt), "v"(m.one) : "memory");
  sum = (r0 + r1) + (r2 + r3);
  return __builtin_amdgcn_readfirstlane(prev) == 3u;
}
__device__ __forceinline__ unsigned wait_place(LAS unsigned char* lds) {
  unsigned p = LDSV(unsigned, F_PF), spin = 0;
  NOUNROLL while (p == 0u) { GIVEUP(spin, CAP_L); __builtin_amdgcn_s_sleep(1); p = LDSV(unsigned, F_PF); }
  return __builtin_amdgcn_readfirstlane(p);
}
__device__ __forceinline__ int partner(unsigned place, int g) {
  const unsigned m = (((place >> g) & 1u) ? place : ~place) & 0xffu;
  return (g + 1 + __builtin_ctz(((m | (m << 8)) >> (g + 1)) & 0xffu)) & 7;
}

#define DESC(n) const void* p_##n, unsigned n##_bytes, unsigned n##_flags
__global__ void __launch_bounds__(NTHR, 3) fused_decode(DESC(k), DESC(v), DESC(ws), DESC(x), DESC(wq), DESC(wg), DESC(wu), DESC(wo), DESC(wd), DESC(out)) {
  __shared__ __attribute__((aligned(16))) unsigned char smem[L_END];
  auto ka = (const __attribute__((address_space(4))) i32x4*)__builtin_amdgcn_kernarg_segment_ptr();
  i32x16 KA, KB;
  unsigned long long pc0;
  asm volatile("s_load_dwordx16 %0, %3, 0x0\n\ts_load_dwordx16 %1, %3, 0x40\n\ts_getpc_b64 %2" : "=&s"(KA), "=&s"(KB), "=&s"(pc0) : "s"(ka));
  SB();
  LAS unsigned char* lds = (LAS unsigned char*)smem;
  const int blk = blockIdx.x, kvh = blk & 7, gi = blk >> 3, b = gi & 3, s = gi >> 2;
  const int tid = threadIdx.x, lane = tid & 63, w = __builtin_amdgcn_readfirstlane(tid >> 6), g = lane >> 4, c = lane & 15;
  u32x2 pfd = {0u, 0u};
  if (w == 3 && lane < CODE_BYTES / 256) {
    const int off = gi * (CODE_BYTES / 32) + lane * 8;
    asm volatile("global_load_dwordx2 %0, %1, %2" : "=v"(pfd) : "v"(off), "s"(pc0 & ~0xffull));
  }
  if (w == 2) {
    LDSV(unsigned, L_MW + lane * 4) = 0u;
    LDSV(unsigned, L_FLAG + c * 4) = 0u;
  }
  const int kv = w - NT;
  int koff = (c >> 2) * 256 + g * 64 + (c & 3) * 16;
  int kvbase = ((b * 8 + kvh) * 4096 + s * 512) * 256 + kv * 4096;
  int so0 = kvbase + (gi & 3) * 1024;
  OPQ(koff);
  asm volatile("s_waitcnt lgkmcnt(0)\n\ts_barrier" : "+s"(KA), "+s"(KB), "+s"(kvbase), "+s"(so0)::"memory");
  const i32x4 rws = {KA[8], KA[9], KA[10], KA[11]}, rx = {KA[12], KA[13], KA[14], KA[15]};
  const int slot = (kvh * 4 + b) * 8 + s;

  if (__builtin_expect(w >= NT, 1)) {
    const i32x4 rk = {KA[0], KA[1], KA[2], KA[3]}, rv = {KA[4], KA[5], KA[6], KA[7]};
    u32x4 K[16], V[16];
    const int voff = lane * 16;
#define SOFF(i) (((i) < 4 ? so0 : kvbase + ((((i) >> 2) + gi) & 3) * 1024) + ((i) & 3) * 32768)
#define WAITN(n) asm volatile("s_waitcnt vmcnt(%0)" ::"n"((n) - 1) : "memory")
#define PACE(i) apace(tgt, sched((i) + 1) - sched(i))
#define VLOAD(i) do { WAITN(WIN); PACE(16 + (i)); V[i] = LD128(rv, voff, SOFF(i), VAUX); \
    if ((i) + 17 == HGPOS && kv == 7) LDSV(unsigned char, F_G + 1) = 1; \
    if ((i) == 15) { LDSV(unsigned, F_TV) = now32(); LDSV(unsigned char, F_VI + kv) = 1; } SB(); } while (0)
    unsigned long long t64;
    asm volatile("s_memrealtime %0" : "=s"(t64));
#pragma unroll
    for (int i = 0; i < N0; i++) { K[i] = LD128(rk, koff, SOFF(i), KAUX); SB(); }
    asm volatile("s_waitcnt lgkmcnt(0)" : "+s"(t64));
    unsigned tgt = (unsigned)t64 + sched(N0) + (kv * TAU) / 80;
    WAITN(N0);
    { const unsigned t1 = now32() + REBASE + (kv * TAU) / 80; if ((int)(t1 - tgt) > 0) tgt = t1; }
#pragma unroll
    for (int i = N0; i < 16; i++) {
      if (i >= WIN) WAITN(WIN);
      PACE(i);
      K[i] = LD128(rk, koff, SOFF(i), KAUX);
      if (i + 1 == QGPOS && kv == 7) LDSV(unsigned char, F_G) = 1;
      SB();
    }
#pragma unroll
    for (int i = 0; i < QKPOS; i++) VLOAD(i);
    u32x2 fl = LDSV(u32x2, F_Q);
    { unsigned spin = 0; NOUNROLL while (fl[0] == 0u) { GIVEUP(spin, CAP_L); __builtin_amdgcn_s_sleep(1); fl = LDSV(u32x2, F_Q); } }
    CBAR();
    const unsigned T = __builtin_amdgcn_readfirstlane(fl[1]);
    float S[4];
    {
      const u32x4 qf = LDSV(u32x4, L_Q + koff);
      const bool a1 = c & 1, a2 = c & 2;
#pragma unroll
      for (int t = 0; t < 4; t++) {
        float y[4];
#pragma unroll
        for (int i = 0; i < 4; i++) {
          const f32x4 d = mfma32(K[t * 4 + i], qf, f32x4{0.f, 0.f, 0.f, 0.f});
          float z = dpp<QB0>(d[0]);
          z += dpp<QB1>(d[1]); OPQ(z);
          z += dpp<QB2>(d[2]); OPQ(z);
          z += dpp<QB3>(d[3]); OPQ(z);
          y[i] = z;
        }
        const float u0 = a1 ? y[1] : y[0], u1 = a1 ? y[3] : y[2];
        S[t] = a2 ? u1 : u0;
      }
#pragma unroll
      for (int i = 0; i < 4; i += 2) {
        const unsigned p = cvtpk(S[i], S[i + 1]);
        const unsigned p2 = cvtpk(lo16(p) * 0.08838834764831845f, hi16(p) * 0.08838834764831845f);
        S[i] = lo16(p2); S[i + 1] = hi16(p2);
      }
      float mw = fmaxf(fmaxf(S[0], S[1]), fmaxf(S[2], S[3]));
      mw = fmaxf(mw, dpp<QX1>(mw)); mw = fmaxf(mw, dpp<QX2>(mw));
      mw = xrow_max(mw);
      LDSV(unsigned, L_MW + (c >> 2) * 32 + kv * 4) = __float_as_uint(mw) | 1u;
    }
    SB();
#pragma unroll
    for (int i = QKPOS; i < SMPOS; i++) VLOAD(i);
    u32x4 P[2];
    {
      u32x4 m0, m1;
      unsigned spin = 0;
      NOUNROLL for (;;) {
        m0 = LDSV(u32x4, L_MW + (c >> 2) * 32); m1 = LDSV(u32x4, L_MW + (c >> 2) * 32 + 16);
        const u32x4 mm = m0 & m1;
        if (none((mm[0] & mm[1] & mm[2] & mm[3] & 1u) == 0u)) break;
        GIVEUP(spin, CAP_L);
        __builtin_amdgcn_s_sleep(1);
      }
      float M = -INFINITY;
#pragma unroll
      for (int i = 0; i < 4; i++) M = fmaxf(M, fmaxf(hi16(m0[i]), hi16(m1[i])));
      float p[4];
#pragma unroll
      for (int i = 0; i < 4; i++) p[i] = __builtin_amdgcn_exp2f((S[i] - M) * LOG2E);
      float lsum = (p[0] + p[1]) + (p[2] + p[3]);
      lsum += dpp<QX1>(lsum); lsum += dpp<QX2>(lsum);
      lsum = xrow_sum(lsum);
      LDSV(unsigned, L_LW + (c >> 2) * 32 + kv * 4) = __float_as_uint(lsum) | 1u;
#pragma unroll
      for (int u = 0; u < 2; u++) { const unsigned pk = cvtpk(p[2 * u], p[2 * u + 1]); P[u] = u32x4{pk, dppu<QB1>(pk), dppu<QB2>(pk), dppu<QB3>(pk)}; }
    }
    unsigned mska = 0xffu;
    asm volatile("" : "+v"(mska), "+v"(P[0]), "+v"(P[1]));
    SB();
#pragma unroll
    for (int i = SMPOS; i < PVPOS; i++) {
      if (i == ORGPOS) tgt += ((int)__builtin_amdgcn_readfirstlane(LDSV(unsigned, F_PF)) >> 16) - 6;
      VLOAD(i);
    }
    f32x4 O[8];
#pragma unroll
    for (int e = 0; e < 8; e++) {
      u32x4 f;
#pragma unroll
      for (int j = 0; j < 4; j++) f[j] = kpair(V[j][e >> 1], V[4 + j][e >> 1], e & 1);
      O[e] = mfma32(P[0], f, f32x4{0.f, 0.f, 0.f, 0.f});
    }
    asm volatile("" : "+v"(O[0]), "+v"(O[1]), "+v"(O[2]), "+v"(O[3]), "+v"(O[4]), "+v"(O[5]), "+v"(O[6]), "+v"(O[7]));
    SB();
    const unsigned place = wait_place(lds);
    const int side = ((place >> kvh) & 1) * 8192;
    int oaoff = WS_OA + (slot >> 2) * 16384 + side + (slot & 3) * 2048 + kv * 512 + ((lane >> 3) & 3) * 128 + (lane >> 5) * 64 + (lane & 7) * 8;
    OPQ(oaoff);
    const int soff = WS_ST + (partner(place, kvh) * 32 + (lane & 31)) * 128 + (gi >> 3) * 16;
    u32x4 s0 = {0u, ~T, 0u, 0u};
    if (kv == 5 && lane < 32) s0 = LD128(rws, soff, 0, SC1);
    SB();
    int redw = kv * 2560 + lane * 4, redw2 = redw + 1280, redr = (lane >> 5) * 10240 + ((lane & 31) >> 2) * 320 + kv * 64 + (lane & 3) * 16;
    unsigned tab = T >> ((lane & 3) * 8);
    asm volatile("" : "+v"(redw), "+v"(redw2), "+v"(redr), "+v"(tab));
#pragma unroll
    for (int i = PVPOS; i < 16; i++) VLOAD(i);
    {
      u32x4 F[8];
#pragma unroll
      for (int j = 0; j < 3; j++) {
#pragma unroll
        for (int e = 0; e < 8; e++) F[e][j] = kpair(V[8 + j][e >> 1], V[12 + j][e >> 1], e & 1);
        SB();
      }
#pragma unroll
      for (int i = 0; i < 4; i++) {
        F[2 * i][3] = kpair(V[11][i], V[15][i], 0); F[2 * i + 1][3] = kpair(V[11][i], V[15][i], 1);
        O[2 * i] = mfma32(P[1], F[2 * i], O[2 * i]); O[2 * i + 1] = mfma32(P[1], F[2 * i + 1], O[2 * i + 1]);
        SB();
      }
    }
#pragma unroll
    for (int e = 0; e < 4; e++) *(LAS float*)(lds + L_RED + redw + e * 320) = O[e][0];
#pragma unroll
    for (int e = 0; e < 4; e++) *(LAS float*)(lds + L_RED + redw2 + e * 320) = O[4 + e][0];
    CBAR();
    LDSV(unsigned char, F_PV + kv) = 1;
    if (kv < 4) {
      f32x4 r[4];
      unsigned spin = 0;
      NOUNROLL for (;;) {
        const u32x2 cnt = LDSV(u32x2, F_PV);
#pragma unroll
        for (int i = 0; i < 4; i++) r[i] = LDSV(f32x4, L_RED + redr + i * 2560);
        if ((cnt[0] & cnt[1]) == 0x01010101u) break;
        GIVEUP(spin, CAP_L);
      }
      const f32x4 a = (r[0] + r[1]) + (r[2] + r[3]);
      ST64(u32x2{tagm(fold32(a[0], a[2]), mska, tab), __float_as_uint(fold32(a[1], a[3]))}, rws, oaoff, 0, SC1);
    }
    if (kv == 5) {
      if (lane < 32) {
        unsigned spin = 0;
        NOUNROLL while (!none(((s0[1] ^ T) | (s0[3] ^ T)) != 0u)) {
          GIVEUP(spin, CAP_G);
          __builtin_amdgcn_s_sleep(2);
          CBAR();
          s0 = LD128(rws, soff, 0, SC1);
        }
        const float m1 = __uint_as_float(s0[0]);
        float MM = fmaxf(m1, dpp<QX1>(m1));
        MM = fmaxf(MM, dpp<QX2>(MM)); MM = fmaxf(MM, dpp<HMIR>(MM));
        const float e1 = __builtin_amdgcn_exp2f((m1 - MM) * LOG2E);
        float L = e1 * __uint_as_float(s0[2]);
        L += dpp<QX1>(L); L += dpp<QX2>(L); L += dpp<HMIR>(L);
        LDSV(float, L_WT + lane * 4) = e1 * __builtin_amdgcn_rcpf(L);
      }
      LDSV(unsigned, F_W) = 1u;
    }
    if (kv >= 6 && kvh == (int)((place >> 9) & 7u)) {
      const int lh = kv - 6, pg = lane >> 3;
      const int oi = (gi >> 3) * 512 + (gi & 7) * 64 + lh * 32 + (c & 7) * 4 + g;
      const i32x4 ro = ka[9];
      float xfin = lo16(LD16(rx, oi * 2, 0, 0));
      int go[4];
#pragma unroll
      for (int i = 0; i < 4; i++) go[i] = WS_PB + ((gi & 7) * 4 + i) * 16384 + side + pg * 1024 + (((gi >> 3) * 2 + lh + 2 * i + (pg >> 2)) & 7) * 128 + (lane & 7) * 16;
      int ooff = c < 8 ? oi * 2 : OOB;
      unsigned ta = (T >> ((lane & 1) * 16)) & 0xffu, tb = (T >> ((lane & 1) * 16 + 8)) & 0xffu;
      asm volatile("" : "+v"(xfin), "+v"(go[0]), "+v"(go[1]), "+v"(go[2]), "+v"(go[3]), "+v"(ooff), "+v"(ta), "+v"(tb));
      lds_wait_eq(lds, F_T, 4u);
      __builtin_amdgcn_s_sleep(PREB);
      u32x4 pb[4];
      unsigned spin = 0;
      NOUNROLL for (;;) {
        CBAR();
#pragma unroll
        for (int i = 0; i < 4; i++) pb[i] = LD128(rws, go[i], 0, SC1);
        unsigned bad = 0;
#pragma unroll
        for (int i = 0; i < 4; i++) { pb[i][0] ^= ta; pb[i][2] ^= tb; bad |= pb[i][0] | pb[i][2]; }
        if (none((bad & 0xffu) != 0)) break;
        GIVEUP(spin, CAP_G);
        __builtin_amdgcn_s_sleep(SLB);
      }
      const f32x4 v = (F4(pb[0]) + F4(pb[1])) + (F4(pb[2]) + F4(pb[3]));
      float z = fold16(fold32(v[0], v[2]), fold32(v[1], v[3]));
      z += dpp<ROR8>(z);
      z += xfin;
      ST16((u16)cvtpk(z, z), ro, ooff, 0, SC1);
      if (gi == 0 && lh == 0 && lane == 0) ST32(T, rws, WS_EPOCH, 0, SC1);
    }
    return;
  }

  __builtin_amdgcn_s_setprio(3);
  const unsigned ep = LD32(rws, WS_EPOCH, 0, SC1);
  const int hl = gi >> 3, ng = gi & 7;
  const int task = gi >> 1, kh = gi & 1;
  const unsigned tstart = (unsigned)__builtin_amdgcn_s_memrealtime();
  if (task < 15) {
    const bool isq = task < 8;
    u32x4 xs[4], xf, W[8];
#pragma unroll
    for (int i = 0; i < 4; i++) xs[i] = LD128(rx, (c >> 2) * 1024 + g * 64 + (c & 3) * 16 + i * 256, 0, 0);
    xf = LD128(rx, (c >> 2) * 1024 + (c & 1) * 64 + g * 16, kh * 512 + w * 128, 0);
    const int kvq = (kvh + 1) & 7, idx = kvh * 7 + task - 8, mat = idx >= 28 ? 1 : 0;
    const int cb = isq ? kvq * 8 + task : idx - 28 * mat;
    const int rstride = isq ? 8192 : FF * 2;
    const i32x4 rw = {isq ? KB[0] : mat ? KB[8] : KB[4], isq ? KB[1] : mat ? KB[9] : KB[5], isq ? KB[2] : KB[6], KB[3]};
    const int woff = ((c >> 3) * 32 + g * 8) * rstride + (c & 7) * 16;
    int wso = (kh * 256 + w * 64) * rstride + cb * 128;
    unsigned tg = tstart + GOFF + (w * GTAU) / 4;
#pragma unroll
    for (int j = 0; j < 8; j++) {
      if (!isq) pace(tg, GTAU);
      W[j] = LD128(rw, woff, wso, WAUX);
      wso += rstride;
      SB();
    }
    float sm = 0.f;
#pragma unroll
    for (int i = 0; i < 4; i++)
#pragma unroll
      for (int j = 0; j < 4; j++) sm = dot2(xs[i][j], 0x3f803f80u, sm);
    sm += dpp<QX1>(sm); sm += dpp<QX2>(sm);
    const float mean = xrow_sum(sm) * (1.f / DM);
    f32x2 ssv = {0.f, 0.f};
#pragma unroll
    for (int i = 0; i < 4; i++)
#pragma unroll
      for (int j = 0; j < 4; j++) { const f32x2 d = f32x2{lo16(xs[i][j]), hi16(xs[i][j])} - mean; ssv += d * d; }
    float ss = ssv[0] + ssv[1];
    ss += dpp<QX1>(ss); ss += dpp<QX2>(ss);
    const float var = rbf(xrow_sum(ss) * (1.f / DM)), mb = rbf(mean);
    const float den = rbf(__builtin_amdgcn_sqrtf(rbf(var + 1e-5f)));
    const float rden = __builtin_amdgcn_rcpf(den);
#pragma unroll
    for (int j = 0; j < 4; j++) {
      const unsigned d = cvtpk(lo16(xf[j]) - mb, hi16(xf[j]) - mb);
      const f32x2 n = {lo16(d), hi16(d)};
      const f32x2 q = n * rden;
      const f32x2 r = (n - q * den) * rden + q;
      xf[j] = cvtpk(r[0], r[1]);
    }
    SB();
    f32x4 acc[8];
#pragma unroll
    for (int e = 0; e < 8; e++) {
      u32x4 f;
#pragma unroll
      for (int j = 0; j < 4; j++) f[j] = kpair(W[2 * j][e >> 1], W[2 * j + 1][e >> 1], e & 1);
      acc[e] = mfma32(xf, f, f32x4{0.f, 0.f, 0.f, 0.f});
    }
    SB();
    f32x4 o, z;
#pragma unroll
    for (int i = 0; i < 4; i++) {
      const float a = acc[i][0] + dpp<ROR8>(acc[i][1]), bq = acc[4 + i][1] + dpp<ROR8>(acc[4 + i][0]);
      o[i] = c < 8 ? a : bq;
    }
    if (meet4(meet_prep(lds, L_PR, F_P, w, lane), o, z)) {
      const unsigned t0 = (__builtin_amdgcn_readfirstlane(ep) + 0x01010101u) >> ((c >> 3) * 16);
      const u32x4 pv = {tagm(z[0], 0xfu, t0), tagm(z[1], 0xfu, t0 >> 4), tagm(z[2], 0xfu, t0 >> 8), tagm(z[3], 0xfu, t0 >> 12)};
      const int col = (c & 7) * 8 + (c >> 3) * 4;
      const int poff = isq ? WS_Q + (((kvq * 4 + g) * 2 + kh) * 512 + task * 64 + col) * 4 : WS_GU + (((mat * 2 + kh) * 4 + g) * FF + cb * 64 + col) * 4;
      ST128(pv, rws, poff, 0, QS);
    }
  }
  const unsigned T = __builtin_amdgcn_readfirstlane(ep) + 0x01010101u;
  asm volatile("" : "+v"(pfd) : "s"(T) : "memory");
  LDSV(unsigned char, F_E + w) = 1;
  const int bb = c >> 2, q4 = c & 3, sh = g >> 1;
  if (w >= 2) {
    int hh3 = 0;
    if (w == 3) hh3 = partner(wait_place(lds), kvh) * 4 + hl;
    const int goff = w == 2 ? WS_Q + (kvh * 4 + b) * 4096 + lane * 16 : WS_GU + (g * FF + hh3 * 56 + (c < 14 ? c : 13) * 4) * 4;
    const int st1 = w == 2 ? 2048 : 4 * FF * 4, st2 = w == 2 ? 1024 : 8 * FF * 4;
    const unsigned tn = T >> (goff & 16);
    const u32x4 TN = {tn & 0xfu, (tn >> 4) & 0xfu, (tn >> 8) & 0xfu, (tn >> 12) & 0xfu};
    u32x4 q[4];
    lds_wait_byte(lds, F_G);
    unsigned spin = 0;
    NOUNROLL for (;;) {
      CBAR();
      q[0] = LD128(rws, goff, 0, QL); q[1] = LD128(rws, goff, st1, QL); q[2] = LD128(rws, goff, st2, QL); q[3] = LD128(rws, goff, st1 + st2, QL);
      unsigned bad = 0;
#pragma unroll
      for (int i = 0; i < 4; i++) { q[i] ^= TN; bad |= (q[i][0] | q[i][1]) | (q[i][2] | q[i][3]); }
      if (none((bad & 0xfu) != 0)) break;
      GIVEUP(spin, CAP_G);
      __builtin_amdgcn_s_sleep(8);
    }
    const f32x4 y0 = F4(q[0]) + F4(q[1]), y1 = F4(q[2]) + F4(q[3]);
    const u32x2 z0 = {cvtpk(y0[0], y0[1]), cvtpk(y0[2], y0[3])}, z1 = {cvtpk(y1[0], y1[1]), cvtpk(y1[2], y1[3])};
    if (w == 2) {
      LDSV(u32x2, L_Q + lane * 8) = z0;
      LDSV(u32x2, L_Q + 512 + lane * 8) = z1;
      lds_wait_eq(lds, F_E, 0x01010101u);
      LDSV(u32x2, F_Q) = u32x2{1u, T};
    } else {
      u32x2 hp;
#pragma unroll
      for (int i = 0; i < 2; i++) hp[i] = c < 14 ? cvtpk(lo16(z0[i]) * lo16(z1[i]), hi16(z0[i]) * hi16(z1[i])) : 0u;
      LDSV(u32x2, L_H + g * 128 + c * 8) = hp;
      LDSV(unsigned, F_H) = 1u;
    }
  } else if (w == 0) {
    unsigned lv, spin = 0;
    NOUNROLL for (;;) {
      lv = LDSV(unsigned, L_LW + (lane & 31) * 4);
      if (none(lv == 0u)) break;
      GIVEUP(spin, CAP_L);
      __builtin_amdgcn_s_sleep(2);
    }
    float mv = hi16(LDSV(unsigned, L_MW + (lane & 31) * 4));
    float lsum = __uint_as_float(lv);
    lsum += dpp<QX1>(lsum); lsum += dpp<QX2>(lsum); lsum += dpp<HMIR>(lsum);
    mv = fmaxf(mv, dpp<QX1>(mv)); mv = fmaxf(mv, dpp<QX2>(mv)); mv = fmaxf(mv, dpp<HMIR>(mv));
    const u32x4 st = {__float_as_uint(mv), T, __float_as_uint(lsum), T};
    if ((lane & 39) == 0) ST128(st, rws, WS_ST + slot * 128 + (lane >> 3) * 16, 0, SC1);
  } else {
    if (gi == 31) {
      if (lane < 16) ST32(T, rws, WS_PR + (lane >> 3) * 8192 + kvh * 1024 + (lane & 7) * 128, 0, SC1);
      asm volatile("s_waitcnt vmcnt(0)" ::: "memory");
      unsigned best0 = ~0u, best1 = ~0u;
      NOUNROLL for (int i = 0; i < 8; i++) {
        unsigned long long c0, c1; unsigned d;
        asm volatile("s_memtime %0\n\ts_waitcnt lgkmcnt(0)\n\ts_buffer_load_dword %2, %3, %4\n\ts_waitcnt lgkmcnt(0)\n\ts_memtime %1\n\ts_waitcnt lgkmcnt(0)"
                     : "=&s"(c0), "=&s"(c1), "=&s"(d) : "s"(rws), "s"(WS_PR + (i & 1) * 8192 + kvh * 1024 + (i >> 1) * 128) : "memory");
        const unsigned dt = (unsigned)c1 - (unsigned)c0;
        if (i >= 2) { if (i & 1) best1 = min(best1, dt); else best0 = min(best0, dt); }
      }
      const unsigned xcc = __builtin_amdgcn_s_getreg(20 | (3 << 11)) & 7u;
      if (lane == 0) ST64(u32x2{T, (tstart << 4) | (xcc << 1) | (best1 < best0 ? 1u : 0u)}, rws, WS_PF + kvh * 8, 0, SC1);
    }
    lds_wait_byte(lds, F_G + 1);
    if (lane < 8) {
      u32x2 pf;
      unsigned spin = 0;
      NOUNROLL for (;;) {
        CBAR();
        pf = LD64(rws, WS_PF + lane * 8, 0, SC1);
        if (none(pf[0] != T)) break;
        GIVEUP(spin, CAP_G);
        __builtin_amdgcn_s_sleep(8);
      }
      unsigned kx = ((pf[1] & 14u) << 2) | lane;
      kx = min(kx, dppu<QX1>(kx)); kx = min(kx, dppu<QX2>(kx)); kx = min(kx, dppu<HMIR>(kx));
      unsigned dl = (unsigned)min(max(((int)(pf[1] - (tstart << 4))) >> 4, -100), 100);
      dl += dppu<QX1>(dl); dl += dppu<QX2>(dl); dl += dppu<HMIR>(dl);
      LDSV(unsigned, F_PF) = (unsigned)__builtin_amdgcn_ballot_w64(pf[1] & 1u) | 0x100u | ((kx & 7u) << 9) | ((unsigned)((int)dl >> 3) << 16);
    }
  }
  const unsigned place = wait_place(lds);
  const int kva = partner(place, kvh), hh = kva * 4 + hl;
  const i32x4 rwo = {KB[12], KB[13], KB[14], KB[15]}, rwd = ka[8];
  const int wos = (hh * 128 + w * 2) * 1024 + ng * 128, wds = (hh * 56 + w * 16) * 1024 + ng * 128;
  int aoff = WS_OA + ((kva * 4 + bb) * 2 + sh) * 16384 + ((place >> kvh) & 1) * 8192 + hl * 512 + w * 128 + (g & 1) * 64 + q4 * 16;
  int pboff = WS_PB + (ng * 4 + (hh >> 3)) * 16384 + ((place >> ((place >> 9) & 7)) & 1) * 8192 + (hh & 7) * 1024 + ((lane * 16 + (hh >> 2) * 128) & 1023);
  int wooff = g * 16384 + c * 8;
  int wdoff = (w == 3 && g >= 2) ? OOB - 4096 : g * 4096 + c * 8;
  unsigned ta = (T >> ((lane & 1) * 16)) & 0xffu, tb = (T >> ((lane & 1) * 16 + 8)) & 0xffu, mska = 0xffu;
  asm volatile("" : "+v"(ta), "+v"(tb));
  asm volatile("" : "+v"(aoff), "+v"(pboff), "+v"(wooff), "+v"(wdoff), "+v"(mska));
  lds_wait8(lds, F_VI);
  const unsigned tvi = __builtin_amdgcn_readfirstlane(LDSV(unsigned, F_TV));
  u32x2 WD[4], WO[8];
  {
    unsigned tw = tvi + WDEL + (w * WTAU) / 4;
#pragma unroll
    for (int j = 0; j < 4; j++) { pace(tw, WTAU); WD[j] = LD64(rwd, wdoff + j * 1024, wds, OAUX); SB(); }
#pragma unroll
    for (int j = 0; j < 8; j++) { pace(tw, WTAU); WO[j] = LD64(rwo, wooff + (j >> 2) * 1024, wos + (j & 1) * 8192 + ((j >> 1) & 1) * 65536, OAUX); SB(); }
  }
  lds_wait_eq(lds, F_H, 1u);
  const u32x2 hf = LDSV(u32x2, L_H + bb * 128 + w * 32 + g * 8);
  lds_wait8(lds, F_PV);
  __builtin_amdgcn_s_sleep(4);
  { unsigned tA = tvi + TA_ADD; pace(tA, 0); }
  u32x4 a[4];
#pragma unroll
  for (int i = 0; i < 4; i++) a[i] = LD128(rws, aoff, i * 2048, SC1);
  SB();
  u32x4 fo[4];
  f32x4 acc[4];
#pragma unroll
  for (int e = 0; e < 4; e++) {
#pragma unroll
    for (int j = 0; j < 4; j++) fo[e][j] = kpair(WO[2 * j][e >> 1], WO[2 * j + 1][e >> 1], e & 1);
    acc[e] = mfma16(hf, u32x2{kpair(WD[0][e >> 1], WD[1][e >> 1], e & 1), kpair(WD[2][e >> 1], WD[3][e >> 1], e & 1)}, f32x4{0.f, 0.f, 0.f, 0.f});
  }
  asm volatile("" : "+v"(acc[0]), "+v"(acc[1]), "+v"(acc[2]), "+v"(acc[3]), "+v"(fo[0]), "+v"(fo[1]), "+v"(fo[2]), "+v"(fo[3]));
  const Meet mt = meet_prep(lds, L_TR, F_T, w, lane);
  lds_wait_eq(lds, F_W, 1u);
  f32x4 wt = LDSV(f32x4, L_WT + bb * 32 + sh * 16);
  OPQ(wt);
  {
    unsigned spin = 0;
    NOUNROLL for (;;) {
      unsigned bad = 0;
#pragma unroll
      for (int i = 0; i < 4; i++) { a[i][0] ^= ta; a[i][2] ^= tb; bad |= a[i][0] | a[i][2]; }
      if (none((bad & 0xffu) != 0)) break;
      GIVEUP(spin, CAP_G);
      __builtin_amdgcn_s_sleep(SLA);
      CBAR();
#pragma unroll
      for (int i = 0; i < 4; i++) a[i] = LD128(rws, aoff, i * 2048, SC1);
    }
  }
  f32x4 t = F4(a[0]) * wt[0];
#pragma unroll
  for (int i = 1; i < 4; i++) t = __builtin_elementwise_fma(F4(a[i]), f32x4{wt[i], wt[i], wt[i], wt[i]}, t);
  const unsigned own = cvtpk(fold32(t[0], t[2]), fold32(t[1], t[3]));
  const u32x4 A = {own, dppu<QB1>(own), dppu<QB2>(own), dppu<QB3>(own)};
#pragma unroll
  for (int e = 0; e < 4; e++) acc[e] = mfma32(A, fo[e], acc[e]);
  SB();
  f32x4 z;
  if (meet4(mt, f32x4{acc[0][0], acc[1][0], acc[2][0], acc[3][0]}, z)) {
    const u32x4 o = {tagm(z[0], mska, ta), __float_as_uint(z[1]), tagm(z[2], mska, tb), __float_as_uint(z[3])};
    ST128(o, rws, pboff, 0, SC1);
  }
}

extern "C" void kernel_launch(void* const* d_in, const int* in_sizes, int n_in, void* d_out, int out_size, void* d_ws, size_t ws_size, hipStream_t stream) {
  const unsigned fl = 0x00020000u;
  fused_decode<<<256, NTHR, 0, stream>>>(d_in[1], 33554432u, fl, d_in[2], 33554432u, fl, d_ws, (unsigned)WS_END, fl, d_in[0], 4096u, fl,
                                         d_in[3], 4194304u, fl, d_in[6], 1835008u, fl, d_in[7], 1835008u, fl, d_in[4], 4194304u, fl,
                                         d_in[8], 1835008u, fl, d_out, 4096u, fl);
}
```

```cpp
#include <hip/hip_runtime.h>
#include <stdint.h>

typedef unsigned short u16;
typedef int i32x4 __attribute__((ext_vector_type(4)));
typedef int i32x16 __attribute__((ext_vector_type(16)));
typedef unsigned u32x2 __attribute__((ext_vector_type(2)));
typedef unsigned u32x4 __attribute__((ext_vector_type(4)));
typedef float f32x2 __attribute__((ext_vector_type(2)));
typedef float f32x4 __attribute__((ext_vector_type(4)));
typedef short s16x4 __attribute__((ext_vector_type(4)));
typedef short s16x8 __attribute__((ext_vector_type(8)));
typedef __bf16 bf16x2 __attribute__((ext_vector_type(2)));
#define LAS __attribute__((address_space(3)))

__device__ u16 LD16(i32x4 r, int v, int s, int a) __asm("llvm.amdgcn.raw.buffer.load.i16");
__device__ unsigned LD32(i32x4 r, int v, int s, int a) __asm("llvm.amdgcn.raw.buffer.load.i32");
__device__ u32x2 LD64(i32x4 r, int v, int s, int a) __asm("llvm.amdgcn.raw.buffer.load.v2i32");
__device__ u32x4 LD128(i32x4 r, int v, int s, int a) __asm("llvm.amdgcn.raw.buffer.load.v4i32");
__device__ void ST16(u16 d, i32x4 r, int v, int s, int a) __asm("llvm.amdgcn.raw.buffer.store.i16");
__device__ void ST32(unsigned d, i32x4 r, int v, int s, int a) __asm("llvm.amdgcn.raw.buffer.store.i32");
__device__ void ST64(u32x2 d, i32x4 r, int v, int s, int a) __asm("llvm.amdgcn.raw.buffer.store.v2i32");
__device__ void ST128(u32x4 d, i32x4 r, int v, int s, int a) __asm("llvm.amdgcn.raw.buffer.store.v4i32");
constexpr int NTL = 2, SC1 = 16;

constexpr int DM = 512, FF = 1792;
constexpr int NT = 4, NTHR = (NT + 8) * 64;

constexpr int WIN = 8;
constexpr int N0 = 2;
constexpr int TOFF = 30, TAU1 = 360, I1 = 8, TAU = 270;
constexpr int W0 = 2, DEL = 40;
constexpr int REBASE = -45;
constexpr int QGPOS = 12, HGPOS = 18;
constexpr int QKPOS = 4, SMPOS = 7, ORGPOS = 8, PVPOS = 14;
constexpr int GOFF = 40, GTAU = 30;
constexpr int WDEL = 8, WTAU = 6;
constexpr int TA_ADD = 125;
constexpr int CODE_BYTES = 12288;
constexpr int sched(int i) { return TOFF + ((i < I1 ? i - N0 + 1 : I1 - N0) * TAU1 + (i < I1 ? 0 : i - I1 + 1) * TAU) / 10; }

constexpr int WS_EPOCH = 0;
constexpr int WS_PF = 1024;
constexpr int WS_Q = 4096;
constexpr int WS_GU = WS_Q + 131072;
constexpr int WS_ST = WS_GU + 114688;
constexpr int WS_PR = 294912;
constexpr int WS_OA = WS_PR + 16384;
constexpr int WS_PB = WS_OA + 64 * 16384;
constexpr int WS_END = WS_PB + 32 * 16384;
static_assert(WS_ST + 32768 <= WS_PR && WS_PR % 16384 == 0, "map");
constexpr int L_RED = 0;
constexpr int L_TR = L_RED + 20480;
constexpr int L_PR = L_TR + 4096;
constexpr int L_Q = L_PR + 4096;
constexpr int L_H = L_Q + 1024;
constexpr int L_WT = L_H + 512;
constexpr int L_MW = L_WT + 128;
constexpr int L_LW = L_MW + 128;
constexpr int L_FLAG = L_LW + 128;
constexpr int F_Q = L_FLAG, F_H = L_FLAG + 8, F_E = L_FLAG + 12, F_VI = L_FLAG + 16, F_PV = L_FLAG + 24, F_T = L_FLAG + 32, F_P = L_FLAG + 36,
              F_TV = L_FLAG + 48, F_W = L_FLAG + 52, F_G = L_FLAG + 56, F_PF = L_FLAG + 60;
constexpr int L_END = L_FLAG + 64;
constexpr unsigned CAP_G = 1u << 18, CAP_L = 1u << 22;
constexpr int OOB = 0x7ffffff0;
constexpr float LOG2E = 1.4426950408889634f;

__device__ __forceinline__ float lo16(unsigned u) { return __uint_as_float(u << 16); }
__device__ __forceinline__ float hi16(unsigned u) { return __uint_as_float(u & 0xffff0000u); }
__device__ __forceinline__ unsigned cvtpk(float lo, float hi) { f32x2 f = {lo, hi}; return __builtin_bit_cast(unsigned, __builtin_convertvector(f, bf16x2)); }
__device__ __forceinline__ float rbf(float f) { return lo16(cvtpk(f, f)); }
__device__ __forceinline__ f32x4 mfma32(u32x4 a, u32x4 b, f32x4 c) { return __builtin_amdgcn_mfma_f32_16x16x32_bf16(__builtin_bit_cast(s16x8, a), __builtin_bit_cast(s16x8, b), c, 0, 0, 0); }
__device__ __forceinline__ f32x4 mfma16(u32x2 a, u32x2 b, f32x4 c) { return __builtin_amdgcn_mfma_f32_16x16x16bf16_1k(__builtin_bit_cast(s16x4, a), __builtin_bit_cast(s16x4, b), c, 0, 0, 0); }
template <int CTRL> __device__ __forceinline__ float dpp(float x) { return __builtin_bit_cast(float, __builtin_amdgcn_mov_dpp(__builtin_bit_cast(int, x), CTRL, 0xf, 0xf, true)); }
template <int CTRL> __device__ __forceinline__ unsigned dppu(unsigned x) { return (unsigned)__builtin_amdgcn_mov_dpp((int)x, CTRL, 0xf, 0xf, true); }
constexpr int ROR8 = 0x128, HMIR = 0x141, QX1 = 0xB1, QX2 = 0x4E, QB0 = 0x00, QB1 = 0x55, QB2 = 0xAA, QB3 = 0xFF;
__device__ __forceinline__ float fold32(float a, float b) {
  auto t = __builtin_amdgcn_permlane32_swap(__float_as_uint(a), __float_as_uint(b), false, false);
  return __uint_as_float(t[0]) + __uint_as_float(t[1]);
}
__device__ __forceinline__ float fold16(float a, float b) {
  auto t = __builtin_amdgcn_permlane16_swap(__float_as_uint(a), __float_as_uint(b), false, false);
  return __uint_as_float(t[0]) + __uint_as_float(t[1]);
}
__device__ __forceinline__ float xrow_sum(float x) { x = fold16(x, x); return fold32(x, x); }
__device__ __forceinline__ float xrow_max(float x) {
  auto s = __builtin_amdgcn_permlane16_swap(__float_as_uint(x), __float_as_uint(x), false, false);
  x = fmaxf(__uint_as_float(s[0]), __uint_as_float(s[1]));
  auto t = __builtin_amdgcn_permlane32_swap(__float_as_uint(x), __float_as_uint(x), false, false);
  return fmaxf(__uint_as_float(t[0]), __uint_as_float(t[1]));
}
__device__ __forceinline__ float dot2(unsigned a, unsigned b, float c) { return __builtin_amdgcn_fdot2_f32_bf16(__builtin_bit_cast(bf16x2, a), __builtin_bit_cast(bf16x2, b), c, false); }
__device__ __forceinline__ unsigned tagm(float v, unsigned m, unsigned t) { return (__float_as_uint(v) & ~m) | (t & m); }
__device__ __forceinline__ unsigned kpair(unsigned r0, unsigned r1, int sel) { return __builtin_amdgcn_perm(r1, r0, sel ? 0x07060302u : 0x05040100u); }
__device__ __forceinline__ bool none(bool p) { return __builtin_amdgcn_ballot_w64(p) == 0ull; }
#define F4(v) __builtin_bit_cast(f32x4, v)
#define SB() __builtin_amdgcn_sched_barrier(0)
#define CBAR() asm volatile("" ::: "memory")
#define OPQ(x) asm volatile("" : "+v"(x))
#define LDSV(T, off) (*(volatile LAS T*)(lds + (off)))
#define NOUNROLL _Pragma("clang loop unroll(disable)")
#define GIVEUP(spin, cap) asm volatile("s_add_u32 %0, %0, 1\n\ts_cmp_lt_u32 %0, %1\n\ts_cbranch_scc1 1f\n\ts_endpgm\n1:" : "+s"(spin) : "n"(cap) : "scc")

__device__ __forceinline__ unsigned now32() { unsigned long long t; asm volatile("s_memrealtime %0\n\ts_waitcnt lgkmcnt(0)" : "=s"(t)::"memory"); return (unsigned)t; }
__device__ __forceinline__ void pace(unsigned& tgt, int step) {
  asm volatile("1:\n\ts_memrealtime vcc\n\ts_waitcnt lgkmcnt(0)\n\ts_sub_u32 vcc_lo, vcc_lo, %0\n\ts_cmp_lt_i32 vcc_lo, 0\n\ts_cbranch_scc1 1b\n\ts_add_u32 %0, %0, %1"
               : "+s"(tgt) : "n"(step) : "memory", "scc", "vcc");
}
__device__ __forceinline__ void apace(unsigned& tgt, int step) {
  unsigned tmp;
  asm volatile("1:\n\ts_memrealtime vcc\n\ts_getreg_b32 %1, hwreg(HW_REG_IB_STS, 0, 4)\n\ts_sub_i32 %1, %1, %3\n\ts_max_i32 %1, %1, 0\n\ts_mul_i32 %1, %1, %4\n\t"
               "s_waitcnt lgkmcnt(0)\n\ts_sub_u32 vcc_lo, vcc_lo, %0\n\ts_cmp_lt_i32 vcc_lo, %1\n\ts_cbranch_scc1 1b\n\ts_add_u32 %0, %0, %2"
               : "+s"(tgt), "=&s"(tmp) : "n"(step), "n"(W0 - 1), "n"(DEL) : "memory", "scc", "vcc");
}
__device__ __forceinline__ void lds_wait_eq(LAS unsigned char* lds, int off, unsigned n) {
  unsigned spin = 0;
  NOUNROLL while (LDSV(unsigned, off) != n) { GIVEUP(spin, CAP_L); __builtin_amdgcn_s_sleep(1); }
  CBAR();
}
__device__ __forceinline__ void lds_wait_byte(LAS unsigned char* lds, int off) {
  unsigned spin = 0;
  NOUNROLL while (LDSV(unsigned char, off) == 0) { GIVEUP(spin, CAP_L); __builtin_amdgcn_s_sleep(2); }
  CBAR();
}
__device__ __forceinline__ void lds_wait8(LAS unsigned char* lds, int off) {
  unsigned spin = 0;
  NOUNROLL for (;;) {
    const u32x2 f = LDSV(u32x2, off);
    if ((f[0] & f[1]) == 0x01010101u) break;
    GIVEUP(spin, CAP_L);
    __builtin_amdgcn_s_sleep(1);
  }
  CBAR();
}
struct Meet { unsigned wr, rd, cnt, one; };
__device__ __forceinline__ Meet meet_prep(LAS unsigned char* lds, int area, int cnt, int w, int lane, int wpos) {
  Meet m = {(unsigned)(uintptr_t)(lds + area) + w * 1024 + wpos * 16, (unsigned)(uintptr_t)(lds + area) + lane * 16, (unsigned)(uintptr_t)(lds + cnt), 1u};
  asm volatile("" : "+v"(m.wr), "+v"(m.rd), "+v"(m.cnt), "+v"(m.one));
  return m;
}
__device__ __forceinline__ bool meet4(const Meet& m, f32x4 v, f32x4& sum) {
  *(volatile LAS f32x4*)(uintptr_t)m.wr = v;
  unsigned prev;
  f32x4 r0, r1, r2, r3;
  asm volatile("s_mov_b64 exec, 1\n\tds_add_rtn_u32 %0, %6, %7\n\ts_mov_b64 exec, -1\n\t"
               "ds_read_b128 %1, %5\n\tds_read_b128 %2, %5 offset:1024\n\tds_read_b128 %3, %5 offset:2048\n\tds_read_b128 %4, %5 offset:3072\n\t"
               "s_waitcnt lgkmcnt(0)"
               : "=&v"(prev), "=&v"(r0), "=&v"(r1), "=&v"(r2), "=&v"(r3) : "v"(m.rd), "v"(m.cnt), "v"(m.one) : "memory");
  sum = (r0 + r1) + (r2 + r3);
  return __builtin_amdgcn_readfirstlane(prev) == 3u;
}
__device__ __forceinline__ unsigned wait_place(LAS unsigned char* lds) {
  unsigned p = LDSV(unsigned, F_PF), spin = 0;
  NOUNROLL while (p == 0u) { GIVEUP(spin, CAP_L); __builtin_amdgcn_s_sleep(1); p = LDSV(unsigned, F_PF); }
  return __builtin_amdgcn_readfirstlane(p);
}
__device__ __forceinline__ int partner(unsigned place, int g) {
  const unsigned m = (((place >> g) & 1u) ? place : ~place) & 0xffu;
  return (g + 1 + __builtin_ctz(((m | (m << 8)) >> (g + 1)) & 0xffu)) & 7;
}

#define DESC(n) const void* p_##n, unsigned n##_bytes, unsigned n##_flags
__global__ void __launch_bounds__(NTHR, 3) fused_decode(DESC(k), DESC(v), DESC(ws), DESC(x), DESC(wq), DESC(wg), DESC(wu), DESC(wo), DESC(wd), DESC(out)) {
  __shared__ __attribute__((aligned(16))) unsigned char smem[L_END];
  auto ka = (const __attribute__((address_space(4))) i32x4*)__builtin_amdgcn_kernarg_segment_ptr();
  i32x16 KA, KB;
  unsigned long long pc0;
  asm volatile("s_load_dwordx16 %0, %3, 0x0\n\ts_load_dwordx16 %1, %3, 0x40\n\ts_getpc_b64 %2" : "=&s"(KA), "=&s"(KB), "=&s"(pc0) : "s"(ka));
  SB();
  LAS unsigned char* lds = (LAS unsigned char*)smem;
  const int blk = blockIdx.x, kvh = blk & 7, gi = blk >> 3, b = gi & 3, s = gi >> 2;
  const int tid = threadIdx.x, lane = tid & 63, w = __builtin_amdgcn_readfirstlane(tid >> 6), g = lane >> 4, c = lane & 15;
  u32x2 pfd = {0u, 0u};
  if (w == 3 && lane < CODE_BYTES / 256) {
    const int off = gi * (CODE_BYTES / 32) + lane * 8;
    asm volatile("global_load_dwordx2 %0, %1, %2" : "=v"(pfd) : "v"(off), "s"(pc0 & ~0xffull));
  }
  if (w == 2) {
    LDSV(unsigned, L_MW + lane * 4) = 0u;
    LDSV(unsigned, L_FLAG + c * 4) = 0u;
  }
  const int kv = w - NT;
  int koff = (c >> 2) * 256 + g * 64 + (c & 3) * 16;
  int kvbase = ((b * 8 + kvh) * 4096 + s * 512) * 256 + kv * 4096;
  int so0 = kvbase + (gi & 3) * 1024;
  OPQ(koff);
  asm volatile("s_waitcnt lgkmcnt(0)\n\ts_barrier" : "+s"(KA), "+s"(KB), "+s"(kvbase), "+s"(so0)::"memory");
  const i32x4 rws = {KA[8], KA[9], KA[10], KA[11]}, rx = {KA[12], KA[13], KA[14], KA[15]};
  const int slot = (kvh * 4 + b) * 8 + s;

  if (__builtin_expect(w >= NT, 1)) {
    const i32x4 rk = {KA[0], KA[1], KA[2], KA[3]}, rv = {KA[4], KA[5], KA[6], KA[7]};
    u32x4 K[16], V[16];
    const int voff = lane * 16;
#define SOFF(i) (((i) < 4 ? so0 : kvbase + ((((i) >> 2) + gi) & 3) * 1024) + ((i) & 3) * 32768)
#define WAITN(n) asm volatile("s_waitcnt vmcnt(%0)" ::"n"((n) - 1) : "memory")
#define PACE(i) apace(tgt, sched((i) + 1) - sched(i))
#define VLOAD(i) do { WAITN(WIN); PACE(16 + (i)); V[i] = LD128(rv, voff, SOFF(i), NTL); \
    if ((i) + 17 == HGPOS && kv == 7) LDSV(unsigned char, F_G + 1) = 1; \
    if ((i) == 15) { LDSV(unsigned, F_TV) = now32(); LDSV(unsigned char, F_VI + kv) = 1; } SB(); } while (0)
    unsigned long long t64;
    asm volatile("s_memrealtime %0" : "=s"(t64));
#pragma unroll
    for (int i = 0; i < N0; i++) { K[i] = LD128(rk, koff, SOFF(i), NTL); SB(); }
    asm volatile("s_waitcnt lgkmcnt(0)" : "+s"(t64));
    unsigned tgt = (unsigned)t64 + sched(N0) + (kv * TAU) / 80;
    WAITN(N0);
    { const unsigned t1 = now32() + REBASE + (kv * TAU) / 80; if ((int)(t1 - tgt) > 0) tgt = t1; }
#pragma unroll
    for (int i = N0; i < 16; i++) {
      if (i >= WIN) WAITN(WIN);
      PACE(i);
      K[i] = LD128(rk, koff, SOFF(i), NTL);
      if (i + 1 == QGPOS && kv == 7) LDSV(unsigned char, F_G) = 1;
      SB();
    }
#pragma unroll
    for (int i = 0; i < QKPOS; i++) VLOAD(i);
    u32x2 fl = LDSV(u32x2, F_Q);
    { unsigned spin = 0; NOUNROLL while (fl[0] == 0u) { GIVEUP(spin, CAP_L); __builtin_amdgcn_s_sleep(1); fl = LDSV(u32x2, F_Q); } }
    CBAR();
    const unsigned T = __builtin_amdgcn_readfirstlane(fl[1]);
    float S[4];
    {
      const u32x4 qf = LDSV(u32x4, L_Q + koff);
      const bool a1 = c & 1, a2 = c & 2;
#pragma unroll
      for (int t = 0; t < 4; t++) {
        float y[4];
#pragma unroll
        for (int i = 0; i < 4; i++) {
          const f32x4 d = mfma32(K[t * 4 + i], qf, f32x4{0.f, 0.f, 0.f, 0.f});
          float z = dpp<QB0>(d[0]);
          z += dpp<QB1>(d[1]); OPQ(z);
          z += dpp<QB2>(d[2]); OPQ(z);
          z += dpp<QB3>(d[3]); OPQ(z);
          y[i] = z;
        }
        const float u0 = a1 ? y[1] : y[0], u1 = a1 ? y[3] : y[2];
        S[t] = a2 ? u1 : u0;
      }
#pragma unroll
      for (int i = 0; i < 4; i += 2) {
        const unsigned p = cvtpk(S[i], S[i + 1]);
        const unsigned p2 = cvtpk(lo16(p) * 0.08838834764831845f, hi16(p) * 0.08838834764831845f);
        S[i] = lo16(p2); S[i + 1] = hi16(p2);
      }
      float mw = fmaxf(fmaxf(S[0], S[1]), fmaxf(S[2], S[3]));
      mw = fmaxf(mw, dpp<QX1>(mw)); mw = fmaxf(mw, dpp<QX2>(mw));
      mw = xrow_max(mw);
      LDSV(unsigned, L_MW + (c >> 2) * 32 + kv * 4) = __float_as_uint(mw) | 1u;
    }
    SB();
#pragma unroll
    for (int i = QKPOS; i < SMPOS; i++) VLOAD(i);
    u32x4 P[2];
    {
      u32x4 m0, m1;
      unsigned spin = 0;
      NOUNROLL for (;;) {
        m0 = LDSV(u32x4, L_MW + (c >> 2) * 32); m1 = LDSV(u32x4, L_MW + (c >> 2) * 32 + 16);
        const u32x4 mm = m0 & m1;
        if (none((mm[0] & mm[1] & mm[2] & mm[3] & 1u) == 0u)) break;
        GIVEUP(spin, CAP_L);
        __builtin_amdgcn_s_sleep(1);
      }
      float M = -INFINITY;
#pragma unroll
      for (int i = 0; i < 4; i++) M = fmaxf(M, fmaxf(hi16(m0[i]), hi16(m1[i])));
      float p[4];
#pragma unroll
      for (int i = 0; i < 4; i++) p[i] = __builtin_amdgcn_exp2f((S[i] - M) * LOG2E);
      float lsum = (p[0] + p[1]) + (p[2] + p[3]);
      lsum += dpp<QX1>(lsum); lsum += dpp<QX2>(lsum);
      lsum = xrow_sum(lsum);
      LDSV(unsigned, L_LW + (c >> 2) * 32 + kv * 4) = __float_as_uint(lsum) | 1u;
#pragma unroll
      for (int u = 0; u < 2; u++) { const unsigned pk = cvtpk(p[2 * u], p[2 * u + 1]); P[u] = u32x4{pk, dppu<QB1>(pk), dppu<QB2>(pk), dppu<QB3>(pk)}; }
    }
    unsigned mska = 0xffu;
    asm volatile("" : "+v"(mska), "+v"(P[0]), "+v"(P[1]));
    SB();
#pragma unroll
    for (int i = SMPOS; i < PVPOS; i++) {
      if (i == ORGPOS) tgt += ((int)__builtin_amdgcn_readfirstlane(LDSV(unsigned, F_PF)) >> 16) - 6;
      VLOAD(i);
    }
    f32x4 O[8];
#pragma unroll
    for (int e = 0; e < 8; e++) {
      u32x4 f;
#pragma unroll
      for (int j = 0; j < 4; j++) f[j] = kpair(V[j][e >> 1], V[4 + j][e >> 1], e & 1);
      O[e] = mfma32(P[0], f, f32x4{0.f, 0.f, 0.f, 0.f});
    }
    asm volatile("" : "+v"(O[0]), "+v"(O[1]), "+v"(O[2]), "+v"(O[3]), "+v"(O[4]), "+v"(O[5]), "+v"(O[6]), "+v"(O[7]));
    SB();
    const unsigned place = wait_place(lds);
    const int side = ((place >> kvh) & 1) * 8192;
    int oaoff = WS_OA + (slot >> 2) * 16384 + side + (slot & 3) * 2048 + kv * 512 + lane * 8;
    OPQ(oaoff);
    const int soff = WS_ST + (partner(place, kvh) * 32 + (lane & 31)) * 128 + (gi >> 3) * 16;
    u32x4 s0 = {0u, ~T, 0u, 0u};
    if (kv == 5 && lane < 32) s0 = LD128(rws, soff, 0, SC1);
    SB();
    int redw = kv * 2560 + lane * 4, redw2 = redw + 1280, redr = (lane >> 5) * 10240 + ((lane & 31) >> 2) * 320 + kv * 64 + (lane & 3) * 16;
    unsigned tab = T >> ((lane & 3) * 8);
    asm volatile("" : "+v"(redw), "+v"(redw2), "+v"(redr), "+v"(tab));
#pragma unroll
    for (int i = PVPOS; i < 16; i++) VLOAD(i);
    {
      u32x4 F[8];
#pragma unroll
      for (int j = 0; j < 3; j++) {
#pragma unroll
        for (int e = 0; e < 8; e++) F[e][j] = kpair(V[8 + j][e >> 1], V[12 + j][e >> 1], e & 1);
        SB();
      }
#pragma unroll
      for (int i = 0; i < 4; i++) {
        F[2 * i][3] = kpair(V[11][i], V[15][i], 0); F[2 * i + 1][3] = kpair(V[11][i], V[15][i], 1);
        O[2 * i] = mfma32(P[1], F[2 * i], O[2 * i]); O[2 * i + 1] = mfma32(P[1], F[2 * i + 1], O[2 * i + 1]);
        SB();
      }
    }
#pragma unroll
    for (int e = 0; e < 4; e++) *(LAS float*)(lds + L_RED + redw + e * 320) = O[e][0];
#pragma unroll
    for (int e = 0; e < 4; e++) *(LAS float*)(lds + L_RED + redw2 + e * 320) = O[4 + e][0];
    CBAR();
    LDSV(unsigned char, F_PV + kv) = 1;
    if (kv < 4) {
      f32x4 r[4];
      unsigned spin = 0;
      NOUNROLL for (;;) {
        const u32x2 cnt = LDSV(u32x2, F_PV);
#pragma unroll
        for (int i = 0; i < 4; i++) r[i] = LDSV(f32x4, L_RED + redr + i * 2560);
        if ((cnt[0] & cnt[1]) == 0x01010101u) break;
        GIVEUP(spin, CAP_L);
      }
      const f32x4 a = (r[0] + r[1]) + (r[2] + r[3]);
      ST64(u32x2{tagm(fold32(a[0], a[2]), mska, tab), __float_as_uint(fold32(a[1], a[3]))}, rws, oaoff, 0, SC1);
    }
    if (kv == 5) {
      if (lane < 32) {
        unsigned spin = 0;
        NOUNROLL while (!none(((s0[1] ^ T) | (s0[3] ^ T)) != 0u)) {
          GIVEUP(spin, CAP_G);
          __builtin_amdgcn_s_sleep(2);
          CBAR();
          s0 = LD128(rws, soff, 0, SC1);
        }
        const float m1 = __uint_as_float(s0[0]);
        float MM = fmaxf(m1, dpp<QX1>(m1));
        MM = fmaxf(MM, dpp<QX2>(MM)); MM = fmaxf(MM, dpp<HMIR>(MM));
        const float e1 = __builtin_amdgcn_exp2f((m1 - MM) * LOG2E);
        float L = e1 * __uint_as_float(s0[2]);
        L += dpp<QX1>(L); L += dpp<QX2>(L); L += dpp<HMIR>(L);
        LDSV(float, L_WT + lane * 4) = e1 * __builtin_amdgcn_rcpf(L);
      }
      LDSV(unsigned, F_W) = 1u;
    }
    if (kv >= 6 && kvh == (int)((place >> 9) & 7u)) {
      const int lh = kv - 6, pg = lane >> 3;
      const int oi = (gi >> 3) * 512 + (gi & 7) * 64 + lh * 32 + (c & 7) * 4 + g;
      const i32x4 ro = ka[9];
      float xfin = lo16(LD16(rx, oi * 2, 0, 0));
      int go[4];
#pragma unroll
      for (int i = 0; i < 4; i++) go[i] = WS_PB + ((gi & 7) * 4 + i) * 16384 + side + pg * 1024 + (((gi >> 3) * 2 + lh + 2 * i + (pg >> 2)) & 7) * 128 + (lane & 7) * 16;
      int ooff = c < 8 ? oi * 2 : OOB;
      unsigned ta = (T >> ((lane & 1) * 16)) & 0xffu, tb = (T >> ((lane & 1) * 16 + 8)) & 0xffu;
      asm volatile("" : "+v"(xfin), "+v"(go[0]), "+v"(go[1]), "+v"(go[2]), "+v"(go[3]), "+v"(ooff), "+v"(ta), "+v"(tb));
      lds_wait_eq(lds, F_T, 4u);
      __builtin_amdgcn_s_sleep(18);
      u32x4 pb[4];
      unsigned spin = 0;
      NOUNROLL for (;;) {
        CBAR();
#pragma unroll
        for (int i = 0; i < 4; i++) pb[i] = LD128(rws, go[i], 0, SC1);
        unsigned bad = 0;
#pragma unroll
        for (int i = 0; i < 4; i++) { pb[i][0] ^= ta; pb[i][2] ^= tb; bad |= pb[i][0] | pb[i][2]; }
        if (none((bad & 0xffu) != 0)) break;
        GIVEUP(spin, CAP_G);
        __builtin_amdgcn_s_sleep(1);
      }
      const f32x4 v = (F4(pb[0]) + F4(pb[1])) + (F4(pb[2]) + F4(pb[3]));
      float z = fold16(fold32(v[0], v[2]), fold32(v[1], v[3]));
      z += dpp<ROR8>(z);
      z += xfin;
      ST16((u16)cvtpk(z, z), ro, ooff, 0, SC1);
      if (gi == 0 && lh == 0 && lane == 0) ST32(T, rws, WS_EPOCH, 0, SC1);
    }
    return;
  }

  __builtin_amdgcn_s_setprio(3);
  const unsigned ep = LD32(rws, WS_EPOCH, 0, SC1);
  const int hl = gi >> 3, ng = gi & 7;
  const int task = gi >> 1, kh = gi & 1;
  const unsigned tstart = (unsigned)__builtin_amdgcn_s_memrealtime();
  if (task < 15) {
    const bool isq = task < 8;
    u32x4 xs[4], xf, W[8];
#pragma unroll
    for (int i = 0; i < 4; i++) xs[i] = LD128(rx, (c >> 2) * 1024 + g * 64 + (c & 3) * 16 + i * 256, 0, 0);
    xf = LD128(rx, (c >> 2) * 1024 + (c & 1) * 64 + g * 16, kh * 512 + w * 128, 0);
    const int kvq = (kvh + 1) & 7, idx = kvh * 7 + task - 8, mat = idx >= 28 ? 1 : 0;
    const int cb = isq ? kvq * 8 + task : idx - 28 * mat;
    const int rstride = isq ? 8192 : FF * 2;
    const i32x4 rw = {isq ? KB[0] : mat ? KB[8] : KB[4], isq ? KB[1] : mat ? KB[9] : KB[5], isq ? KB[2] : KB[6], KB[3]};
    const int woff = ((c >> 3) * 32 + g * 8) * rstride + (c & 7) * 16;
    int wso = (kh * 256 + w * 64) * rstride + cb * 128;
    unsigned tg = tstart + GOFF + (w * GTAU) / 4;
#pragma unroll
    for (int j = 0; j < 8; j++) {
      if (!isq) pace(tg, GTAU);
      W[j] = LD128(rw, woff, wso, NTL);
      wso += rstride;
      SB();
    }
    float sm = 0.f;
#pragma unroll
    for (int i = 0; i < 4; i++)
#pragma unroll
      for (int j = 0; j < 4; j++) sm = dot2(xs[i][j], 0x3f803f80u, sm);
    sm += dpp<QX1>(sm); sm += dpp<QX2>(sm);
    const float mean = xrow_sum(sm) * (1.f / DM);
    f32x2 ssv = {0.f, 0.f};
#pragma unroll
    for (int i = 0; i < 4; i++)
#pragma unroll
      for (int j = 0; j < 4; j++) { const f32x2 d = f32x2{lo16(xs[i][j]), hi16(xs[i][j])} - mean; ssv += d * d; }
    float ss = ssv[0] + ssv[1];
    ss += dpp<QX1>(ss); ss += dpp<QX2>(ss);
    const float var = rbf(xrow_sum(ss) * (1.f / DM)), mb = rbf(mean);
    const float den = rbf(__builtin_amdgcn_sqrtf(rbf(var + 1e-5f)));
    const float rden = __builtin_amdgcn_rcpf(den);
#pragma unroll
    for (int j = 0; j < 4; j++) {
      const unsigned d = cvtpk(lo16(xf[j]) - mb, hi16(xf[j]) - mb);
      const f32x2 n = {lo16(d), hi16(d)};
      const f32x2 q = n * rden;
      const f32x2 r = (n - q * den) * rden + q;
      xf[j] = cvtpk(r[0], r[1]);
    }
    SB();
    f32x4 acc[8];
#pragma unroll
    for (int e = 0; e < 8; e++) {
      u32x4 f;
#pragma unroll
      for (int j = 0; j < 4; j++) f[j] = kpair(W[2 * j][e >> 1], W[2 * j + 1][e >> 1], e & 1);
      acc[e] = mfma32(xf, f, f32x4{0.f, 0.f, 0.f, 0.f});
    }
    SB();
    f32x4 o, z;
#pragma unroll
    for (int i = 0; i < 4; i++) {
      const float a = acc[i][0] + dpp<ROR8>(acc[i][1]), bq = acc[4 + i][1] + dpp<ROR8>(acc[4 + i][0]);
      o[i] = c < 8 ? a : bq;
    }
    if (meet4(meet_prep(lds, L_PR, F_P, w, lane, g * 16 + (c & 7) * 2 + (c >> 3)), o, z)) {
      const unsigned t0 = (__builtin_amdgcn_readfirstlane(ep) + 0x01010101u) >> ((c & 1) * 16);
      const u32x4 pv = {tagm(z[0], 0xfu, t0), tagm(z[1], 0xfu, t0 >> 4), tagm(z[2], 0xfu, t0 >> 8), tagm(z[3], 0xfu, t0 >> 12)};
      const int poff = isq ? WS_Q + (((kvq * 4 + g) * 2 + kh) * 512 + task * 64 + c * 4) * 4 : WS_GU + (((mat * 2 + kh) * 4 + g) * FF + cb * 64 + c * 4) * 4;
      ST128(pv, rws, poff, 0, SC1);
    }
  }
  const unsigned T = __builtin_amdgcn_readfirstlane(ep) + 0x01010101u;
  asm volatile("" : "+v"(pfd) : "s"(T) : "memory");
  LDSV(unsigned char, F_E + w) = 1;
  const int bb = c >> 2, q4 = c & 3, sh = g >> 1;
  if (w >= 2) {
    int hh3 = 0;
    if (w == 3) hh3 = partner(wait_place(lds), kvh) * 4 + hl;
    const int goff = w == 2 ? WS_Q + (kvh * 4 + b) * 4096 + lane * 16 : WS_GU + (g * FF + hh3 * 56 + (c < 14 ? c : 13) * 4) * 4;
    const int st1 = w == 2 ? 2048 : 4 * FF * 4, st2 = w == 2 ? 1024 : 8 * FF * 4;
    const unsigned tn = T >> (goff & 16);
    const u32x4 TN = {tn & 0xfu, (tn >> 4) & 0xfu, (tn >> 8) & 0xfu, (tn >> 12) & 0xfu};
    u32x4 q[4];
    lds_wait_byte(lds, F_G);
    unsigned spin = 0;
    NOUNROLL for (;;) {
      CBAR();
      q[0] = LD128(rws, goff, 0, SC1); q[1] = LD128(rws, goff, st1, SC1); q[2] = LD128(rws, goff, st2, SC1); q[3] = LD128(rws, goff, st1 + st2, SC1);
      unsigned bad = 0;
#pragma unroll
      for (int i = 0; i < 4; i++) { q[i] ^= TN; bad |= (q[i][0] | q[i][1]) | (q[i][2] | q[i][3]); }
      if (none((bad & 0xfu) != 0)) break;
      GIVEUP(spin, CAP_G);
      __builtin_amdgcn_s_sleep(8);
    }
    const f32x4 y0 = F4(q[0]) + F4(q[1]), y1 = F4(q[2]) + F4(q[3]);
    const u32x2 z0 = {cvtpk(y0[0], y0[1]), cvtpk(y0[2], y0[3])}, z1 = {cvtpk(y1[0], y1[1]), cvtpk(y1[2], y1[3])};
    if (w == 2) {
      LDSV(u32x2, L_Q + lane * 8) = z0;
      LDSV(u32x2, L_Q + 512 + lane * 8) = z1;
      lds_wait_eq(lds, F_E, 0x01010101u);
      LDSV(u32x2, F_Q) = u32x2{1u, T};
    } else {
      u32x2 hp;
#pragma unroll
      for (int i = 0; i < 2; i++) hp[i] = c < 14 ? cvtpk(lo16(z0[i]) * lo16(z1[i]), hi16(z0[i]) * hi16(z1[i])) : 0u;
      LDSV(u32x2, L_H + g * 128 + c * 8) = hp;
      LDSV(unsigned, F_H) = 1u;
    }
  } else if (w == 0) {
    unsigned lv, spin = 0;
    NOUNROLL for (;;) {
      lv = LDSV(unsigned, L_LW + (lane & 31) * 4);
      if (none(lv == 0u)) break;
      GIVEUP(spin, CAP_L);
      __builtin_amdgcn_s_sleep(2);
    }
    float mv = hi16(LDSV(unsigned, L_MW + (lane & 31) * 4));
    float lsum = __uint_as_float(lv);
    lsum += dpp<QX1>(lsum); lsum += dpp<QX2>(lsum); lsum += dpp<HMIR>(lsum);
    mv = fmaxf(mv, dpp<QX1>(mv)); mv = fmaxf(mv, dpp<QX2>(mv)); mv = fmaxf(mv, dpp<HMIR>(mv));
    const u32x4 st = {__float_as_uint(mv), T, __float_as_uint(lsum), T};
    if ((lane & 7) == 0) ST128(st, rws, WS_ST + slot * 128 + (lane >> 3) * 16, 0, SC1);
  } else {
    if (gi == 31) {
      ST128(u32x4{T, T, T, T}, rws, WS_PR + (lane >> 5) * 8192 + kvh * 1024 + (lane & 31) * 16, 0, SC1);
      asm volatile("s_waitcnt vmcnt(0)" ::: "memory");
      unsigned best0 = ~0u, best1 = ~0u;
      NOUNROLL for (int i = 0; i < 8; i++) {
        unsigned long long c0, c1; unsigned d;
        asm volatile("s_memtime %0\n\ts_waitcnt lgkmcnt(0)\n\ts_buffer_load_dword %2, %3, %4\n\ts_waitcnt lgkmcnt(0)\n\ts_memtime %1\n\ts_waitcnt lgkmcnt(0)"
                     : "=&s"(c0), "=&s"(c1), "=&s"(d) : "s"(rws), "s"(WS_PR + (i & 1) * 8192 + kvh * 1024 + (i >> 1) * 128) : "memory");
        const unsigned dt = (unsigned)c1 - (unsigned)c0;
        if (i >= 2) { if (i & 1) best1 = min(best1, dt); else best0 = min(best0, dt); }
      }
      const unsigned xcc = __builtin_amdgcn_s_getreg(20 | (3 << 11)) & 7u;
      if (lane == 0) ST64(u32x2{T, (tstart << 4) | (xcc << 1) | (best1 < best0 ? 1u : 0u)}, rws, WS_PF + kvh * 8, 0, SC1);
    }
    lds_wait_byte(lds, F_G + 1);
    if (lane < 8) {
      u32x2 pf;
      unsigned spin = 0;
      NOUNROLL for (;;) {
        CBAR();
        pf = LD64(rws, WS_PF + lane * 8, 0, SC1);
        if (none(pf[0] != T)) break;
        GIVEUP(spin, CAP_G);
        __builtin_amdgcn_s_sleep(8);
      }
      unsigned kx = ((pf[1] & 14u) << 2) | lane;
      kx = min(kx, dppu<QX1>(kx)); kx = min(kx, dppu<QX2>(kx)); kx = min(kx, dppu<HMIR>(kx));
      unsigned dl = (unsigned)min(max(((int)(pf[1] - (tstart << 4))) >> 4, -100), 100);
      dl += dppu<QX1>(dl); dl += dppu<QX2>(dl); dl += dppu<HMIR>(dl);
      LDSV(unsigned, F_PF) = (unsigned)__builtin_amdgcn_ballot_w64(pf[1] & 1u) | 0x100u | ((kx & 7u) << 9) | ((unsigned)((int)dl >> 3) << 16);
    }
  }
  const unsigned place = wait_place(lds);
  const int kva = partner(place, kvh), hh = kva * 4 + hl;
  const i32x4 rwo = {KB[12], KB[13], KB[14], KB[15]}, rwd = ka[8];
  const int wos = (hh * 128 + 16 * (w >> 1) + 4 * (w & 1)) * 1024 + ng * 128, wds = (hh * 56 + w * 16) * 1024 + ng * 128;
  int aoff = WS_OA + ((kva * 4 + bb) * 2 + sh) * 16384 + ((place >> kvh) & 1) * 8192 + hl * 512 + w * 128 + (g & 1) * 64 + q4 * 16;
  int pboff = WS_PB + (ng * 4 + (hh >> 3)) * 16384 + ((place >> ((place >> 9) & 7)) & 1) * 8192 + (hh & 7) * 1024 + ((lane * 16 + (hh >> 2) * 128) & 1023);
  int wooff = (32 * (g >> 1) + 2 * (g & 1)) * 1024 + c * 8;
  int wdoff = (w == 3 && g >= 2) ? OOB - 4096 : g * 4096 + c * 8;
  unsigned ta = (T >> ((lane & 1) * 16)) & 0xffu, tb = (T >> ((lane & 1) * 16 + 8)) & 0xffu, mska = 0xffu;
  asm volatile("" : "+v"(ta), "+v"(tb));
  asm volatile("" : "+v"(aoff), "+v"(pboff), "+v"(wooff), "+v"(wdoff), "+v"(mska));
  lds_wait8(lds, F_VI);
  const unsigned tvi = __builtin_amdgcn_readfirstlane(LDSV(unsigned, F_TV));
  u32x2 WD[4], WO[8];
  {
    unsigned tw = tvi + WDEL + (w * WTAU) / 4;
#pragma unroll
    for (int j = 0; j < 4; j++) { pace(tw, WTAU); WD[j] = LD64(rwd, wdoff + j * 1024, wds, NTL); SB(); }
#pragma unroll
    for (int j = 0; j < 8; j++) { pace(tw, WTAU); WO[j] = LD64(rwo, wooff + (j >> 2) * 1024, wos + (j & 1) * 8192 + ((j >> 1) & 1) * 65536, NTL); SB(); }
  }
  lds_wait_eq(lds, F_H, 1u);
  const u32x2 hf = LDSV(u32x2, L_H + bb * 128 + w * 32 + g * 8);
  lds_wait8(lds, F_PV);
  __builtin_amdgcn_s_sleep(4);
  { unsigned tA = tvi + TA_ADD; pace(tA, 0); }
  u32x4 a[4];
#pragma unroll
  for (int i = 0; i < 4; i++) a[i] = LD128(rws, aoff, i * 2048, SC1);
  SB();
  u32x4 fo[4];
  f32x4 acc[4];
#pragma unroll
  for (int e = 0; e < 4; e++) {
#pragma unroll
    for (int j = 0; j < 4; j++) fo[e][j] = kpair(WO[2 * j][e >> 1], WO[2 * j + 1][e >> 1], e & 1);
    acc[e] = mfma16(hf, u32x2{kpair(WD[0][e >> 1], WD[1][e >> 1], e & 1), kpair(WD[2][e >> 1], WD[3][e >> 1], e & 1)}, f32x4{0.f, 0.f, 0.f, 0.f});
  }
  asm volatile("" : "+v"(acc[0]), "+v"(acc[1]), "+v"(acc[2]), "+v"(acc[3]), "+v"(fo[0]), "+v"(fo[1]), "+v"(fo[2]), "+v"(fo[3]));
  const Meet mt = meet_prep(lds, L_TR, F_T, w, lane, lane);
  lds_wait_eq(lds, F_W, 1u);
  f32x4 wt = LDSV(f32x4, L_WT + bb * 32 + sh * 16);
  OPQ(wt);
  {
    unsigned spin = 0;
    NOUNROLL for (;;) {
      unsigned bad = 0;
#pragma unroll
      for (int i = 0; i < 4; i++) { a[i][0] ^= ta; a[i][2] ^= tb; bad |= a[i][0] | a[i][2]; }
      if (none((bad & 0xffu) != 0)) break;
      GIVEUP(spin, CAP_G);
      __builtin_amdgcn_s_sleep(1);
      CBAR();
#pragma unroll
      for (int i = 0; i < 4; i++) a[i] = LD128(rws, aoff, i * 2048, SC1);
    }
  }
  f32x4 t = F4(a[0]) * wt[0];
#pragma unroll
  for (int i = 1; i < 4; i++) t = __builtin_elementwise_fma(F4(a[i]), f32x4{wt[i], wt[i], wt[i], wt[i]}, t);
  const unsigned own = cvtpk(fold32(t[0], t[2]), fold32(t[1], t[3]));
  const u32x4 A = {own, dppu<QB1>(own), dppu<QB2>(own), dppu<QB3>(own)};
#pragma unroll
  for (int e = 0; e < 4; e++) acc[e] = mfma32(A, fo[e], acc[e]);
  SB();
  f32x4 z;
  if (meet4(mt, f32x4{acc[0][0], acc[1][0], acc[2][0], acc[3][0]}, z)) {
    const u32x4 o = {tagm(z[0], mska, ta), __float_as_uint(z[1]), tagm(z[2], mska, tb), __float_as_uint(z[3])};
    ST128(o, rws, pboff, 0, SC1);
  }
}

extern "C" void kernel_launch(void* const* d_in, const int* in_sizes, int n_in, void* d_out, int out_size, void* d_ws, size_t ws_size, hipStream_t stream) {
  const unsigned fl = 0x00020000u;
  fused_decode<<<256, NTHR, 0, stream>>>(d_in[1], 33554432u, fl, d_in[2], 33554432u, fl, d_ws, (unsigned)WS_END, fl, d_in[0], 4096u, fl,
                                         d_in[3], 4194304u, fl, d_in[6], 1835008u, fl, d_in[7], 1835008u, fl, d_in[4], 4194304u, fl,
                                         d_in[8], 1835008u, fl, d_out, 4096u, fl);
}
```

```cpp
#include <hip/hip_runtime.h>
#include <stdint.h>

typedef unsigned short u16;
typedef int i32x4 __attribute__((ext_vector_type(4)));
typedef int i32x16 __attribute__((ext_vector_type(16)));
typedef unsigned u32x2 __attribute__((ext_vector_type(2)));
typedef unsigned u32x4 __attribute__((ext_vector_type(4)));
typedef float f32x2 __attribute__((ext_vector_type(2)));
typedef float f32x4 __attribute__((ext_vector_type(4)));
typedef short s16x4 __attribute__((ext_vector_type(4)));
typedef short s16x8 __attribute__((ext_vector_type(8)));
typedef __bf16 bf16x2 __attribute__((ext_vector_type(2)));
#define LAS __attribute__((address_space(3)))

__device__ u16 LD16(i32x4 r, int v, int s, int a) __asm("llvm.amdgcn.raw.buffer.load.i16");
__device__ unsigned LD32(i32x4 r, int v, int s, int a) __asm("llvm.amdgcn.raw.buffer.load.i32");
__device__ u32x2 LD64(i32x4 r, int v, int s, int a) __asm("llvm.amdgcn.raw.buffer.load.v2i32");
__device__ u32x4 LD128(i32x4 r, int v, int s, int a) __asm("llvm.amdgcn.raw.buffer.load.v4i32");
__device__ void ST16(u16 d, i32x4 r, int v, int s, int a) __asm("llvm.amdgcn.raw.buffer.store.i16");
__device__ void ST32(unsigned d, i32x4 r, int v, int s, int a) __asm("llvm.amdgcn.raw.buffer.store.i32");
__device__ void ST64(u32x2 d, i32x4 r, int v, int s, int a) __asm("llvm.amdgcn.raw.buffer.store.v2i32");
__device__ void ST128(u32x4 d, i32x4 r, int v, int s, int a) __asm("llvm.amdgcn.raw.buffer.store.v4i32");
constexpr int NTL = 2, SC1 = 16;

constexpr int DM = 512, FF = 1792;
constexpr int NT = 4, NTHR = (NT + 8) * 64;

constexpr int WIN = 8;
constexpr int N0 = 2;
constexpr int TOFF = 30, TAU1 = 360, I1 = 8, TAU = 270;
constexpr int W0 = 2, DEL = 40;
constexpr int REBASE = -45;
constexpr int QGPOS = 12, HGPOS = 18;
constexpr int QKPOS = 4, SMPOS = 7, ORGPOS = 8, PVPOS = 14;
constexpr int GOFF = 40, GTAU = 30;
constexpr int WDEL = 8, WTAU = 6;
constexpr int TA_ADD = 125;
constexpr int CODE_BYTES = 12288;
constexpr int sched(int i) { return TOFF + ((i < I1 ? i - N0 + 1 : I1 - N0) * TAU1 + (i < I1 ? 0 : i - I1 + 1) * TAU) / 10; }

constexpr int WS_EPOCH = 0;
constexpr int WS_PF = 1024;
constexpr int WS_Q = 4096;
constexpr int WS_GU = WS_Q + 131072;
constexpr int WS_ST = WS_GU + 114688;
constexpr int WS_PR = 294912;
constexpr int WS_OA = WS_PR + 16384;
constexpr int WS_PB = WS_OA + 64 * 16384;
constexpr int WS_END = WS_PB + 32 * 16384;
static_assert(WS_ST + 32768 <= WS_PR && WS_PR % 16384 == 0, "map");
constexpr int L_RED = 0;
constexpr int L_TR = L_RED + 20480;
constexpr int L_PR = L_TR + 4096;
constexpr int L_Q = L_PR + 4096;
constexpr int L_H = L_Q + 1024;
constexpr int L_WT = L_H + 512;
constexpr int L_MW = L_WT + 128;
constexpr int L_LW = L_MW + 128;
constexpr int L_FLAG = L_LW + 128;
constexpr int F_Q = L_FLAG, F_H = L_FLAG + 8, F_E = L_FLAG + 12, F_VI = L_FLAG + 16, F_PV = L_FLAG + 24, F_T = L_FLAG + 32, F_P = L_FLAG + 36,
              F_TV = L_FLAG + 48, F_W = L_FLAG + 52, F_G = L_FLAG + 56, F_PF = L_FLAG + 60;
constexpr int L_END = L_FLAG + 64;
constexpr unsigned CAP_G = 1u << 18, CAP_L = 1u << 22;
constexpr int OOB = 0x7ffffff0;
constexpr float LOG2E = 1.4426950408889634f;

__device__ __forceinline__ float lo16(unsigned u) { return __uint_as_float(u << 16); }
__device__ __forceinline__ float hi16(unsigned u) { return __uint_as_float(u & 0xffff0000u); }
__device__ __forceinline__ unsigned cvtpk(float lo, float hi) { f32x2 f = {lo, hi}; return __builtin_bit_cast(unsigned, __builtin_convertvector(f, bf16x2)); }
__device__ __forceinline__ float rbf(float f) { return lo16(cvtpk(f, f)); }
__device__ __forceinline__ f32x4 mfma32(u32x4 a, u32x4 b, f32x4 c) { return __builtin_amdgcn_mfma_f32_16x16x32_bf16(__builtin_bit_cast(s16x8, a), __builtin_bit_cast(s16x8, b), c, 0, 0, 0); }
__device__ __forceinline__ f32x4 mfma16(u32x2 a, u32x2 b, f32x4 c) { return __builtin_amdgcn_mfma_f32_16x16x16bf16_1k(__builtin_bit_cast(s16x4, a), __builtin_bit_cast(s16x4, b), c, 0, 0, 0); }
template <int CTRL> __device__ __forceinline__ float dpp(float x) { return __builtin_bit_cast(float, __builtin_amdgcn_mov_dpp(__builtin_bit_cast(int, x), CTRL, 0xf, 0xf, true)); }
template <int CTRL> __device__ __forceinline__ unsigned dppu(unsigned x) { return (unsigned)__builtin_amdgcn_mov_dpp((int)x, CTRL, 0xf, 0xf, true); }
constexpr int ROR8 = 0x128, HMIR = 0x141, QX1 = 0xB1, QX2 = 0x4E, QB0 = 0x00, QB1 = 0x55, QB2 = 0xAA, QB3 = 0xFF;
__device__ __forceinline__ float fold32(float a, float b) {
  auto t = __builtin_amdgcn_permlane32_swap(__float_as_uint(a), __float_as_uint(b), false, false);
  return __uint_as_float(t[0]) + __uint_as_float(t[1]);
}
__device__ __forceinline__ float fold16(float a, float b) {
  auto t = __builtin_amdgcn_permlane16_swap(__float_as_uint(a), __float_as_uint(b), false, false);
  return __uint_as_float(t[0]) + __uint_as_float(t[1]);
}
__device__ __forceinline__ float xrow_sum(float x) { x = fold16(x, x); return fold32(x, x); }
__device__ __forceinline__ float xrow_max(float x) {
  auto s = __builtin_amdgcn_permlane16_swap(__float_as_uint(x), __float_as_uint(x), false, false);
  x = fmaxf(__uint_as_float(s[0]), __uint_as_float(s[1]));
  auto t = __builtin_amdgcn_permlane32_swap(__float_as_uint(x), __float_as_uint(x), false, false);
  return fmaxf(__uint_as_float(t[0]), __uint_as_float(t[1]));
}
__device__ __forceinline__ float dot2(unsigned a, unsigned b, float c) { return __builtin_amdgcn_fdot2_f32_bf16(__builtin_bit_cast(bf16x2, a), __builtin_bit_cast(bf16x2, b), c, false); }
__device__ __forceinline__ unsigned tagm(float v, unsigned m, unsigned t) { return (__float_as_uint(v) & ~m) | (t & m); }
__device__ __forceinline__ unsigned kpair(unsigned r0, unsigned r1, int sel) { return __builtin_amdgcn_perm(r1, r0, sel ? 0x07060302u : 0x05040100u); }
__device__ __forceinline__ bool none(bool p) { return __builtin_amdgcn_ballot_w64(p) == 0ull; }
#define F4(v) __builtin_bit_cast(f32x4, v)
#define SB() __builtin_amdgcn_sched_barrier(0)
#define CBAR() asm volatile("" ::: "memory")
#define OPQ(x) asm volatile("" : "+v"(x))
#define LDSV(T, off) (*(volatile LAS T*)(lds + (off)))
#define NOUNROLL _Pragma("clang loop unroll(disable)")
#define GIVEUP(spin, cap) asm volatile("s_add_u32 %0, %0, 1\n\ts_cmp_lt_u32 %0, %1\n\ts_cbranch_scc1 1f\n\ts_endpgm\n1:" : "+s"(spin) : "n"(cap) : "scc")

__device__ __forceinline__ unsigned now32() { unsigned long long t; asm volatile("s_memrealtime %0\n\ts_waitcnt lgkmcnt(0)" : "=s"(t)::"memory"); return (unsigned)t; }
__device__ __forceinline__ void pace(unsigned& tgt, int step) {
  asm volatile("1:\n\ts_memrealtime vcc\n\ts_waitcnt lgkmcnt(0)\n\ts_sub_u32 vcc_lo, vcc_lo, %0\n\ts_cmp_lt_i32 vcc_lo, 0\n\ts_cbranch_scc1 1b\n\ts_add_u32 %0, %0, %1"
               : "+s"(tgt) : "n"(step) : "memory", "scc", "vcc");
}
__device__ __forceinline__ void apace(unsigned& tgt, int step) {
  unsigned tmp;
  asm volatile("1:\n\ts_memrealtime vcc\n\ts_getreg_b32 %1, hwreg(HW_REG_IB_STS, 0, 4)\n\ts_sub_i32 %1, %1, %3\n\ts_max_i32 %1, %1, 0\n\ts_mul_i32 %1, %1, %4\n\t"
               "s_waitcnt lgkmcnt(0)\n\ts_sub_u32 vcc_lo, vcc_lo, %0\n\ts_cmp_lt_i32 vcc_lo, %1\n\ts_cbranch_scc1 1b\n\ts_add_u32 %0, %0, %2"
               : "+s"(tgt), "=&s"(tmp) : "n"(step), "n"(W0 - 1), "n"(DEL) : "memory", "scc", "vcc");
}
__device__ __forceinline__ void lds_wait_eq(LAS unsigned char* lds, int off, unsigned n) {
  unsigned spin = 0;
  NOUNROLL while (LDSV(unsigned, off) != n) { GIVEUP(spin, CAP_L); __builtin_amdgcn_s_sleep(1); }
  CBAR();
}
__device__ __forceinline__ void lds_wait_byte(LAS unsigned char* lds, int off) {
  unsigned spin = 0;
  NOUNROLL while (LDSV(unsigned char, off) == 0) { GIVEUP(spin, CAP_L); __builtin_amdgcn_s_sleep(2); }
  CBAR();
}
__device__ __forceinline__ void lds_wait8(LAS unsigned char* lds, int off) {
  unsigned spin = 0;
  NOUNROLL for (;;) {
    const u32x2 f = LDSV(u32x2, off);
    if ((f[0] & f[1]) == 0x01010101u) break;
    GIVEUP(spin, CAP_L);
    __builtin_amdgcn_s_sleep(1);
  }
  CBAR();
}
struct Meet { unsigned wr, rd, cnt, one; };
__device__ __forceinline__ Meet meet_prep(LAS unsigned char* lds, int area, int cnt, int w, int lane, int wpos) {
  Meet m = {(unsigned)(uintptr_t)(lds + area) + w * 1024 + wpos * 16, (unsigned)(uintptr_t)(lds + area) + lane * 16, (unsigned)(uintptr_t)(lds + cnt), 1u};
  asm volatile("" : "+v"(m.wr), "+v"(m.rd), "+v"(m.cnt), "+v"(m.one));
  return m;
}
__device__ __forceinline__ bool meet4(const Meet& m, f32x4 v, f32x4& sum) {
  *(volatile LAS f32x4*)(uintptr_t)m.wr = v;
  unsigned prev;
  f32x4 r0, r1, r2, r3;
  asm volatile("s_mov_b64 exec, 1\n\tds_add_rtn_u32 %0, %6, %7\n\ts_mov_b64 exec, -1\n\t"
               "ds_read_b128 %1, %5\n\tds_read_b128 %2, %5 offset:1024\n\tds_read_b128 %3, %5 offset:2048\n\tds_read_b128 %4, %5 offset:3072\n\t"
               "s_waitcnt lgkmcnt(0)"
               : "=&v"(prev), "=&v"(r0), "=&v"(r1), "=&v"(r2), "=&v"(r3) : "v"(m.rd), "v"(m.cnt), "v"(m.one) : "memory");
  sum = (r0 + r1) + (r2 + r3);
  return __builtin_amdgcn_readfirstlane(prev) == 3u;
}
__device__ __forceinline__ unsigned wait_place(LAS unsigned char* lds) {
  unsigned p = LDSV(unsigned, F_PF), spin = 0;
  NOUNROLL while (p == 0u) { GIVEUP(spin, CAP_L); __builtin_amdgcn_s_sleep(1); p = LDSV(unsigned, F_PF); }
  return __builtin_amdgcn_readfirstlane(p);
}
__device__ __forceinline__ int partner(unsigned place, int g) {
  const unsigned m = (((place >> g) & 1u) ? place : ~place) & 0xffu;
  return (g + 1 + __builtin_ctz(((m | (m << 8)) >> (g + 1)) & 0xffu)) & 7;
}

#define DESC(n) const void* p_##n, unsigned n##_bytes, unsigned n##_flags
__global__ void __launch_bounds__(NTHR, 3) fused_decode(DESC(k), DESC(v), DESC(ws), DESC(x), DESC(wq), DESC(wg), DESC(wu), DESC(wo), DESC(wd), DESC(out)) {
  __shared__ __attribute__((aligned(16))) unsigned char smem[L_END];
  auto ka = (const __attribute__((address_space(4))) i32x4*)__builtin_amdgcn_kernarg_segment_ptr();
  i32x16 KA, KB;
  unsigned long long pc0, tent;
  asm volatile("s_memrealtime %0" : "=s"(tent));
  asm volatile("s_load_dwordx16 %0, %3, 0x0\n\ts_load_dwordx16 %1, %3, 0x40\n\ts_getpc_b64 %2" : "=&s"(KA), "=&s"(KB), "=&s"(pc0) : "s"(ka));
  SB();
  LAS unsigned char* lds = (LAS unsigned char*)smem;
  const int blk = blockIdx.x, kvh = blk & 7, gi = blk >> 3, b = gi & 3, s = gi >> 2;
  const int tid = threadIdx.x, lane = tid & 63, w = __builtin_amdgcn_readfirstlane(tid >> 6), g = lane >> 4, c = lane & 15;
  u32x2 pfd = {0u, 0u};
  if (w == 3 && lane < CODE_BYTES / 256) {
    const int off = gi * (CODE_BYTES / 32) + lane * 8;
    asm volatile("global_load_dwordx2 %0, %1, %2" : "=v"(pfd) : "v"(off), "s"(pc0 & ~0xffull));
  }
  if (w == 2) {
    LDSV(unsigned, L_MW + lane * 4) = 0u;
    LDSV(unsigned, L_FLAG + c * 4) = 0u;
  }
  const int kv = w - NT;
  int koff = (c >> 2) * 256 + g * 64 + (c & 3) * 16;
  int kvbase = ((b * 8 + kvh) * 4096 + s * 512) * 256 + kv * 4096;
  int so0 = kvbase + (gi & 3) * 1024;
  OPQ(koff);
  asm volatile("s_waitcnt lgkmcnt(0)\n\ts_barrier" : "+s"(KA), "+s"(KB), "+s"(kvbase), "+s"(so0), "+s"(tent)::"memory");
  const i32x4 rws = {KA[8], KA[9], KA[10], KA[11]}, rx = {KA[12], KA[13], KA[14], KA[15]};
  const int slot = (kvh * 4 + b) * 8 + s;

  if (__builtin_expect(w >= NT, 1)) {
    const i32x4 rk = {KA[0], KA[1], KA[2], KA[3]}, rv = {KA[4], KA[5], KA[6], KA[7]};
    u32x4 K[16], V[16];
    const int voff = lane * 16;
#define SOFF(i) (((i) < 4 ? so0 : kvbase + ((((i) >> 2) + gi) & 3) * 1024) + ((i) & 3) * 32768)
#define WAITN(n) asm volatile("s_waitcnt vmcnt(%0)" ::"n"((n) - 1) : "memory")
#define PACE(i) apace(tgt, sched((i) + 1) - sched(i))
#define VLOAD(i) do { WAITN(WIN); PACE(16 + (i)); V[i] = LD128(rv, voff, SOFF(i), NTL); \
    if ((i) + 17 == HGPOS && kv == 7) LDSV(unsigned char, F_G + 1) = 1; \
    if ((i) == 15) { LDSV(unsigned, F_TV) = now32(); LDSV(unsigned char, F_VI + kv) = 1; } SB(); } while (0)
    unsigned long long t64;
    asm volatile("s_memrealtime %0" : "=s"(t64));
#pragma unroll
    for (int i = 0; i < N0; i++) { K[i] = LD128(rk, koff, SOFF(i), NTL); SB(); }
    asm volatile("s_waitcnt lgkmcnt(0)" : "+s"(t64));
    unsigned tgt = (unsigned)t64 + sched(N0) + (kv * TAU) / 80;
    WAITN(N0);
    { const unsigned t1 = now32() + REBASE + (kv * TAU) / 80; if ((int)(t1 - tgt) > 0) tgt = t1; }
#pragma unroll
    for (int i = N0; i < 16; i++) {
      if (i >= WIN) WAITN(WIN);
      PACE(i);
      K[i] = LD128(rk, koff, SOFF(i), NTL);
      if (i + 1 == QGPOS && kv == 7) LDSV(unsigned char, F_G) = 1;
      SB();
    }
#pragma unroll
    for (int i = 0; i < QKPOS; i++) VLOAD(i);
    u32x2 fl = LDSV(u32x2, F_Q);
    { unsigned spin = 0; NOUNROLL while (fl[0] == 0u) { GIVEUP(spin, CAP_L); __builtin_amdgcn_s_sleep(1); fl = LDSV(u32x2, F_Q); } }
    CBAR();
    const unsigned T = __builtin_amdgcn_readfirstlane(fl[1]);
    float S[4];
    {
      const u32x4 qf = LDSV(u32x4, L_Q + koff);
      const bool a1 = c & 1, a2 = c & 2;
#pragma unroll
      for (int t = 0; t < 4; t++) {
        float y[4];
#pragma unroll
        for (int i = 0; i < 4; i++) {
          const f32x4 d = mfma32(K[t * 4 + i], qf, f32x4{0.f, 0.f, 0.f, 0.f});
          float z = dpp<QB0>(d[0]);
          z += dpp<QB1>(d[1]); OPQ(z);
          z += dpp<QB2>(d[2]); OPQ(z);
          z += dpp<QB3>(d[3]); OPQ(z);
          y[i] = z;
        }
        const float u0 = a1 ? y[1] : y[0], u1 = a1 ? y[3] : y[2];
        S[t] = a2 ? u1 : u0;
      }
#pragma unroll
      for (int i = 0; i < 4; i += 2) {
        const unsigned p = cvtpk(S[i], S[i + 1]);
        const unsigned p2 = cvtpk(lo16(p) * 0.08838834764831845f, hi16(p) * 0.08838834764831845f);
        S[i] = lo16(p2); S[i + 1] = hi16(p2);
      }
      float mw = fmaxf(fmaxf(S[0], S[1]), fmaxf(S[2], S[3]));
      mw = fmaxf(mw, dpp<QX1>(mw)); mw = fmaxf(mw, dpp<QX2>(mw));
      mw = xrow_max(mw);
      LDSV(unsigned, L_MW + (c >> 2) * 32 + kv * 4) = __float_as_uint(mw) | 1u;
    }
    SB();
#pragma unroll
    for (int i = QKPOS; i < SMPOS; i++) VLOAD(i);
    u32x4 P[2];
    {
      u32x4 m0, m1;
      unsigned spin = 0;
      NOUNROLL for (;;) {
        m0 = LDSV(u32x4, L_MW + (c >> 2) * 32); m1 = LDSV(u32x4, L_MW + (c >> 2) * 32 + 16);
        const u32x4 mm = m0 & m1;
        if (none((mm[0] & mm[1] & mm[2] & mm[3] & 1u) == 0u)) break;
        GIVEUP(spin, CAP_L);
        __builtin_amdgcn_s_sleep(1);
      }
      float M = -INFINITY;
#pragma unroll
      for (int i = 0; i < 4; i++) M = fmaxf(M, fmaxf(hi16(m0[i]), hi16(m1[i])));
      float p[4];
#pragma unroll
      for (int i = 0; i < 4; i++) p[i] = __builtin_amdgcn_exp2f((S[i] - M) * LOG2E);
      float lsum = (p[0] + p[1]) + (p[2] + p[3]);
      lsum += dpp<QX1>(lsum); lsum += dpp<QX2>(lsum);
      lsum = xrow_sum(lsum);
      LDSV(unsigned, L_LW + (c >> 2) * 32 + kv * 4) = __float_as_uint(lsum) | 1u;
#pragma unroll
      for (int u = 0; u < 2; u++) { const unsigned pk = cvtpk(p[2 * u], p[2 * u + 1]); P[u] = u32x4{pk, dppu<QB1>(pk), dppu<QB2>(pk), dppu<QB3>(pk)}; }
    }
    unsigned mska = 0xffu;
    asm volatile("" : "+v"(mska), "+v"(P[0]), "+v"(P[1]));
    SB();
#pragma unroll
    for (int i = SMPOS; i < PVPOS; i++) {
      if (i == ORGPOS) tgt += ((int)__builtin_amdgcn_readfirstlane(LDSV(unsigned, F_PF)) >> 16) - 6;
      VLOAD(i);
    }
    f32x4 O[8];
#pragma unroll
    for (int e = 0; e < 8; e++) {
      u32x4 f;
#pragma unroll
      for (int j = 0; j < 4; j++) f[j] = kpair(V[j][e >> 1], V[4 + j][e >> 1], e & 1);
      O[e] = mfma32(P[0], f, f32x4{0.f, 0.f, 0.f, 0.f});
    }
    asm volatile("" : "+v"(O[0]), "+v"(O[1]), "+v"(O[2]), "+v"(O[3]), "+v"(O[4]), "+v"(O[5]), "+v"(O[6]), "+v"(O[7]));
    SB();
    const unsigned place = wait_place(lds);
    const int side = ((place >> kvh) & 1) * 8192;
    int oaoff = WS_OA + (slot >> 2) * 16384 + side + (slot & 3) * 2048 + kv * 512 + lane * 8;
    OPQ(oaoff);
    const int soff = WS_ST + (partner(place, kvh) * 32 + (lane & 31)) * 128 + (gi >> 3) * 16;
    u32x4 s0 = {0u, ~T, 0u, 0u};
    if (kv == 5 && lane < 32) s0 = LD128(rws, soff, 0, SC1);
    SB();
    int redw = kv * 2560 + lane * 4, redw2 = redw + 1280, redr = (lane >> 5) * 10240 + ((lane & 31) >> 2) * 320 + kv * 64 + (lane & 3) * 16;
    unsigned tab = T >> ((lane & 3) * 8);
    asm volatile("" : "+v"(redw), "+v"(redw2), "+v"(redr), "+v"(tab));
#pragma unroll
    for (int i = PVPOS; i < 16; i++) VLOAD(i);
    {
      u32x4 F[8];
#pragma unroll
      for (int j = 0; j < 3; j++) {
#pragma unroll
        for (int e = 0; e < 8; e++) F[e][j] = kpair(V[8 + j][e >> 1], V[12 + j][e >> 1], e & 1);
        SB();
      }
#pragma unroll
      for (int i = 0; i < 4; i++) {
        F[2 * i][3] = kpair(V[11][i], V[15][i], 0); F[2 * i + 1][3] = kpair(V[11][i], V[15][i], 1);
        O[2 * i] = mfma32(P[1], F[2 * i], O[2 * i]); O[2 * i + 1] = mfma32(P[1], F[2 * i + 1], O[2 * i + 1]);
        SB();
      }
    }
#pragma unroll
    for (int e = 0; e < 4; e++) *(LAS float*)(lds + L_RED + redw + e * 320) = O[e][0];
#pragma unroll
    for (int e = 0; e < 4; e++) *(LAS float*)(lds + L_RED + redw2 + e * 320) = O[4 + e][0];
    CBAR();
    LDSV(unsigned char, F_PV + kv) = 1;
    if (kv < 4) {
      f32x4 r[4];
      unsigned spin = 0;
      NOUNROLL for (;;) {
        const u32x2 cnt = LDSV(u32x2, F_PV);
#pragma unroll
        for (int i = 0; i < 4; i++) r[i] = LDSV(f32x4, L_RED + redr + i * 2560);
        if ((cnt[0] & cnt[1]) == 0x01010101u) break;
        GIVEUP(spin, CAP_L);
      }
      const f32x4 a = (r[0] + r[1]) + (r[2] + r[3]);
      ST64(u32x2{tagm(fold32(a[0], a[2]), mska, tab), __float_as_uint(fold32(a[1], a[3]))}, rws, oaoff, 0, SC1);
    }
    if (kv == 5) {
      if (lane < 32) {
        unsigned spin = 0;
        NOUNROLL while (!none(((s0[1] ^ T) | (s0[3] ^ T)) != 0u)) {
          GIVEUP(spin, CAP_G);
          __builtin_amdgcn_s_sleep(2);
          CBAR();
          s0 = LD128(rws, soff, 0, SC1);
        }
        const float m1 = __uint_as_float(s0[0]);
        float MM = fmaxf(m1, dpp<QX1>(m1));
        MM = fmaxf(MM, dpp<QX2>(MM)); MM = fmaxf(MM, dpp<HMIR>(MM));
        const float e1 = __builtin_amdgcn_exp2f((m1 - MM) * LOG2E);
        float L = e1 * __uint_as_float(s0[2]);
        L += dpp<QX1>(L); L += dpp<QX2>(L); L += dpp<HMIR>(L);
        LDSV(float, L_WT + lane * 4) = e1 * __builtin_amdgcn_rcpf(L);
      }
      LDSV(unsigned, F_W) = 1u;
    }
    if (kv >= 6 && kvh == (int)((place >> 9) & 7u)) {
      const int lh = kv - 6, pg = lane >> 3;
      const int oi = (gi >> 3) * 512 + (gi & 7) * 64 + lh * 32 + (c & 7) * 4 + g;
      const i32x4 ro = ka[9];
      float xfin = lo16(LD16(rx, oi * 2, 0, 0));
      int go[4];
#pragma unroll
      for (int i = 0; i < 4; i++) go[i] = WS_PB + ((gi & 7) * 4 + i) * 16384 + side + pg * 1024 + (((gi >> 3) * 2 + lh + 2 * i + (pg >> 2)) & 7) * 128 + (lane & 7) * 16;
      int ooff = c < 8 ? oi * 2 : OOB;
      unsigned ta = (T >> ((lane & 1) * 16)) & 0xffu, tb = (T >> ((lane & 1) * 16 + 8)) & 0xffu;
      asm volatile("" : "+v"(xfin), "+v"(go[0]), "+v"(go[1]), "+v"(go[2]), "+v"(go[3]), "+v"(ooff), "+v"(ta), "+v"(tb));
      lds_wait_eq(lds, F_T, 4u);
      __builtin_amdgcn_s_sleep(18);
      u32x4 pb[4];
      unsigned spin = 0;
      NOUNROLL for (;;) {
        CBAR();
#pragma unroll
        for (int i = 0; i < 4; i++) pb[i] = LD128(rws, go[i], 0, SC1);
        unsigned bad = 0;
#pragma unroll
        for (int i = 0; i < 4; i++) { pb[i][0] ^= ta; pb[i][2] ^= tb; bad |= pb[i][0] | pb[i][2]; }
        if (none((bad & 0xffu) != 0)) break;
        GIVEUP(spin, CAP_G);
        __builtin_amdgcn_s_sleep(1);
      }
      const f32x4 v = (F4(pb[0]) + F4(pb[1])) + (F4(pb[2]) + F4(pb[3]));
      float z = fold16(fold32(v[0], v[2]), fold32(v[1], v[3]));
      z += dpp<ROR8>(z);
      z += xfin;
      { unsigned te = (unsigned)tent + 1800; pace(te, 0); }
      ST16((u16)cvtpk(z, z), ro, ooff, 0, SC1);
      if (gi == 0 && lh == 0 && lane == 0) ST32(T, rws, WS_EPOCH, 0, SC1);
    }
    return;
  }

  __builtin_amdgcn_s_setprio(3);
  const unsigned ep = LD32(rws, WS_EPOCH, 0, SC1);
  const int hl = gi >> 3, ng = gi & 7;
  const int task = gi >> 1, kh = gi & 1;
  const unsigned tstart = (unsigned)__builtin_amdgcn_s_memrealtime();
  if (task < 15) {
    const bool isq = task < 8;
    u32x4 xs[4], xf, W[8];
#pragma unroll
    for (int i = 0; i < 4; i++) xs[i] = LD128(rx, (c >> 2) * 1024 + g * 64 + (c & 3) * 16 + i * 256, 0, 0);
    xf = LD128(rx, (c >> 2) * 1024 + (c & 1) * 64 + g * 16, kh * 512 + w * 128, 0);
    const int kvq = (kvh + 1) & 7, idx = kvh * 7 + task - 8, mat = idx >= 28 ? 1 : 0;
    const int cb = isq ? kvq * 8 + task : idx - 28 * mat;
    const int rstride = isq ? 8192 : FF * 2;
    const i32x4 rw = {isq ? KB[0] : mat ? KB[8] : KB[4], isq ? KB[1] : mat ? KB[9] : KB[5], isq ? KB[2] : KB[6], KB[3]};
    const int woff = ((c >> 3) * 32 + g * 8) * rstride + (c & 7) * 16;
    int wso = (kh * 256 + w * 64) * rstride + cb * 128;
    unsigned tg = tstart + GOFF + (w * GTAU) / 4;
#pragma unroll
    for (int j = 0; j < 8; j++) {
      if (!isq) pace(tg, GTAU);
      W[j] = LD128(rw, woff, wso, NTL);
      wso += rstride;
      SB();
    }
    float sm = 0.f;
#pragma unroll
    for (int i = 0; i < 4; i++)
#pragma unroll
      for (int j = 0; j < 4; j++) sm = dot2(xs[i][j], 0x3f803f80u, sm);
    sm += dpp<QX1>(sm); sm += dpp<QX2>(sm);
    const float mean = xrow_sum(sm) * (1.f / DM);
    f32x2 ssv = {0.f, 0.f};
#pragma unroll
    for (int i = 0; i < 4; i++)
#pragma unroll
      for (int j = 0; j < 4; j++) { const f32x2 d = f32x2{lo16(xs[i][j]), hi16(xs[i][j])} - mean; ssv += d * d; }
    float ss = ssv[0] + ssv[1];
    ss += dpp<QX1>(ss); ss += dpp<QX2>(ss);
    const float var = rbf(xrow_sum(ss) * (1.f / DM)), mb = rbf(mean);
    const float den = rbf(__builtin_amdgcn_sqrtf(rbf(var + 1e-5f)));
    const float rden = __builtin_amdgcn_rcpf(den);
#pragma unroll
    for (int j = 0; j < 4; j++) {
      const unsigned d = cvtpk(lo16(xf[j]) - mb, hi16(xf[j]) - mb);
      const f32x2 n = {lo16(d), hi16(d)};
      const f32x2 q = n * rden;
      const f32x2 r = (n - q * den) * rden + q;
      xf[j] = cvtpk(r[0], r[1]);
    }
    SB();
    f32x4 acc[8];
#pragma unroll
    for (int e = 0; e < 8; e++) {
      u32x4 f;
#pragma unroll
      for (int j = 0; j < 4; j++) f[j] = kpair(W[2 * j][e >> 1], W[2 * j + 1][e >> 1], e & 1);
      acc[e] = mfma32(xf, f, f32x4{0.f, 0.f, 0.f, 0.f});
    }
    SB();
    f32x4 o, z;
#pragma unroll
    for (int i = 0; i < 4; i++) {
      const float a = acc[i][0] + dpp<ROR8>(acc[i][1]), bq = acc[4 + i][1] + dpp<ROR8>(acc[4 + i][0]);
      o[i] = c < 8 ? a : bq;
    }
    if (meet4(meet_prep(lds, L_PR, F_P, w, lane, g * 16 + (c & 7) * 2 + (c >> 3)), o, z)) {
      const unsigned t0 = (__builtin_amdgcn_readfirstlane(ep) + 0x01010101u) >> ((c & 1) * 16);
      const u32x4 pv = {tagm(z[0], 0xfu, t0), tagm(z[1], 0xfu, t0 >> 4), tagm(z[2], 0xfu, t0 >> 8), tagm(z[3], 0xfu, t0 >> 12)};
      const int poff = isq ? WS_Q + (((kvq * 4 + g) * 2 + kh) * 512 + task * 64 + c * 4) * 4 : WS_GU + (((mat * 2 + kh) * 4 + g) * FF + cb * 64 + c * 4) * 4;
      ST128(pv, rws, poff, 0, SC1);
    }
  }
  const unsigned T = __builtin_amdgcn_readfirstlane(ep) + 0x01010101u;
  asm volatile("" : "+v"(pfd) : "s"(T) : "memory");
  LDSV(unsigned char, F_E + w) = 1;
  const int bb = c >> 2, q4 = c & 3, sh = g >> 1;
  if (w >= 2) {
    int hh3 = 0;
    if (w == 3) hh3 = partner(wait_place(lds), kvh) * 4 + hl;
    const int goff = w == 2 ? WS_Q + (kvh * 4 + b) * 4096 + lane * 16 : WS_GU + (g * FF + hh3 * 56 + (c < 14 ? c : 13) * 4) * 4;
    const int st1 = w == 2 ? 2048 : 4 * FF * 4, st2 = w == 2 ? 1024 : 8 * FF * 4;
    const unsigned tn = T >> (goff & 16);
    const u32x4 TN = {tn & 0xfu, (tn >> 4) & 0xfu, (tn >> 8) & 0xfu, (tn >> 12) & 0xfu};
    u32x4 q[4];
    lds_wait_byte(lds, F_G);
    unsigned spin = 0;
    NOUNROLL for (;;) {
      CBAR();
      q[0] = LD128(rws, goff, 0, SC1); q[1] = LD128(rws, goff, st1, SC1); q[2] = LD128(rws, goff, st2, SC1); q[3] = LD128(rws, goff, st1 + st2, SC1);
      unsigned bad = 0;
#pragma unroll
      for (int i = 0; i < 4; i++) { q[i] ^= TN; bad |= (q[i][0] | q[i][1]) | (q[i][2] | q[i][3]); }
      if (none((bad & 0xfu) != 0)) break;
      GIVEUP(spin, CAP_G);
      __builtin_amdgcn_s_sleep(8);
    }
    const f32x4 y0 = F4(q[0]) + F4(q[1]), y1 = F4(q[2]) + F4(q[3]);
    const u32x2 z0 = {cvtpk(y0[0], y0[1]), cvtpk(y0[2], y0[3])}, z1 = {cvtpk(y1[0], y1[1]), cvtpk(y1[2], y1[3])};
    if (w == 2) {
      LDSV(u32x2, L_Q + lane * 8) = z0;
      LDSV(u32x2, L_Q + 512 + lane * 8) = z1;
      lds_wait_eq(lds, F_E, 0x01010101u);
      LDSV(u32x2, F_Q) = u32x2{1u, T};
    } else {
      u32x2 hp;
#pragma unroll
      for (int i = 0; i < 2; i++) hp[i] = c < 14 ? cvtpk(lo16(z0[i]) * lo16(z1[i]), hi16(z0[i]) * hi16(z1[i])) : 0u;
      LDSV(u32x2, L_H + g * 128 + c * 8) = hp;
      LDSV(unsigned, F_H) = 1u;
    }
  } else if (w == 0) {
    unsigned lv, spin = 0;
    NOUNROLL for (;;) {
      lv = LDSV(unsigned, L_LW + (lane & 31) * 4);
      if (none(lv == 0u)) break;
      GIVEUP(spin, CAP_L);
      __builtin_amdgcn_s_sleep(2);
    }
    float mv = hi16(LDSV(unsigned, L_MW + (lane & 31) * 4));
    float lsum = __uint_as_float(lv);
    lsum += dpp<QX1>(lsum); lsum += dpp<QX2>(lsum); lsum += dpp<HMIR>(lsum);
    mv = fmaxf(mv, dpp<QX1>(mv)); mv = fmaxf(mv, dpp<QX2>(mv)); mv = fmaxf(mv, dpp<HMIR>(mv));
    const u32x4 st = {__float_as_uint(mv), T, __float_as_uint(lsum), T};
    if ((lane & 7) == 0) ST128(st, rws, WS_ST + slot * 128 + (lane >> 3) * 16, 0, SC1);
  } else {
    if (gi == 31) {
      ST128(u32x4{T, T, T, T}, rws, WS_PR + (lane >> 5) * 8192 + kvh * 1024 + (lane & 31) * 16, 0, SC1);
      asm volatile("s_waitcnt vmcnt(0)" ::: "memory");
      unsigned best0 = ~0u, best1 = ~0u;
      NOUNROLL for (int i = 0; i < 8; i++) {
        unsigned long long c0, c1; unsigned d;
        asm volatile("s_memtime %0\n\ts_waitcnt lgkmcnt(0)\n\ts_buffer_load_dword %2, %3, %4\n\ts_waitcnt lgkmcnt(0)\n\ts_memtime %1\n\ts_waitcnt lgkmcnt(0)"
                     : "=&s"(c0), "=&s"(c1), "=&s"(d) : "s"(rws), "s"(WS_PR + (i & 1) * 8192 + kvh * 1024 + (i >> 1) * 128) : "memory");
        const unsigned dt = (unsigned)c1 - (unsigned)c0;
        if (i >= 2) { if (i & 1) best1 = min(best1, dt); else best0 = min(best0, dt); }
      }
      const unsigned xcc = __builtin_amdgcn_s_getreg(20 | (3 << 11)) & 7u;
      if (lane == 0) ST64(u32x2{T, (tstart << 4) | (xcc << 1) | (best1 < best0 ? 1u : 0u)}, rws, WS_PF + kvh * 8, 0, SC1);
    }
    lds_wait_byte(lds, F_G + 1);
    if (lane < 8) {
      u32x2 pf;
      unsigned spin = 0;
      NOUNROLL for (;;) {
        CBAR();
        pf = LD64(rws, WS_PF + lane * 8, 0, SC1);
        if (none(pf[0] != T)) break;
        GIVEUP(spin, CAP_G);
        __builtin_amdgcn_s_sleep(8);
      }
      unsigned kx = ((pf[1] & 14u) << 2) | lane;
      kx = min(kx, dppu<QX1>(kx)); kx = min(kx, dppu<QX2>(kx)); kx = min(kx, dppu<HMIR>(kx));
      unsigned dl = (unsigned)min(max(((int)(pf[1] - (tstart << 4))) >> 4, -100), 100);
      dl += dppu<QX1>(dl); dl += dppu<QX2>(dl); dl += dppu<HMIR>(dl);
      LDSV(unsigned, F_PF) = (unsigned)__builtin_amdgcn_ballot_w64(pf[1] & 1u) | 0x100u | ((kx & 7u) << 9) | ((unsigned)((int)dl >> 3) << 16);
    }
  }
  const unsigned place = wait_place(lds);
  const int kva = partner(place, kvh), hh = kva * 4 + hl;
  const i32x4 rwo = {KB[12], KB[13], KB[14], KB[15]}, rwd = ka[8];
  const int wos = (hh * 128 + 16 * (w >> 1) + 4 * (w & 1)) * 1024 + ng * 128, wds = (hh * 56 + w * 16) * 1024 + ng * 128;
  int aoff = WS_OA + ((kva * 4 + bb) * 2 + sh) * 16384 + ((place >> kvh) & 1) * 8192 + hl * 512 + w * 128 + (g & 1) * 64 + q4 * 16;
  int pboff = WS_PB + (ng * 4 + (hh >> 3)) * 16384 + ((place >> ((place >> 9) & 7)) & 1) * 8192 + (hh & 7) * 1024 + ((lane * 16 + (hh >> 2) * 128) & 1023);
  int wooff = (32 * (g >> 1) + 2 * (g & 1)) * 1024 + c * 8;
  int wdoff = (w == 3 && g >= 2) ? OOB - 4096 : g * 4096 + c * 8;
  unsigned ta = (T >> ((lane & 1) * 16)) & 0xffu, tb = (T >> ((lane & 1) * 16 + 8)) & 0xffu, mska = 0xffu;
  asm volatile("" : "+v"(ta), "+v"(tb));
  asm volatile("" : "+v"(aoff), "+v"(pboff), "+v"(wooff), "+v"(wdoff), "+v"(mska));
  lds_wait8(lds, F_VI);
  const unsigned tvi = __builtin_amdgcn_readfirstlane(LDSV(unsigned, F_TV));
  u32x2 WD[4], WO[8];
  {
    unsigned tw = tvi + WDEL + (w * WTAU) / 4;
#pragma unroll
    for (int j = 0; j < 4; j++) { pace(tw, WTAU); WD[j] = LD64(rwd, wdoff + j * 1024, wds, NTL); SB(); }
#pragma unroll
    for (int j = 0; j < 8; j++) { pace(tw, WTAU); WO[j] = LD64(rwo, wooff + (j >> 2) * 1024, wos + (j & 1) * 8192 + ((j >> 1) & 1) * 65536, NTL); SB(); }
  }
  lds_wait_eq(lds, F_H, 1u);
  const u32x2 hf = LDSV(u32x2, L_H + bb * 128 + w * 32 + g * 8);
  lds_wait8(lds, F_PV);
  __builtin_amdgcn_s_sleep(4);
  { unsigned tA = tvi + TA_ADD; pace(tA, 0); }
  u32x4 a[4];
#pragma unroll
  for (int i = 0; i < 4; i++) a[i] = LD128(rws, aoff, i * 2048, SC1);
  SB();
  u32x4 fo[4];
  f32x4 acc[4];
#pragma unroll
  for (int e = 0; e < 4; e++) {
#pragma unroll
    for (int j = 0; j < 4; j++) fo[e][j] = kpair(WO[2 * j][e >> 1], WO[2 * j + 1][e >> 1], e & 1);
    acc[e] = mfma16(hf, u32x2{kpair(WD[0][e >> 1], WD[1][e >> 1], e & 1), kpair(WD[2][e >> 1], WD[3][e >> 1], e & 1)}, f32x4{0.f, 0.f, 0.f, 0.f});
  }
  asm volatile("" : "+v"(acc[0]), "+v"(acc[1]), "+v"(acc[2]), "+v"(acc[3]), "+v"(fo[0]), "+v"(fo[1]), "+v"(fo[2]), "+v"(fo[3]));
  const Meet mt = meet_prep(lds, L_TR, F_T, w, lane, lane);
  lds_wait_eq(lds, F_W, 1u);
  f32x4 wt = LDSV(f32x4, L_WT + bb * 32 + sh * 16);
  OPQ(wt);
  {
    unsigned spin = 0;
    NOUNROLL for (;;) {
      unsigned bad = 0;
#pragma unroll
      for (int i = 0; i < 4; i++) { a[i][0] ^= ta; a[i][2] ^= tb; bad |= a[i][0] | a[i][2]; }
      if (none((bad & 0xffu) != 0)) break;
      GIVEUP(spin, CAP_G);
      __builtin_amdgcn_s_sleep(1);
      CBAR();
#pragma unroll
      for (int i = 0; i < 4; i++) a[i] = LD128(rws, aoff, i * 2048, SC1);
    }
  }
  f32x4 t = F4(a[0]) * wt[0];
#pragma unroll
  for (int i = 1; i < 4; i++) t = __builtin_elementwise_fma(F4(a[i]), f32x4{wt[i], wt[i], wt[i], wt[i]}, t);
  const unsigned own = cvtpk(fold32(t[0], t[2]), fold32(t[1], t[3]));
  const u32x4 A = {own, dppu<QB1>(own), dppu<QB2>(own), dppu<QB3>(own)};
#pragma unroll
  for (int e = 0; e < 4; e++) acc[e] = mfma32(A, fo[e], acc[e]);
  SB();
  f32x4 z;
  if (meet4(mt, f32x4{acc[0][0], acc[1][0], acc[2][0], acc[3][0]}, z)) {
    const u32x4 o = {tagm(z[0], mska, ta), __float_as_uint(z[1]), tagm(z[2], mska, tb), __float_as_uint(z[3])};
    ST128(o, rws, pboff, 0, SC1);
  }
}

extern "C" void kernel_launch(void* const* d_in, const int* in_sizes, int n_in, void* d_out, int out_size, void* d_ws, size_t ws_size, hipStream_t stream) {
  const unsigned fl = 0x00020000u;
  fused_decode<<<256, NTHR, 0, stream>>>(d_in[1], 33554432u, fl, d_in[2], 33554432u, fl, d_ws, (unsigned)WS_END, fl, d_in[0], 4096u, fl,
                                         d_in[3], 4194304u, fl, d_in[6], 1835008u, fl, d_in[7], 1835008u, fl, d_in[4], 4194304u, fl,
                                         d_in[8], 1835008u, fl, d_out, 4096u, fl);
}
```

```cpp
#include <hip/hip_runtime.h>
#include <stdint.h>

typedef unsigned short u16;
typedef int i32x4 __attribute__((ext_vector_type(4)));
typedef int i32x16 __attribute__((ext_vector_type(16)));
typedef unsigned u32x2 __attribute__((ext_vector_type(2)));
typedef unsigned u32x4 __attribute__((ext_vector_type(4)));
typedef float f32x2 __attribute__((ext_vector_type(2)));
typedef float f32x4 __attribute__((ext_vector_type(4)));
typedef short s16x4 __attribute__((ext_vector_type(4)));
typedef short s16x8 __attribute__((ext_vector_type(8)));
typedef __bf16 bf16x2 __attribute__((ext_vector_type(2)));
#define LAS __attribute__((address_space(3)))

__device__ u16 LD16(i32x4 r, int v, int s, int a) __asm("llvm.amdgcn.raw.buffer.load.i16");
__device__ unsigned LD32(i32x4 r, int v, int s, int a) __asm("llvm.amdgcn.raw.buffer.load.i32");
__device__ u32x2 LD64(i32x4 r, int v, int s, int a) __asm("llvm.amdgcn.raw.buffer.load.v2i32");
__device__ u32x4 LD128(i32x4 r, int v, int s, int a) __asm("llvm.amdgcn.raw.buffer.load.v4i32");
__device__ void ST16(u16 d, i32x4 r, int v, int s, int a) __asm("llvm.amdgcn.raw.buffer.store.i16");
__device__ void ST32(unsigned d, i32x4 r, int v, int s, int a) __asm("llvm.amdgcn.raw.buffer.store.i32");
__device__ void ST64(u32x2 d, i32x4 r, int v, int s, int a) __asm("llvm.amdgcn.raw.buffer.store.v2i32");
__device__ void ST128(u32x4 d, i32x4 r, int v, int s, int a) __asm("llvm.amdgcn.raw.buffer.store.v4i32");
constexpr int NTL = 2, SC1 = 16;

constexpr int DM = 512, FF = 1792;
constexpr int NT = 4, NTHR = (NT + 8) * 64;

constexpr int WIN = 8;
constexpr int N0 = 2;
constexpr int TOFF = 30, TAU1 = 360, I1 = 8, TAU = 270;
constexpr int W0 = 2, DEL = 40;
constexpr int REBASE = -45;
constexpr int QGPOS = 12, HGPOS = 18;
constexpr int QKPOS = 4, SMPOS = 7, ORGPOS = 8, PVPOS = 14;
constexpr int GOFF = 40, GTAU = 30;
constexpr int WDEL = 8, WTAU = 6;
constexpr int TA_ADD = 125;
constexpr int CODE_BYTES = 12288;
constexpr int sched(int i) { return TOFF + ((i < I1 ? i - N0 + 1 : I1 - N0) * TAU1 + (i < I1 ? 0 : i - I1 + 1) * TAU) / 10; }

constexpr int WS_EPOCH = 0;
constexpr int WS_PF = 1024;
constexpr int WS_Q = 4096;
constexpr int WS_GU = WS_Q + 131072;
constexpr int WS_ST = WS_GU + 114688;
constexpr int WS_PR = 294912;
constexpr int WS_OA = WS_PR + 16384;
constexpr int WS_PB = WS_OA + 64 * 16384;
constexpr int WS_END = WS_PB + 32 * 16384;
static_assert(WS_ST + 32768 <= WS_PR && WS_PR % 16384 == 0, "map");
constexpr int L_RED = 0;
constexpr int L_TR = L_RED + 20480;
constexpr int L_PR = L_TR + 4096;
constexpr int L_Q = L_PR + 4096;
constexpr int L_H = L_Q + 1024;
constexpr int L_WT = L_H + 512;
constexpr int L_MW = L_WT + 128;
constexpr int L_LW = L_MW + 128;
constexpr int L_FLAG = L_LW + 128;
constexpr int F_Q = L_FLAG, F_H = L_FLAG + 8, F_E = L_FLAG + 12, F_VI = L_FLAG + 16, F_PV = L_FLAG + 24, F_T = L_FLAG + 32, F_P = L_FLAG + 36,
              F_TV = L_FLAG + 48, F_W = L_FLAG + 52, F_G = L_FLAG + 56, F_PF = L_FLAG + 60;
constexpr int L_END = L_FLAG + 64;
constexpr unsigned CAP_G = 1u << 18, CAP_L = 1u << 22;
constexpr int OOB = 0x7ffffff0;
constexpr float LOG2E = 1.4426950408889634f;

__device__ __forceinline__ float lo16(unsigned u) { return __uint_as_float(u << 16); }
__device__ __forceinline__ float hi16(unsigned u) { return __uint_as_float(u & 0xffff0000u); }
__device__ __forceinline__ unsigned cvtpk(float lo, float hi) { f32x2 f = {lo, hi}; return __builtin_bit_cast(unsigned, __builtin_convertvector(f, bf16x2)); }
__device__ __forceinline__ float rbf(float f) { return lo16(cvtpk(f, f)); }
__device__ __forceinline__ f32x4 mfma32(u32x4 a, u32x4 b, f32x4 c) { return __builtin_amdgcn_mfma_f32_16x16x32_bf16(__builtin_bit_cast(s16x8, a), __builtin_bit_cast(s16x8, b), c, 0, 0, 0); }
__device__ __forceinline__ f32x4 mfma16(u32x2 a, u32x2 b, f32x4 c) { return __builtin_amdgcn_mfma_f32_16x16x16bf16_1k(__builtin_bit_cast(s16x4, a), __builtin_bit_cast(s16x4, b), c, 0, 0, 0); }
template <int CTRL> __device__ __forceinline__ float dpp(float x) { return __builtin_bit_cast(float, __builtin_amdgcn_mov_dpp(__builtin_bit_cast(int, x), CTRL, 0xf, 0xf, true)); }
template <int CTRL> __device__ __forceinline__ unsigned dppu(unsigned x) { return (unsigned)__builtin_amdgcn_mov_dpp((int)x, CTRL, 0xf, 0xf, true); }
constexpr int ROR8 = 0x128, HMIR = 0x141, QX1 = 0xB1, QX2 = 0x4E, QB0 = 0x00, QB1 = 0x55, QB2 = 0xAA, QB3 = 0xFF;
__device__ __forceinline__ float fold32(float a, float b) {
  auto t = __builtin_amdgcn_permlane32_swap(__float_as_uint(a), __float_as_uint(b), false, false);
  return __uint_as_float(t[0]) + __uint_as_float(t[1]);
}
__device__ __forceinline__ float fold16(float a, float b) {
  auto t = __builtin_amdgcn_permlane16_swap(__float_as_uint(a), __float_as_uint(b), false, false);
  return __uint_as_float(t[0]) + __uint_as_float(t[1]);
}
__device__ __forceinline__ float xrow_sum(float x) { x = fold16(x, x); return fold32(x, x); }
__device__ __forceinline__ float xrow_max(float x) {
  auto s = __builtin_amdgcn_permlane16_swap(__float_as_uint(x), __float_as_uint(x), false, false);
  x = fmaxf(__uint_as_float(s[0]), __uint_as_float(s[1]));
  auto t = __builtin_amdgcn_permlane32_swap(__float_as_uint(x), __float_as_uint(x), false, false);
  return fmaxf(__uint_as_float(t[0]), __uint_as_float(t[1]));
}
__device__ __forceinline__ float dot2(unsigned a, unsigned b, float c) { return __builtin_amdgcn_fdot2_f32_bf16(__builtin_bit_cast(bf16x2, a), __builtin_bit_cast(bf16x2, b), c, false); }
__device__ __forceinline__ unsigned tagm(float v, unsigned m, unsigned t) { return (__float_as_uint(v) & ~m) | (t & m); }
__device__ __forceinline__ unsigned kpair(unsigned r0, unsigned r1, int sel) { return __builtin_amdgcn_perm(r1, r0, sel ? 0x07060302u : 0x05040100u); }
__device__ __forceinline__ bool none(bool p) { return __builtin_amdgcn_ballot_w64(p) == 0ull; }
#define F4(v) __builtin_bit_cast(f32x4, v)
#define SB() __builtin_amdgcn_sched_barrier(0)
#define CBAR() asm volatile("" ::: "memory")
#define OPQ(x) asm volatile("" : "+v"(x))
#define LDSV(T, off) (*(volatile LAS T*)(lds + (off)))
#define NOUNROLL _Pragma("clang loop unroll(disable)")
#define GIVEUP(spin, cap) asm volatile("s_add_u32 %0, %0, 1\n\ts_cmp_lt_u32 %0, %1\n\ts_cbranch_scc1 1f\n\ts_endpgm\n1:" : "+s"(spin) : "n"(cap) : "scc")

__device__ __forceinline__ unsigned now32() { unsigned long long t; asm volatile("s_memrealtime %0\n\ts_waitcnt lgkmcnt(0)" : "=s"(t)::"memory"); return (unsigned)t; }
__device__ __forceinline__ void pace(unsigned& tgt, int step) {
  asm volatile("1:\n\ts_memrealtime vcc\n\ts_waitcnt lgkmcnt(0)\n\ts_sub_u32 vcc_lo, vcc_lo, %0\n\ts_cmp_lt_i32 vcc_lo, 0\n\ts_cbranch_scc1 1b\n\ts_add_u32 %0, %0, %1"
               : "+s"(tgt) : "n"(step) : "memory", "scc", "vcc");
}
__device__ __forceinline__ void apace(unsigned& tgt, int step) {
  unsigned tmp;
  asm volatile("1:\n\ts_memrealtime vcc\n\ts_getreg_b32 %1, hwreg(HW_REG_IB_STS, 0, 4)\n\ts_sub_i32 %1, %1, %3\n\ts_max_i32 %1, %1, 0\n\ts_mul_i32 %1, %1, %4\n\t"
               "s_waitcnt lgkmcnt(0)\n\ts_sub_u32 vcc_lo, vcc_lo, %0\n\ts_cmp_lt_i32 vcc_lo, %1\n\ts_cbranch_scc1 1b\n\ts_add_u32 %0, %0, %2"
               : "+s"(tgt), "=&s"(tmp) : "n"(step), "n"(W0 - 1), "n"(DEL) : "memory", "scc", "vcc");
}
__device__ __forceinline__ void lds_wait_eq(LAS unsigned char* lds, int off, unsigned n) {
  unsigned spin = 0;
  NOUNROLL while (LDSV(unsigned, off) != n) { GIVEUP(spin, CAP_L); __builtin_amdgcn_s_sleep(1); }
  CBAR();
}
__device__ __forceinline__ void lds_wait_byte(LAS unsigned char* lds, int off) {
  unsigned spin = 0;
  NOUNROLL while (LDSV(unsigned char, off) == 0) { GIVEUP(spin, CAP_L); __builtin_amdgcn_s_sleep(2); }
  CBAR();
}
__device__ __forceinline__ void lds_wait8(LAS unsigned char* lds, int off) {
  unsigned spin = 0;
  NOUNROLL for (;;) {
    const u32x2 f = LDSV(u32x2, off);
    if ((f[0] & f[1]) == 0x01010101u) break;
    GIVEUP(spin, CAP_L);
    __builtin_amdgcn_s_sleep(1);
  }
  CBAR();
}
struct Meet { unsigned wr, rd, cnt, one; };
__device__ __forceinline__ Meet meet_prep(LAS unsigned char* lds, int area, int cnt, int w, int lane, int wpos) {
  Meet m = {(unsigned)(uintptr_t)(lds + area) + w * 1024 + wpos * 16, (unsigned)(uintptr_t)(lds + area) + lane * 16, (unsigned)(uintptr_t)(lds + cnt), 1u};
  asm volatile("" : "+v"(m.wr), "+v"(m.rd), "+v"(m.cnt), "+v"(m.one));
  return m;
}
__device__ __forceinline__ bool meet4(const Meet& m, f32x4 v, f32x4& sum) {
  *(volatile LAS f32x4*)(uintptr_t)m.wr = v;
  unsigned prev;
  f32x4 r0, r1, r2, r3;
  asm volatile("s_mov_b64 exec, 1\n\tds_add_rtn_u32 %0, %6, %7\n\ts_mov_b64 exec, -1\n\t"
               "ds_read_b128 %1, %5\n\tds_read_b128 %2, %5 offset:1024\n\tds_read_b128 %3, %5 offset:2048\n\tds_read_b128 %4, %5 offset:3072\n\t"
               "s_waitcnt lgkmcnt(0)"
               : "=&v"(prev), "=&v"(r0), "=&v"(r1), "=&v"(r2), "=&v"(r3) : "v"(m.rd), "v"(m.cnt), "v"(m.one) : "memory");
  sum = (r0 + r1) + (r2 + r3);
  return __builtin_amdgcn_readfirstlane(prev) == 3u;
}
__device__ __forceinline__ unsigned wait_place(LAS unsigned char* lds) {
  unsigned p = LDSV(unsigned, F_PF), spin = 0;
  NOUNROLL while (p == 0u) { GIVEUP(spin, CAP_L); __builtin_amdgcn_s_sleep(1); p = LDSV(unsigned, F_PF); }
  return __builtin_amdgcn_readfirstlane(p);
}
__device__ __forceinline__ int partner(unsigned place, int g) {
  const unsigned m = (((place >> g) & 1u) ? place : ~place) & 0xffu;
  return (g + 1 + __builtin_ctz(((m | (m << 8)) >> (g + 1)) & 0xffu)) & 7;
}

#define DESC(n) const void* p_##n, unsigned n##_bytes, unsigned n##_flags
__global__ void __launch_bounds__(NTHR, 3) fused_decode(DESC(k), DESC(v), DESC(ws), DESC(x), DESC(wq), DESC(wg), DESC(wu), DESC(wo), DESC(wd), DESC(out)) {
  __shared__ __attribute__((aligned(16))) unsigned char smem[L_END];
  auto ka = (const __attribute__((address_space(4))) i32x4*)__builtin_amdgcn_kernarg_segment_ptr();
  i32x16 KA, KB;
  unsigned long long pc0;
  asm volatile("s_getpc_b64 %0" : "=&s"(pc0));
  SB();
  LAS unsigned char* lds = (LAS unsigned char*)smem;
  const int blk = blockIdx.x, kvh = blk & 7, gi = blk >> 3, b = gi & 3, s = gi >> 2;
  const int tid = threadIdx.x, lane = tid & 63, w = __builtin_amdgcn_readfirstlane(tid >> 6), g = lane >> 4, c = lane & 15;
  u32x2 pfd = {0u, 0u};
  if (w == 3 && lane < CODE_BYTES / 256) {
    const int off = gi * (CODE_BYTES / 32) + lane * 8;
    asm volatile("global_load_dwordx2 %0, %1, %2" : "=v"(pfd) : "v"(off), "s"(pc0 & ~0xffull));
  }
  if (w == 2) {
    LDSV(unsigned, L_MW + lane * 4) = 0u;
    LDSV(unsigned, L_FLAG + c * 4) = 0u;
  }
  const int kv = w - NT;
  int koff = (c >> 2) * 256 + g * 64 + (c & 3) * 16;
  int kvbase = ((b * 8 + kvh) * 4096 + s * 512) * 256 + kv * 4096;
  int so0 = kvbase + (gi & 3) * 1024;
  OPQ(koff);
  unsigned long long tent;
  asm volatile("s_waitcnt lgkmcnt(0)\n\ts_barrier\n\ts_memrealtime %4\n\ts_waitcnt lgkmcnt(0)\n\ts_load_dwordx16 %0, %5, 0x0\n\ts_load_dwordx16 %1, %5, 0x40\n\ts_waitcnt lgkmcnt(0)" : "=&s"(KA), "=&s"(KB), "+s"(kvbase), "+s"(so0), "=&s"(tent) : "s"(ka) : "memory");
  const i32x4 rws = {KA[8], KA[9], KA[10], KA[11]}, rx = {KA[12], KA[13], KA[14], KA[15]};
  const int slot = (kvh * 4 + b) * 8 + s;

  if (__builtin_expect(w >= NT, 1)) {
    const i32x4 rk = {KA[0], KA[1], KA[2], KA[3]}, rv = {KA[4], KA[5], KA[6], KA[7]};
    u32x4 K[16], V[16];
    const int voff = lane * 16;
#define SOFF(i) (((i) < 4 ? so0 : kvbase + ((((i) >> 2) + gi) & 3) * 1024) + ((i) & 3) * 32768)
#define WAITN(n) asm volatile("s_waitcnt vmcnt(%0)" ::"n"((n) - 1) : "memory")
#define PACE(i) apace(tgt, sched((i) + 1) - sched(i))
#define VLOAD(i) do { WAITN(WIN); PACE(16 + (i)); V[i] = LD128(rv, voff, SOFF(i), NTL); \
    if ((i) + 17 == HGPOS && kv == 7) LDSV(unsigned char, F_G + 1) = 1; \
    if ((i) == 15) { LDSV(unsigned, F_TV) = now32(); LDSV(unsigned char, F_VI + kv) = 1; } SB(); } while (0)
    unsigned long long t64;
    asm volatile("s_memrealtime %0" : "=s"(t64));
#pragma unroll
    for (int i = 0; i < N0; i++) { K[i] = LD128(rk, koff, SOFF(i), NTL); SB(); }
    asm volatile("s_waitcnt lgkmcnt(0)" : "+s"(t64));
    unsigned tgt = (unsigned)t64 + sched(N0) + (kv * TAU) / 80;
    WAITN(N0);
    { const unsigned t1 = now32() + REBASE + (kv * TAU) / 80; if ((int)(t1 - tgt) > 0) tgt = t1; }
#pragma unroll
    for (int i = N0; i < 16; i++) {
      if (i >= WIN) WAITN(WIN);
      PACE(i);
      K[i] = LD128(rk, koff, SOFF(i), NTL);
      if (i + 1 == QGPOS && kv == 7) LDSV(unsigned char, F_G) = 1;
      SB();
    }
#pragma unroll
    for (int i = 0; i < QKPOS; i++) VLOAD(i);
    u32x2 fl = LDSV(u32x2, F_Q);
    { unsigned spin = 0; NOUNROLL while (fl[0] == 0u) { GIVEUP(spin, CAP_L); __builtin_amdgcn_s_sleep(1); fl = LDSV(u32x2, F_Q); } }
    CBAR();
    const unsigned T = __builtin_amdgcn_readfirstlane(fl[1]);
    float S[4];
    {
      const u32x4 qf = LDSV(u32x4, L_Q + koff);
      const bool a1 = c & 1, a2 = c & 2;
#pragma unroll
      for (int t = 0; t < 4; t++) {
        float y[4];
#pragma unroll
        for (int i = 0; i < 4; i++) {
          const f32x4 d = mfma32(K[t * 4 + i], qf, f32x4{0.f, 0.f, 0.f, 0.f});
          float z = dpp<QB0>(d[0]);
          z += dpp<QB1>(d[1]); OPQ(z);
          z += dpp<QB2>(d[2]); OPQ(z);
          z += dpp<QB3>(d[3]); OPQ(z);
          y[i] = z;
        }
        const float u0 = a1 ? y[1] : y[0], u1 = a1 ? y[3] : y[2];
        S[t] = a2 ? u1 : u0;
      }
#pragma unroll
      for (int i = 0; i < 4; i += 2) {
        const unsigned p = cvtpk(S[i], S[i + 1]);
        const unsigned p2 = cvtpk(lo16(p) * 0.08838834764831845f, hi16(p) * 0.08838834764831845f);
        S[i] = lo16(p2); S[i + 1] = hi16(p2);
      }
      float mw = fmaxf(fmaxf(S[0], S[1]), fmaxf(S[2], S[3]));
      mw = fmaxf(mw, dpp<QX1>(mw)); mw = fmaxf(mw, dpp<QX2>(mw));
      mw = xrow_max(mw);
      LDSV(unsigned, L_MW + (c >> 2) * 32 + kv * 4) = __float_as_uint(mw) | 1u;
    }
    SB();
#pragma unroll
    for (int i = QKPOS; i < SMPOS; i++) VLOAD(i);
    u32x4 P[2];
    {
      u32x4 m0, m1;
      unsigned spin = 0;
      NOUNROLL for (;;) {
        m0 = LDSV(u32x4, L_MW + (c >> 2) * 32); m1 = LDSV(u32x4, L_MW + (c >> 2) * 32 + 16);
        const u32x4 mm = m0 & m1;
        if (none((mm[0] & mm[1] & mm[2] & mm[3] & 1u) == 0u)) break;
        GIVEUP(spin, CAP_L);
        __builtin_amdgcn_s_sleep(1);
      }
      float M = -INFINITY;
#pragma unroll
      for (int i = 0; i < 4; i++) M = fmaxf(M, fmaxf(hi16(m0[i]), hi16(m1[i])));
      float p[4];
#pragma unroll
      for (int i = 0; i < 4; i++) p[i] = __builtin_amdgcn_exp2f((S[i] - M) * LOG2E);
      float lsum = (p[0] + p[1]) + (p[2] + p[3]);
      lsum += dpp<QX1>(lsum); lsum += dpp<QX2>(lsum);
      lsum = xrow_sum(lsum);
      LDSV(unsigned, L_LW + (c >> 2) * 32 + kv * 4) = __float_as_uint(lsum) | 1u;
#pragma unroll
      for (int u = 0; u < 2; u++) { const unsigned pk = cvtpk(p[2 * u], p[2 * u + 1]); P[u] = u32x4{pk, dppu<QB1>(pk), dppu<QB2>(pk), dppu<QB3>(pk)}; }
    }
    unsigned mska = 0xffu;
    asm volatile("" : "+v"(mska), "+v"(P[0]), "+v"(P[1]));
    SB();
#pragma unroll
    for (int i = SMPOS; i < PVPOS; i++) {
      if (i == ORGPOS) tgt += ((int)__builtin_amdgcn_readfirstlane(LDSV(unsigned, F_PF)) >> 16) - 6;
      VLOAD(i);
    }
    f32x4 O[8];
#pragma unroll
    for (int e = 0; e < 8; e++) {
      u32x4 f;
#pragma unroll
      for (int j = 0; j < 4; j++) f[j] = kpair(V[j][e >> 1], V[4 + j][e >> 1], e & 1);
      O[e] = mfma32(P[0], f, f32x4{0.f, 0.f, 0.f, 0.f});
    }
    asm volatile("" : "+v"(O[0]), "+v"(O[1]), "+v"(O[2]), "+v"(O[3]), "+v"(O[4]), "+v"(O[5]), "+v"(O[6]), "+v"(O[7]));
    SB();
    const unsigned place = wait_place(lds);
    const int side = ((place >> kvh) & 1) * 8192;
    int oaoff = WS_OA + (slot >> 2) * 16384 + side + (slot & 3) * 2048 + kv * 512 + lane * 8;
    OPQ(oaoff);
    const int soff = WS_ST + (partner(place, kvh) * 32 + (lane & 31)) * 128 + (gi >> 3) * 16;
    u32x4 s0 = {0u, ~T, 0u, 0u};
    if (kv == 5 && lane < 32) s0 = LD128(rws, soff, 0, SC1);
    SB();
    int redw = kv * 2560 + lane * 4, redw2 = redw + 1280, redr = (lane >> 5) * 10240 + ((lane & 31) >> 2) * 320 + kv * 64 + (lane & 3) * 16;
    unsigned tab = T >> ((lane & 3) * 8);
    asm volatile("" : "+v"(redw), "+v"(redw2), "+v"(redr), "+v"(tab));
#pragma unroll
    for (int i = PVPOS; i < 16; i++) VLOAD(i);
    {
      u32x4 F[8];
#pragma unroll
      for (int j = 0; j < 3; j++) {
#pragma unroll
        for (int e = 0; e < 8; e++) F[e][j] = kpair(V[8 + j][e >> 1], V[12 + j][e >> 1], e & 1);
        SB();
      }
#pragma unroll
      for (int i = 0; i < 4; i++) {
        F[2 * i][3] = kpair(V[11][i], V[15][i], 0); F[2 * i + 1][3] = kpair(V[11][i], V[15][i], 1);
        O[2 * i] = mfma32(P[1], F[2 * i], O[2 * i]); O[2 * i + 1] = mfma32(P[1], F[2 * i + 1], O[2 * i + 1]);
        SB();
      }
    }
#pragma unroll
    for (int e = 0; e < 4; e++) *(LAS float*)(lds + L_RED + redw + e * 320) = O[e][0];
#pragma unroll
    for (int e = 0; e < 4; e++) *(LAS float*)(lds + L_RED + redw2 + e * 320) = O[4 + e][0];
    CBAR();
    LDSV(unsigned char, F_PV + kv) = 1;
    if (kv < 4) {
      f32x4 r[4];
      unsigned spin = 0;
      NOUNROLL for (;;) {
        const u32x2 cnt = LDSV(u32x2, F_PV);
#pragma unroll
        for (int i = 0; i < 4; i++) r[i] = LDSV(f32x4, L_RED + redr + i * 2560);
        if ((cnt[0] & cnt[1]) == 0x01010101u) break;
        GIVEUP(spin, CAP_L);
      }
      const f32x4 a = (r[0] + r[1]) + (r[2] + r[3]);
      ST64(u32x2{tagm(fold32(a[0], a[2]), mska, tab), __float_as_uint(fold32(a[1], a[3]))}, rws, oaoff, 0, SC1);
    }
    if (kv == 5) {
      if (lane < 32) {
        unsigned spin = 0;
        NOUNROLL while (!none(((s0[1] ^ T) | (s0[3] ^ T)) != 0u)) {
          GIVEUP(spin, CAP_G);
          __builtin_amdgcn_s_sleep(2);
          CBAR();
          s0 = LD128(rws, soff, 0, SC1);
        }
        const float m1 = __uint_as_float(s0[0]);
        float MM = fmaxf(m1, dpp<QX1>(m1));
        MM = fmaxf(MM, dpp<QX2>(MM)); MM = fmaxf(MM, dpp<HMIR>(MM));
        const float e1 = __builtin_amdgcn_exp2f((m1 - MM) * LOG2E);
        float L = e1 * __uint_as_float(s0[2]);
        L += dpp<QX1>(L); L += dpp<QX2>(L); L += dpp<HMIR>(L);
        LDSV(float, L_WT + lane * 4) = e1 * __builtin_amdgcn_rcpf(L);
      }
      LDSV(unsigned, F_W) = 1u;
    }
    if (kv >= 6 && kvh == (int)((place >> 9) & 7u)) {
      const int lh = kv - 6, pg = lane >> 3;
      const int oi = (gi >> 3) * 512 + (gi & 7) * 64 + lh * 32 + (c & 7) * 4 + g;
      const i32x4 ro = ka[9];
      float xfin = lo16(LD16(rx, oi * 2, 0, 0));
      int go[4];
#pragma unroll
      for (int i = 0; i < 4; i++) go[i] = WS_PB + ((gi & 7) * 4 + i) * 16384 + side + pg * 1024 + (((gi >> 3) * 2 + lh + 2 * i + (pg >> 2)) & 7) * 128 + (lane & 7) * 16;
      int ooff = c < 8 ? oi * 2 : OOB;
      unsigned ta = (T >> ((lane & 1) * 16)) & 0xffu, tb = (T >> ((lane & 1) * 16 + 8)) & 0xffu;
      asm volatile("" : "+v"(xfin), "+v"(go[0]), "+v"(go[1]), "+v"(go[2]), "+v"(go[3]), "+v"(ooff), "+v"(ta), "+v"(tb));
      lds_wait_eq(lds, F_T, 4u);
      __builtin_amdgcn_s_sleep(18);
      u32x4 pb[4];
      unsigned spin = 0;
      NOUNROLL for (;;) {
        CBAR();
#pragma unroll
        for (int i = 0; i < 4; i++) pb[i] = LD128(rws, go[i], 0, SC1);
        unsigned bad = 0;
#pragma unroll
        for (int i = 0; i < 4; i++) { pb[i][0] ^= ta; pb[i][2] ^= tb; bad |= pb[i][0] | pb[i][2]; }
        if (none((bad & 0xffu) != 0)) break;
        GIVEUP(spin, CAP_G);
        __builtin_amdgcn_s_sleep(1);
      }
      const f32x4 v = (F4(pb[0]) + F4(pb[1])) + (F4(pb[2]) + F4(pb[3]));
      float z = fold16(fold32(v[0], v[2]), fold32(v[1], v[3]));
      z += dpp<ROR8>(z);
      z += xfin;
      { unsigned te = (unsigned)tent + 1800; pace(te, 0); }
      ST16((u16)cvtpk(z, z), ro, ooff, 0, SC1);
      if (gi == 0 && lh == 0 && lane == 0) ST32(T, rws, WS_EPOCH, 0, SC1);
    }
    return;
  }

  __builtin_amdgcn_s_setprio(3);
  const unsigned ep = LD32(rws, WS_EPOCH, 0, SC1);
  const int hl = gi >> 3, ng = gi & 7;
  const int task = gi >> 1, kh = gi & 1;
  const unsigned tstart = (unsigned)__builtin_amdgcn_s_memrealtime();
  if (task < 15) {
    const bool isq = task < 8;
    u32x4 xs[4], xf, W[8];
#pragma unroll
    for (int i = 0; i < 4; i++) xs[i] = LD128(rx, (c >> 2) * 1024 + g * 64 + (c & 3) * 16 + i * 256, 0, 0);
    xf = LD128(rx, (c >> 2) * 1024 + (c & 1) * 64 + g * 16, kh * 512 + w * 128, 0);
    const int kvq = (kvh + 1) & 7, idx = kvh * 7 + task - 8, mat = idx >= 28 ? 1 : 0;
    const int cb = isq ? kvq * 8 + task : idx - 28 * mat;
    const int rstride = isq ? 8192 : FF * 2;
    const i32x4 rw = {isq ? KB[0] : mat ? KB[8] : KB[4], isq ? KB[1] : mat ? KB[9] : KB[5], isq ? KB[2] : KB[6], KB[3]};
    const int woff = ((c >> 3) * 32 + g * 8) * rstride + (c & 7) * 16;
    int wso = (kh * 256 + w * 64) * rstride + cb * 128;
    unsigned tg = tstart + GOFF + (w * GTAU) / 4;
#pragma unroll
    for (int j = 0; j < 8; j++) {
      if (!isq) pace(tg, GTAU);
      W[j] = LD128(rw, woff, wso, NTL);
      wso += rstride;
      SB();
    }
    float sm = 0.f;
#pragma unroll
    for (int i = 0; i < 4; i++)
#pragma unroll
      for (int j = 0; j < 4; j++) sm = dot2(xs[i][j], 0x3f803f80u, sm);
    sm += dpp<QX1>(sm); sm += dpp<QX2>(sm);
    const float mean = xrow_sum(sm) * (1.f / DM);
    f32x2 ssv = {0.f, 0.f};
#pragma unroll
    for (int i = 0; i < 4; i++)
#pragma unroll
      for (int j = 0; j < 4; j++) { const f32x2 d = f32x2{lo16(xs[i][j]), hi16(xs[i][j])} - mean; ssv += d * d; }
    float ss = ssv[0] + ssv[1];
    ss += dpp<QX1>(ss); ss += dpp<QX2>(ss);
    const float var = rbf(xrow_sum(ss) * (1.f / DM)), mb = rbf(mean);
    const float den = rbf(__builtin_amdgcn_sqrtf(rbf(var + 1e-5f)));
    const float rden = __builtin_amdgcn_rcpf(den);
#pragma unroll
    for (int j = 0; j < 4; j++) {
      const unsigned d = cvtpk(lo16(xf[j]) - mb, hi16(xf[j]) - mb);
      const f32x2 n = {lo16(d), hi16(d)};
      const f32x2 q = n * rden;
      const f32x2 r = (n - q * den) * rden + q;
      xf[j] = cvtpk(r[0], r[1]);
    }
    SB();
    f32x4 acc[8];
#pragma unroll
    for (int e = 0; e < 8; e++) {
      u32x4 f;
#pragma unroll
      for (int j = 0; j < 4; j++) f[j] = kpair(W[2 * j][e >> 1], W[2 * j + 1][e >> 1], e & 1);
      acc[e] = mfma32(xf, f, f32x4{0.f, 0.f, 0.f, 0.f});
    }
    SB();
    f32x4 o, z;
#pragma unroll
    for (int i = 0; i < 4; i++) {
      const float a = acc[i][0] + dpp<ROR8>(acc[i][1]), bq = acc[4 + i][1] + dpp<ROR8>(acc[4 + i][0]);
      o[i] = c < 8 ? a : bq;
    }
    if (meet4(meet_prep(lds, L_PR, F_P, w, lane, g * 16 + (c & 7) * 2 + (c >> 3)), o, z)) {
      const unsigned t0 = (__builtin_amdgcn_readfirstlane(ep) + 0x01010101u) >> ((c & 1) * 16);
      const u32x4 pv = {tagm(z[0], 0xfu, t0), tagm(z[1], 0xfu, t0 >> 4), tagm(z[2], 0xfu, t0 >> 8), tagm(z[3], 0xfu, t0 >> 12)};
      const int poff = isq ? WS_Q + (((kvq * 4 + g) * 2 + kh) * 512 + task * 64 + c * 4) * 4 : WS_GU + (((mat * 2 + kh) * 4 + g) * FF + cb * 64 + c * 4) * 4;
      ST128(pv, rws, poff, 0, SC1);
    }
  }
  const unsigned T = __builtin_amdgcn_readfirstlane(ep) + 0x01010101u;
  asm volatile("" : "+v"(pfd) : "s"(T) : "memory");
  LDSV(unsigned char, F_E + w) = 1;
  const int bb = c >> 2, q4 = c & 3, sh = g >> 1;
  if (w >= 2) {
    int hh3 = 0;
    if (w == 3) hh3 = partner(wait_place(lds), kvh) * 4 + hl;
    const int goff = w == 2 ? WS_Q + (kvh * 4 + b) * 4096 + lane * 16 : WS_GU + (g * FF + hh3 * 56 + (c < 14 ? c : 13) * 4) * 4;
    const int st1 = w == 2 ? 2048 : 4 * FF * 4, st2 = w == 2 ? 1024 : 8 * FF * 4;
    const unsigned tn = T >> (goff & 16);
    const u32x4 TN = {tn & 0xfu, (tn >> 4) & 0xfu, (tn >> 8) & 0xfu, (tn >> 12) & 0xfu};
    u32x4 q[4];
    lds_wait_byte(lds, F_G);
    unsigned spin = 0;
    NOUNROLL for (;;) {
      CBAR();
      q[0] = LD128(rws, goff, 0, SC1); q[1] = LD128(rws, goff, st1, SC1); q[2] = LD128(rws, goff, st2, SC1); q[3] = LD128(rws, goff, st1 + st2, SC1);
      unsigned bad = 0;
#pragma unroll
      for (int i = 0; i < 4; i++) { q[i] ^= TN; bad |= (q[i][0] | q[i][1]) | (q[i][2] | q[i][3]); }
      if (none((bad & 0xfu) != 0)) break;
      GIVEUP(spin, CAP_G);
      __builtin_amdgcn_s_sleep(8);
    }
    const f32x4 y0 = F4(q[0]) + F4(q[1]), y1 = F4(q[2]) + F4(q[3]);
    const u32x2 z0 = {cvtpk(y0[0], y0[1]), cvtpk(y0[2], y0[3])}, z1 = {cvtpk(y1[0], y1[1]), cvtpk(y1[2], y1[3])};
    if (w == 2) {
      LDSV(u32x2, L_Q + lane * 8) = z0;
      LDSV(u32x2, L_Q + 512 + lane * 8) = z1;
      lds_wait_eq(lds, F_E, 0x01010101u);
      LDSV(u32x2, F_Q) = u32x2{1u, T};
    } else {
      u32x2 hp;
#pragma unroll
      for (int i = 0; i < 2; i++) hp[i] = c < 14 ? cvtpk(lo16(z0[i]) * lo16(z1[i]), hi16(z0[i]) * hi16(z1[i])) : 0u;
      LDSV(u32x2, L_H + g * 128 + c * 8) = hp;
      LDSV(unsigned, F_H) = 1u;
    }
  } else if (w == 0) {
    unsigned lv, spin = 0;
    NOUNROLL for (;;) {
      lv = LDSV(unsigned, L_LW + (lane & 31) * 4);
      if (none(lv == 0u)) break;
      GIVEUP(spin, CAP_L);
      __builtin_amdgcn_s_sleep(2);
    }
    float mv = hi16(LDSV(unsigned, L_MW + (lane & 31) * 4));
    float lsum = __uint_as_float(lv);
    lsum += dpp<QX1>(lsum); lsum += dpp<QX2>(lsum); lsum += dpp<HMIR>(lsum);
    mv = fmaxf(mv, dpp<QX1>(mv)); mv = fmaxf(mv, dpp<QX2>(mv)); mv = fmaxf(mv, dpp<HMIR>(mv));
    const u32x4 st = {__float_as_uint(mv), T, __float_as_uint(lsum), T};
    if ((lane & 7) == 0) ST128(st, rws, WS_ST + slot * 128 + (lane >> 3) * 16, 0, SC1);
  } else {
    if (gi == 31) {
      ST128(u32x4{T, T, T, T}, rws, WS_PR + (lane >> 5) * 8192 + kvh * 1024 + (lane & 31) * 16, 0, SC1);
      asm volatile("s_waitcnt vmcnt(0)" ::: "memory");
      unsigned best0 = ~0u, best1 = ~0u;
      NOUNROLL for (int i = 0; i < 8; i++) {
        unsigned long long c0, c1; unsigned d;
        asm volatile("s_memtime %0\n\ts_waitcnt lgkmcnt(0)\n\ts_buffer_load_dword %2, %3, %4\n\ts_waitcnt lgkmcnt(0)\n\ts_memtime %1\n\ts_waitcnt lgkmcnt(0)"
                     : "=&s"(c0), "=&s"(c1), "=&s"(d) : "s"(rws), "s"(WS_PR + (i & 1) * 8192 + kvh * 1024 + (i >> 1) * 128) : "memory");
        const unsigned dt = (unsigned)c1 - (unsigned)c0;
        if (i >= 2) { if (i & 1) best1 = min(best1, dt); else best0 = min(best0, dt); }
      }
      const unsigned xcc = __builtin_amdgcn_s_getreg(20 | (3 << 11)) & 7u;
      if (lane == 0) ST64(u32x2{T, (tstart << 4) | (xcc << 1) | (best1 < best0 ? 1u : 0u)}, rws, WS_PF + kvh * 8, 0, SC1);
    }
    lds_wait_byte(lds, F_G + 1);
    if (lane < 8) {
      u32x2 pf;
      unsigned spin = 0;
      NOUNROLL for (;;) {
        CBAR();
        pf = LD64(rws, WS_PF + lane * 8, 0, SC1);
        if (none(pf[0] != T)) break;
        GIVEUP(spin, CAP_G);
        __builtin_amdgcn_s_sleep(8);
      }
      unsigned kx = ((pf[1] & 14u) << 2) | lane;
      kx = min(kx, dppu<QX1>(kx)); kx = min(kx, dppu<QX2>(kx)); kx = min(kx, dppu<HMIR>(kx));
      unsigned dl = (unsigned)min(max(((int)(pf[1] - (tstart << 4))) >> 4, -100), 100);
      dl += dppu<QX1>(dl); dl += dppu<QX2>(dl); dl += dppu<HMIR>(dl);
      LDSV(unsigned, F_PF) = (unsigned)__builtin_amdgcn_ballot_w64(pf[1] & 1u) | 0x100u | ((kx & 7u) << 9) | ((unsigned)((int)dl >> 3) << 16);
    }
  }
  const unsigned place = wait_place(lds);
  const int kva = partner(place, kvh), hh = kva * 4 + hl;
  const i32x4 rwo = {KB[12], KB[13], KB[14], KB[15]}, rwd = ka[8];
  const int wos = (hh * 128 + 16 * (w >> 1) + 4 * (w & 1)) * 1024 + ng * 128, wds = (hh * 56 + w * 16) * 1024 + ng * 128;
  int aoff = WS_OA + ((kva * 4 + bb) * 2 + sh) * 16384 + ((place >> kvh) & 1) * 8192 + hl * 512 + w * 128 + (g & 1) * 64 + q4 * 16;
  int pboff = WS_PB + (ng * 4 + (hh >> 3)) * 16384 + ((place >> ((place >> 9) & 7)) & 1) * 8192 + (hh & 7) * 1024 + ((lane * 16 + (hh >> 2) * 128) & 1023);
  int wooff = (32 * (g >> 1) + 2 * (g & 1)) * 1024 + c * 8;
  int wdoff = (w == 3 && g >= 2) ? OOB - 4096 : g * 4096 + c * 8;
  unsigned ta = (T >> ((lane & 1) * 16)) & 0xffu, tb = (T >> ((lane & 1) * 16 + 8)) & 0xffu, mska = 0xffu;
  asm volatile("" : "+v"(ta), "+v"(tb));
  asm volatile("" : "+v"(aoff), "+v"(pboff), "+v"(wooff), "+v"(wdoff), "+v"(mska));
  lds_wait8(lds, F_VI);
  const unsigned tvi = __builtin_amdgcn_readfirstlane(LDSV(unsigned, F_TV));
  u32x2 WD[4], WO[8];
  {
    unsigned tw = tvi + WDEL + (w * WTAU) / 4;
#pragma unroll
    for (int j = 0; j < 4; j++) { pace(tw, WTAU); WD[j] = LD64(rwd, wdoff + j * 1024, wds, NTL); SB(); }
#pragma unroll
    for (int j = 0; j < 8; j++) { pace(tw, WTAU); WO[j] = LD64(rwo, wooff + (j >> 2) * 1024, wos + (j & 1) * 8192 + ((j >> 1) & 1) * 65536, NTL); SB(); }
  }
  lds_wait_eq(lds, F_H, 1u);
  const u32x2 hf = LDSV(u32x2, L_H + bb * 128 + w * 32 + g * 8);
  lds_wait8(lds, F_PV);
  __builtin_amdgcn_s_sleep(4);
  { unsigned tA = tvi + TA_ADD; pace(tA, 0); }
  u32x4 a[4];
#pragma unroll
  for (int i = 0; i < 4; i++) a[i] = LD128(rws, aoff, i * 2048, SC1);
  SB();
  u32x4 fo[4];
  f32x4 acc[4];
#pragma unroll
  for (int e = 0; e < 4; e++) {
#pragma unroll
    for (int j = 0; j < 4; j++) fo[e][j] = kpair(WO[2 * j][e >> 1], WO[2 * j + 1][e >> 1], e & 1);
    acc[e] = mfma16(hf, u32x2{kpair(WD[0][e >> 1], WD[1][e >> 1], e & 1), kpair(WD[2][e >> 1], WD[3][e >> 1], e & 1)}, f32x4{0.f, 0.f, 0.f, 0.f});
  }
  asm volatile("" : "+v"(acc[0]), "+v"(acc[1]), "+v"(acc[2]), "+v"(acc[3]), "+v"(fo[0]), "+v"(fo[1]), "+v"(fo[2]), "+v"(fo[3]));
  const Meet mt = meet_prep(lds, L_TR, F_T, w, lane, lane);
  lds_wait_eq(lds, F_W, 1u);
  f32x4 wt = LDSV(f32x4, L_WT + bb * 32 + sh * 16);
  OPQ(wt);
  {
    unsigned spin = 0;
    NOUNROLL for (;;) {
      unsigned bad = 0;
#pragma unroll
      for (int i = 0; i < 4; i++) { a[i][0] ^= ta; a[i][2] ^= tb; bad |= a[i][0] | a[i][2]; }
      if (none((bad & 0xffu) != 0)) break;
      GIVEUP(spin, CAP_G);
      __builtin_amdgcn_s_sleep(1);
      CBAR();
#pragma unroll
      for (int i = 0; i < 4; i++) a[i] = LD128(rws, aoff, i * 2048, SC1);
    }
  }
  f32x4 t = F4(a[0]) * wt[0];
#pragma unroll
  for (int i = 1; i < 4; i++) t = __builtin_elementwise_fma(F4(a[i]), f32x4{wt[i], wt[i], wt[i], wt[i]}, t);
  const unsigned own = cvtpk(fold32(t[0], t[2]), fold32(t[1], t[3]));
  const u32x4 A = {own, dppu<QB1>(own), dppu<QB2>(own), dppu<QB3>(own)};
#pragma unroll
  for (int e = 0; e < 4; e++) acc[e] = mfma32(A, fo[e], acc[e]);
  SB();
  f32x4 z;
  if (meet4(mt, f32x4{acc[0][0], acc[1][0], acc[2][0], acc[3][0]}, z)) {
    const u32x4 o = {tagm(z[0], mska, ta), __float_as_uint(z[1]), tagm(z[2], mska, tb), __float_as_uint(z[3])};
    ST128(o, rws, pboff, 0, SC1);
  }
}

extern "C" void kernel_launch(void* const* d_in, const int* in_sizes, int n_in, void* d_out, int out_size, void* d_ws, size_t ws_size, hipStream_t stream) {
  const unsigned fl = 0x00020000u;
  fused_decode<<<256, NTHR, 0, stream>>>(d_in[1], 33554432u, fl, d_in[2], 33554432u, fl, d_ws, (unsigned)WS_END, fl, d_in[0], 4096u, fl,
                                         d_in[3], 4194304u, fl, d_in[6], 1835008u, fl, d_in[7], 1835008u, fl, d_in[4], 4194304u, fl,
                                         d_in[8], 1835008u, fl, d_out, 4096u, fl);
}
```

```cpp
#include <hip/hip_runtime.h>
#include <stdint.h>

typedef unsigned short u16;
typedef int i32x4 __attribute__((ext_vector_type(4)));
typedef int i32x16 __attribute__((ext_vector_type(16)));
typedef unsigned u32x2 __attribute__((ext_vector_type(2)));
typedef unsigned u32x4 __attribute__((ext_vector_type(4)));
typedef float f32x2 __attribute__((ext_vector_type(2)));
typedef float f32x4 __attribute__((ext_vector_type(4)));
typedef short s16x4 __attribute__((ext_vector_type(4)));
typedef short s16x8 __attribute__((ext_vector_type(8)));
typedef __bf16 bf16x2 __attribute__((ext_vector_type(2)));
#define LAS __attribute__((address_space(3)))

__device__ u16 LD16(i32x4 r, int v, int s, int a) __asm("llvm.amdgcn.raw.buffer.load.i16");
__device__ unsigned LD32(i32x4 r, int v, int s, int a) __asm("llvm.amdgcn.raw.buffer.load.i32");
__device__ u32x2 LD64(i32x4 r, int v, int s, int a) __asm("llvm.amdgcn.raw.buffer.load.v2i32");
__device__ u32x4 LD128(i32x4 r, int v, int s, int a) __asm("llvm.amdgcn.raw.buffer.load.v4i32");
__device__ void ST16(u16 d, i32x4 r, int v, int s, int a) __asm("llvm.amdgcn.raw.buffer.store.i16");
__device__ void ST32(unsigned d, i32x4 r, int v, int s, int a) __asm("llvm.amdgcn.raw.buffer.store.i32");
__device__ void ST64(u32x2 d, i32x4 r, int v, int s, int a) __asm("llvm.amdgcn.raw.buffer.store.v2i32");
__device__ void ST128(u32x4 d, i32x4 r, int v, int s, int a) __asm("llvm.amdgcn.raw.buffer.store.v4i32");
constexpr int NTL = 2, SC1 = 16;

constexpr int DM = 512, FF = 1792;
constexpr int NT = 4, NTHR = (NT + 8) * 64;

constexpr int WIN = 8;
constexpr int N0 = 2;
constexpr int TOFF = 30, TAU1 = 360, I1 = 8, TAU = 270;
constexpr int W0 = 2, DEL = 40;
constexpr int REBASE = -45;
constexpr int QGPOS = 12, HGPOS = 18;
constexpr int QKPOS = 4, SMPOS = 7, ORGPOS = 8, PVPOS = 14;
constexpr int GOFF = 40, GTAU = 30;
constexpr int WDEL = 8, WTAU = 6;
constexpr int TA_ADD = 125;
constexpr int CODE_BYTES = 12288;
constexpr int sched(int i) { return TOFF + ((i < I1 ? i - N0 + 1 : I1 - N0) * TAU1 + (i < I1 ? 0 : i - I1 + 1) * TAU) / 10; }

constexpr int WS_EPOCH = 0;
constexpr int WS_PF = 1024;
constexpr int WS_Q = 4096;
constexpr int WS_GU = WS_Q + 131072;
constexpr int WS_ST = WS_GU + 114688;
constexpr int WS_PR = 294912;
constexpr int WS_OA = WS_PR + 16384;
constexpr int WS_PB = WS_OA + 64 * 16384;
constexpr int WS_END = WS_PB + 32 * 16384;
static_assert(WS_ST + 32768 <= WS_PR && WS_PR % 16384 == 0, "map");
constexpr int L_RED = 0;
constexpr int L_TR = L_RED + 20480;
constexpr int L_PR = L_TR + 4096;
constexpr int L_Q = L_PR + 4096;
constexpr int L_H = L_Q + 1024;
constexpr int L_WT = L_H + 512;
constexpr int L_MW = L_WT + 128;
constexpr int L_LW = L_MW + 128;
constexpr int L_FLAG = L_LW + 128;
constexpr int F_Q = L_FLAG, F_H = L_FLAG + 8, F_E = L_FLAG + 12, F_VI = L_FLAG + 16, F_PV = L_FLAG + 24, F_T = L_FLAG + 32, F_P = L_FLAG + 36,
              F_TV = L_FLAG + 48, F_W = L_FLAG + 52, F_G = L_FLAG + 56, F_PF = L_FLAG + 60;
constexpr int L_END = L_FLAG + 64;
constexpr unsigned CAP_G = 1u << 18, CAP_L = 1u << 22;
constexpr int OOB = 0x7ffffff0;
constexpr float LOG2E = 1.4426950408889634f;

__device__ __forceinline__ float lo16(unsigned u) { return __uint_as_float(u << 16); }
__device__ __forceinline__ float hi16(unsigned u) { return __uint_as_float(u & 0xffff0000u); }
__device__ __forceinline__ unsigned cvtpk(float lo, float hi) { f32x2 f = {lo, hi}; return __builtin_bit_cast(unsigned, __builtin_convertvector(f, bf16x2)); }
__device__ __forceinline__ float rbf(float f) { return lo16(cvtpk(f, f)); }
__device__ __forceinline__ f32x4 mfma32(u32x4 a, u32x4 b, f32x4 c) { return __builtin_amdgcn_mfma_f32_16x16x32_bf16(__builtin_bit_cast(s16x8, a), __builtin_bit_cast(s16x8, b), c, 0, 0, 0); }
__device__ __forceinline__ f32x4 mfma16(u32x2 a, u32x2 b, f32x4 c) { return __builtin_amdgcn_mfma_f32_16x16x16bf16_1k(__builtin_bit_cast(s16x4, a), __builtin_bit_cast(s16x4, b), c, 0, 0, 0); }
template <int CTRL> __device__ __forceinline__ float dpp(float x) { return __builtin_bit_cast(float, __builtin_amdgcn_mov_dpp(__builtin_bit_cast(int, x), CTRL, 0xf, 0xf, true)); }
template <int CTRL> __device__ __forceinline__ unsigned dppu(unsigned x) { return (unsigned)__builtin_amdgcn_mov_dpp((int)x, CTRL, 0xf, 0xf, true); }
constexpr int ROR8 = 0x128, HMIR = 0x141, QX1 = 0xB1, QX2 = 0x4E, QB0 = 0x00, QB1 = 0x55, QB2 = 0xAA, QB3 = 0xFF;
__device__ __forceinline__ float fold32(float a, float b) {
  auto t = __builtin_amdgcn_permlane32_swap(__float_as_uint(a), __float_as_uint(b), false, false);
  return __uint_as_float(t[0]) + __uint_as_float(t[1]);
}
__device__ __forceinline__ float fold16(float a, float b) {
  auto t = __builtin_amdgcn_permlane16_swap(__float_as_uint(a), __float_as_uint(b), false, false);
  return __uint_as_float(t[0]) + __uint_as_float(t[1]);
}
__device__ __forceinline__ float xrow_sum(float x) { x = fold16(x, x); return fold32(x, x); }
__device__ __forceinline__ float xrow_max(float x) {
  auto s = __builtin_amdgcn_permlane16_swap(__float_as_uint(x), __float_as_uint(x), false, false);
  x = fmaxf(__uint_as_float(s[0]), __uint_as_float(s[1]));
  auto t = __builtin_amdgcn_permlane32_swap(__float_as_uint(x), __float_as_uint(x), false, false);
  return fmaxf(__uint_as_float(t[0]), __uint_as_float(t[1]));
}
__device__ __forceinline__ float dot2(unsigned a, unsigned b, float c) { return __builtin_amdgcn_fdot2_f32_bf16(__builtin_bit_cast(bf16x2, a), __builtin_bit_cast(bf16x2, b), c, false); }
__device__ __forceinline__ unsigned tagm(float v, unsigned m, unsigned t) { return (__float_as_uint(v) & ~m) | (t & m); }
__device__ __forceinline__ unsigned kpair(unsigned r0, unsigned r1, int sel) { return __builtin_amdgcn_perm(r1, r0, sel ? 0x07060302u : 0x05040100u); }
__device__ __forceinline__ bool none(bool p) { return __builtin_amdgcn_ballot_w64(p) == 0ull; }
#define F4(v) __builtin_bit_cast(f32x4, v)
#define SB() __builtin_amdgcn_sched_barrier(0)
#define CBAR() asm volatile("" ::: "memory")
#define OPQ(x) asm volatile("" : "+v"(x))
#define LDSV(T, off) (*(volatile LAS T*)(lds + (off)))
#define NOUNROLL _Pragma("clang loop unroll(disable)")
#define GIVEUP(spin, cap) asm volatile("s_add_u32 %0, %0, 1\n\ts_cmp_lt_u32 %0, %1\n\ts_cbranch_scc1 1f\n\ts_endpgm\n1:" : "+s"(spin) : "n"(cap) : "scc")

__device__ __forceinline__ unsigned now32() { unsigned long long t; asm volatile("s_memrealtime %0\n\ts_waitcnt lgkmcnt(0)" : "=s"(t)::"memory"); return (unsigned)t; }
__device__ __forceinline__ void pace(unsigned& tgt, int step) {
  asm volatile("1:\n\ts_memrealtime vcc\n\ts_waitcnt lgkmcnt(0)\n\ts_sub_u32 vcc_lo, vcc_lo, %0\n\ts_cmp_lt_i32 vcc_lo, 0\n\ts_cbranch_scc1 1b\n\ts_add_u32 %0, %0, %1"
               : "+s"(tgt) : "n"(step) : "memory", "scc", "vcc");
}
__device__ __forceinline__ void apace(unsigned& tgt, int step) {
  unsigned tmp;
  asm volatile("1:\n\ts_memrealtime vcc\n\ts_getreg_b32 %1, hwreg(HW_REG_IB_STS, 0, 4)\n\ts_sub_i32 %1, %1, %3\n\ts_max_i32 %1, %1, 0\n\ts_mul_i32 %1, %1, %4\n\t"
               "s_waitcnt lgkmcnt(0)\n\ts_sub_u32 vcc_lo, vcc_lo, %0\n\ts_cmp_lt_i32 vcc_lo, %1\n\ts_cbranch_scc1 1b\n\ts_add_u32 %0, %0, %2"
               : "+s"(tgt), "=&s"(tmp) : "n"(step), "n"(W0 - 1), "n"(DEL) : "memory", "scc", "vcc");
}
__device__ __forceinline__ void lds_wait_eq(LAS unsigned char* lds, int off, unsigned n) {
  unsigned spin = 0;
  NOUNROLL while (LDSV(unsigned, off) != n) { GIVEUP(spin, CAP_L); __builtin_amdgcn_s_sleep(1); }
  CBAR();
}
__device__ __forceinline__ void lds_wait_byte(LAS unsigned char* lds, int off) {
  unsigned spin = 0;
  NOUNROLL while (LDSV(unsigned char, off) == 0) { GIVEUP(spin, CAP_L); __builtin_amdgcn_s_sleep(2); }
  CBAR();
}
__device__ __forceinline__ void lds_wait8(LAS unsigned char* lds, int off) {
  unsigned spin = 0;
  NOUNROLL for (;;) {
    const u32x2 f = LDSV(u32x2, off);
    if ((f[0] & f[1]) == 0x01010101u) break;
    GIVEUP(spin, CAP_L);
    __builtin_amdgcn_s_sleep(1);
  }
  CBAR();
}
struct Meet { unsigned wr, rd, cnt, one; };
__device__ __forceinline__ Meet meet_prep(LAS unsigned char* lds, int area, int cnt, int w, int lane, int wpos) {
  Meet m = {(unsigned)(uintptr_t)(lds + area) + w * 1024 + wpos * 16, (unsigned)(uintptr_t)(lds + area) + lane * 16, (unsigned)(uintptr_t)(lds + cnt), 1u};
  asm volatile("" : "+v"(m.wr), "+v"(m.rd), "+v"(m.cnt), "+v"(m.one));
  return m;
}
__device__ __forceinline__ bool meet4(const Meet& m, f32x4 v, f32x4& sum) {
  *(volatile LAS f32x4*)(uintptr_t)m.wr = v;
  unsigned prev;
  f32x4 r0, r1, r2, r3;
  asm volatile("s_mov_b64 exec, 1\n\tds_add_rtn_u32 %0, %6, %7\n\ts_mov_b64 exec, -1\n\t"
               "ds_read_b128 %1, %5\n\tds_read_b128 %2, %5 offset:1024\n\tds_read_b128 %3, %5 offset:2048\n\tds_read_b128 %4, %5 offset:3072\n\t"
               "s_waitcnt lgkmcnt(0)"
               : "=&v"(prev), "=&v"(r0), "=&v"(r1), "=&v"(r2), "=&v"(r3) : "v"(m.rd), "v"(m.cnt), "v"(m.one) : "memory");
  sum = (r0 + r1) + (r2 + r3);
  return __builtin_amdgcn_readfirstlane(prev) == 3u;
}
__device__ __forceinline__ unsigned wait_place(LAS unsigned char* lds) {
  unsigned p = LDSV(unsigned, F_PF), spin = 0;
  NOUNROLL while (p == 0u) { GIVEUP(spin, CAP_L); __builtin_amdgcn_s_sleep(1); p = LDSV(unsigned, F_PF); }
  return __builtin_amdgcn_readfirstlane(p);
}
__device__ __forceinline__ int partner(unsigned place, int g) {
  const unsigned m = (((place >> g) & 1u) ? place : ~place) & 0xffu;
  return (g + 1 + __builtin_ctz(((m | (m << 8)) >> (g + 1)) & 0xffu)) & 7;
}

#define DESC(n) const void* p_##n, unsigned n##_bytes, unsigned n##_flags
__global__ void __launch_bounds__(NTHR, 3) fused_decode(DESC(k), DESC(v), DESC(ws), DESC(x), DESC(wq), DESC(wg), DESC(wu), DESC(wo), DESC(wd), DESC(out)) {
  __shared__ __attribute__((aligned(16))) unsigned char smem[L_END];
  auto ka = (const __attribute__((address_space(4))) i32x4*)__builtin_amdgcn_kernarg_segment_ptr();
  i32x16 KA, KB;
  unsigned long long pc0;
  asm volatile("s_load_dwordx16 %0, %3, 0x0\n\ts_load_dwordx16 %1, %3, 0x40\n\ts_getpc_b64 %2" : "=&s"(KA), "=&s"(KB), "=&s"(pc0) : "s"(ka));
  SB();
  LAS unsigned char* lds = (LAS unsigned char*)smem;
  const int blk = blockIdx.x, kvh = blk & 7, gi = blk >> 3, b = gi & 3, s = gi >> 2;
  const int tid = threadIdx.x, lane = tid & 63, w = __builtin_amdgcn_readfirstlane(tid >> 6), g = lane >> 4, c = lane & 15;
  u32x2 pfd = {0u, 0u};
  if (w < NT) {
    if (w == 3 && lane < CODE_BYTES / 256) {
      const int off = gi * (CODE_BYTES / 32) + lane * 8;
      asm volatile("global_load_dwordx2 %0, %1, %2" : "=v"(pfd) : "v"(off), "s"(pc0 & ~0xffull));
    }
    if (w == 2) {
      LDSV(unsigned, L_MW + lane * 4) = 0u;
      LDSV(unsigned, L_FLAG + c * 4) = 0u;
    }
  }
  const int kv = w - NT;
  int koff = (c >> 2) * 256 + g * 64 + (c & 3) * 16;
  int kvbase = ((b * 8 + kvh) * 4096 + s * 512) * 256 + kv * 4096;
  int so0 = kvbase + (gi & 3) * 1024;
  OPQ(koff);
  asm volatile("s_waitcnt lgkmcnt(0)" : "+s"(KA), "+s"(KB), "+s"(kvbase), "+s"(so0)::"memory");
  const i32x4 rws = {KA[8], KA[9], KA[10], KA[11]}, rx = {KA[12], KA[13], KA[14], KA[15]};

  if (__builtin_expect(w >= NT, 1)) {
    const i32x4 rk = {KA[0], KA[1], KA[2], KA[3]}, rv = {KA[4], KA[5], KA[6], KA[7]};
    u32x4 K[16], V[16];
    const int voff = lane * 16;
#define SOFF(i) (((i) < 4 ? so0 : kvbase + ((((i) >> 2) + gi) & 3) * 1024) + ((i) & 3) * 32768)
#define WAITN(n) asm volatile("s_waitcnt vmcnt(%0)" ::"n"((n) - 1) : "memory")
#define PACE(i) apace(tgt, sched((i) + 1) - sched(i))
#define VLOAD(i) do { WAITN(WIN); PACE(16 + (i)); V[i] = LD128(rv, voff, SOFF(i), NTL); \
    if ((i) + 17 == HGPOS && kv == 7) LDSV(unsigned char, F_G + 1) = 1; \
    if ((i) == 15) { LDSV(unsigned, F_TV) = now32(); LDSV(unsigned char, F_VI + kv) = 1; } SB(); } while (0)
    unsigned long long t64;
    asm volatile("s_memrealtime %0" : "=s"(t64));
#pragma unroll
    for (int i = 0; i < N0; i++) { K[i] = LD128(rk, koff, SOFF(i), NTL); SB(); }
    asm volatile("s_barrier\n\ts_waitcnt lgkmcnt(0)" : "+s"(t64)::"memory");
    unsigned tgt = (unsigned)t64 + sched(N0) + (kv * TAU) / 80;
    WAITN(N0);
    { const unsigned t1 = now32() + REBASE + (kv * TAU) / 80; if ((int)(t1 - tgt) > 0) tgt = t1; }
#pragma unroll
    for (int i = N0; i < 16; i++) {
      if (i >= WIN) WAITN(WIN);
      PACE(i);
      K[i] = LD128(rk, koff, SOFF(i), NTL);
      if (i + 1 == QGPOS && kv == 7) LDSV(unsigned char, F_G) = 1;
      SB();
    }
#pragma unroll
    for (int i = 0; i < QKPOS; i++) VLOAD(i);
    u32x2 fl = LDSV(u32x2, F_Q);
    { unsigned spin = 0; NOUNROLL while (fl[0] == 0u) { GIVEUP(spin, CAP_L); __builtin_amdgcn_s_sleep(1); fl = LDSV(u32x2, F_Q); } }
    CBAR();
    const unsigned T = __builtin_amdgcn_readfirstlane(fl[1]);
    float S[4];
    {
      const u32x4 qf = LDSV(u32x4, L_Q + koff);
      const bool a1 = c & 1, a2 = c & 2;
#pragma unroll
      for (int t = 0; t < 4; t++) {
        float y[4];
#pragma unroll
        for (int i = 0; i < 4; i++) {
          const f32x4 d = mfma32(K[t * 4 + i], qf, f32x4{0.f, 0.f, 0.f, 0.f});
          float z = dpp<QB0>(d[0]);
          z += dpp<QB1>(d[1]); OPQ(z);
          z += dpp<QB2>(d[2]); OPQ(z);
          z += dpp<QB3>(d[3]); OPQ(z);
          y[i] = z;
        }
        const float u0 = a1 ? y[1] : y[0], u1 = a1 ? y[3] : y[2];
        S[t] = a2 ? u1 : u0;
      }
#pragma unroll
      for (int i = 0; i < 4; i += 2) {
        const unsigned p = cvtpk(S[i], S[i + 1]);
        const unsigned p2 = cvtpk(lo16(p) * 0.08838834764831845f, hi16(p) * 0.08838834764831845f);
        S[i] = lo16(p2); S[i + 1] = hi16(p2);
      }
      float mw = fmaxf(fmaxf(S[0], S[1]), fmaxf(S[2], S[3]));
      mw = fmaxf(mw, dpp<QX1>(mw)); mw = fmaxf(mw, dpp<QX2>(mw));
      mw = xrow_max(mw);
      LDSV(unsigned, L_MW + (c >> 2) * 32 + kv * 4) = __float_as_uint(mw) | 1u;
    }
    SB();
#pragma unroll
    for (int i = QKPOS; i < SMPOS; i++) VLOAD(i);
    u32x4 P[2];
    {
      u32x4 m0, m1;
      unsigned spin = 0;
      NOUNROLL for (;;) {
        m0 = LDSV(u32x4, L_MW + (c >> 2) * 32); m1 = LDSV(u32x4, L_MW + (c >> 2) * 32 + 16);
        const u32x4 mm = m0 & m1;
        if (none((mm[0] & mm[1] & mm[2] & mm[3] & 1u) == 0u)) break;
        GIVEUP(spin, CAP_L);
        __builtin_amdgcn_s_sleep(1);
      }
      float M = -INFINITY;
#pragma unroll
      for (int i = 0; i < 4; i++) M = fmaxf(M, fmaxf(hi16(m0[i]), hi16(m1[i])));
      float p[4];
#pragma unroll
      for (int i = 0; i < 4; i++) p[i] = __builtin_amdgcn_exp2f((S[i] - M) * LOG2E);
      float lsum = (p[0] + p[1]) + (p[2] + p[3]);
      lsum += dpp<QX1>(lsum); lsum += dpp<QX2>(lsum);
      lsum = xrow_sum(lsum);
      LDSV(unsigned, L_LW + (c >> 2) * 32 + kv * 4) = __float_as_uint(lsum) | 1u;
#pragma unroll
      for (int u = 0; u < 2; u++) { const unsigned pk = cvtpk(p[2 * u], p[2 * u + 1]); P[u] = u32x4{pk, dppu<QB1>(pk), dppu<QB2>(pk), dppu<QB3>(pk)}; }
    }
    unsigned mska = 0xffu;
    asm volatile("" : "+v"(mska), "+v"(P[0]), "+v"(P[1]));
    SB();
#pragma unroll
    for (int i = SMPOS; i < PVPOS; i++) {
      if (i == ORGPOS) tgt += ((int)__builtin_amdgcn_readfirstlane(LDSV(unsigned, F_PF)) >> 16) - 6;
      VLOAD(i);
    }
    f32x4 O[8];
#pragma unroll
    for (int e = 0; e < 8; e++) {
      u32x4 f;
#pragma unroll
      for (int j = 0; j < 4; j++) f[j] = kpair(V[j][e >> 1], V[4 + j][e >> 1], e & 1);
      O[e] = mfma32(P[0], f, f32x4{0.f, 0.f, 0.f, 0.f});
    }
    asm volatile("" : "+v"(O[0]), "+v"(O[1]), "+v"(O[2]), "+v"(O[3]), "+v"(O[4]), "+v"(O[5]), "+v"(O[6]), "+v"(O[7]));
    SB();
    const unsigned place = wait_place(lds);
    const int slot = (kvh * 4 + b) * 8 + s;
    const int side = ((place >> kvh) & 1) * 8192;
    int oaoff = WS_OA + (slot >> 2) * 16384 + side + (slot & 3) * 2048 + kv * 512 + lane * 8;
    OPQ(oaoff);
    const int soff = WS_ST + (partner(place, kvh) * 32 + (lane & 31)) * 128 + (gi >> 3) * 16;
    u32x4 s0 = {0u, ~T, 0u, 0u};
    if (kv == 5 && lane < 32) s0 = LD128(rws, soff, 0, SC1);
    SB();
    int redw = kv * 2560 + lane * 4, redw2 = redw + 1280, redr = (lane >> 5) * 10240 + ((lane & 31) >> 2) * 320 + kv * 64 + (lane & 3) * 16;
    unsigned tab = T >> ((lane & 3) * 8);
    asm volatile("" : "+v"(redw), "+v"(redw2), "+v"(redr), "+v"(tab));
#pragma unroll
    for (int i = PVPOS; i < 16; i++) VLOAD(i);
    {
      u32x4 F[8];
#pragma unroll
      for (int j = 0; j < 3; j++) {
#pragma unroll
        for (int e = 0; e < 8; e++) F[e][j] = kpair(V[8 + j][e >> 1], V[12 + j][e >> 1], e & 1);
        SB();
      }
#pragma unroll
      for (int i = 0; i < 4; i++) {
        F[2 * i][3] = kpair(V[11][i], V[15][i], 0); F[2 * i + 1][3] = kpair(V[11][i], V[15][i], 1);
        O[2 * i] = mfma32(P[1], F[2 * i], O[2 * i]); O[2 * i + 1] = mfma32(P[1], F[2 * i + 1], O[2 * i + 1]);
        SB();
      }
    }
#pragma unroll
    for (int e = 0; e < 4; e++) *(LAS float*)(lds + L_RED + redw + e * 320) = O[e][0];
#pragma unroll
    for (int e = 0; e < 4; e++) *(LAS float*)(lds + L_RED + redw2 + e * 320) = O[4 + e][0];
    CBAR();
    LDSV(unsigned char, F_PV + kv) = 1;
    if (kv < 4) {
      f32x4 r[4];
      unsigned spin = 0;
      NOUNROLL for (;;) {
        const u32x2 cnt = LDSV(u32x2, F_PV);
#pragma unroll
        for (int i = 0; i < 4; i++) r[i] = LDSV(f32x4, L_RED + redr + i * 2560);
        if ((cnt[0] & cnt[1]) == 0x01010101u) break;
        GIVEUP(spin, CAP_L);
      }
      const f32x4 a = (r[0] + r[1]) + (r[2] + r[3]);
      ST64(u32x2{tagm(fold32(a[0], a[2]), mska, tab), __float_as_uint(fold32(a[1], a[3]))}, rws, oaoff, 0, SC1);
    }
    if (kv == 5) {
      if (lane < 32) {
        unsigned spin = 0;
        NOUNROLL while (!none(((s0[1] ^ T) | (s0[3] ^ T)) != 0u)) {
          GIVEUP(spin, CAP_G);
          __builtin_amdgcn_s_sleep(2);
          CBAR();
          s0 = LD128(rws, soff, 0, SC1);
        }
        const float m1 = __uint_as_float(s0[0]);
        float MM = fmaxf(m1, dpp<QX1>(m1));
        MM = fmaxf(MM, dpp<QX2>(MM)); MM = fmaxf(MM, dpp<HMIR>(MM));
        const float e1 = __builtin_amdgcn_exp2f((m1 - MM) * LOG2E);
        float L = e1 * __uint_as_float(s0[2]);
        L += dpp<QX1>(L); L += dpp<QX2>(L); L += dpp<HMIR>(L);
        LDSV(float, L_WT + lane * 4) = e1 * __builtin_amdgcn_rcpf(L);
      }
      LDSV(unsigned, F_W) = 1u;
    }
    if (kv >= 6 && kvh == (int)((place >> 9) & 7u)) {
      const int lh = kv - 6, pg = lane >> 3;
      const int oi = (gi >> 3) * 512 + (gi & 7) * 64 + lh * 32 + (c & 7) * 4 + g;
      const i32x4 ro = ka[9];
      float xfin = lo16(LD16(rx, oi * 2, 0, 0));
      int go[4];
#pragma unroll
      for (int i = 0; i < 4; i++) go[i] = WS_PB + ((gi & 7) * 4 + i) * 16384 + side + pg * 1024 + (((gi >> 3) * 2 + lh + 2 * i + (pg >> 2)) & 7) * 128 + (lane & 7) * 16;
      int ooff = c < 8 ? oi * 2 : OOB;
      unsigned ta = (T >> ((lane & 1) * 16)) & 0xffu, tb = (T >> ((lane & 1) * 16 + 8)) & 0xffu;
      asm volatile("" : "+v"(xfin), "+v"(go[0]), "+v"(go[1]), "+v"(go[2]), "+v"(go[3]), "+v"(ooff), "+v"(ta), "+v"(tb));
      lds_wait_eq(lds, F_T, 4u);
      __builtin_amdgcn_s_sleep(18);
      u32x4 pb[4];
      unsigned spin = 0;
      NOUNROLL for (;;) {
        CBAR();
#pragma unroll
        for (int i = 0; i < 4; i++) pb[i] = LD128(rws, go[i], 0, SC1);
        unsigned bad = 0;
#pragma unroll
        for (int i = 0; i < 4; i++) { pb[i][0] ^= ta; pb[i][2] ^= tb; bad |= pb[i][0] | pb[i][2]; }
        if (none((bad & 0xffu) != 0)) break;
        GIVEUP(spin, CAP_G);
        __builtin_amdgcn_s_sleep(1);
      }
      const f32x4 v = (F4(pb[0]) + F4(pb[1])) + (F4(pb[2]) + F4(pb[3]));
      float z = fold16(fold32(v[0], v[2]), fold32(v[1], v[3]));
      z += dpp<ROR8>(z);
      z += xfin;
      ST16((u16)cvtpk(z, z), ro, ooff, 0, SC1);
      if (gi == 0 && lh == 0 && lane == 0) ST32(T, rws, WS_EPOCH, 0, SC1);
    }
    return;
  }

  __builtin_amdgcn_s_setprio(3);
  const unsigned ep = LD32(rws, WS_EPOCH, 0, SC1);
  asm volatile("s_barrier" ::: "memory");
  const int hl = gi >> 3, ng = gi & 7;
  const int task = gi >> 1, kh = gi & 1;
  const unsigned tstart = (unsigned)__builtin_amdgcn_s_memrealtime();
  if (task < 15) {
    const bool isq = task < 8;
    u32x4 xs[4], xf, W[8];
#pragma unroll
    for (int i = 0; i < 4; i++) xs[i] = LD128(rx, (c >> 2) * 1024 + g * 64 + (c & 3) * 16 + i * 256, 0, 0);
    xf = LD128(rx, (c >> 2) * 1024 + (c & 1) * 64 + g * 16, kh * 512 + w * 128, 0);
    const int kvq = (kvh + 1) & 7, idx = kvh * 7 + task - 8, mat = idx >= 28 ? 1 : 0;
    const int cb = isq ? kvq * 8 + task : idx - 28 * mat;
    const int rstride = isq ? 8192 : FF * 2;
    const i32x4 rw = {isq ? KB[0] : mat ? KB[8] : KB[4], isq ? KB[1] : mat ? KB[9] : KB[5], isq ? KB[2] : KB[6], KB[3]};
    const int woff = ((c >> 3) * 32 + g * 8) * rstride + (c & 7) * 16;
    int wso = (kh * 256 + w * 64) * rstride + cb * 128;
    unsigned tg = tstart + GOFF + (w * GTAU) / 4;
#pragma unroll
    for (int j = 0; j < 8; j++) {
      if (!isq) pace(tg, GTAU);
      W[j] = LD128(rw, woff, wso, NTL);
      wso += rstride;
      SB();
    }
    float sm = 0.f;
#pragma unroll
    for (int i = 0; i < 4; i++)
#pragma unroll
      for (int j = 0; j < 4; j++) sm = dot2(xs[i][j], 0x3f803f80u, sm);
    sm += dpp<QX1>(sm); sm += dpp<QX2>(sm);
    const float mean = xrow_sum(sm) * (1.f / DM);
    f32x2 ssv = {0.f, 0.f};
#pragma unroll
    for (int i = 0; i < 4; i++)
#pragma unroll
      for (int j = 0; j < 4; j++) { const f32x2 d = f32x2{lo16(xs[i][j]), hi16(xs[i][j])} - mean; ssv += d * d; }
    float ss = ssv[0] + ssv[1];
    ss += dpp<QX1>(ss); ss += dpp<QX2>(ss);
    const float var = rbf(xrow_sum(ss) * (1.f / DM)), mb = rbf(mean);
    const float den = rbf(__builtin_amdgcn_sqrtf(rbf(var + 1e-5f)));
    const float rden = __builtin_amdgcn_rcpf(den);
#pragma unroll
    for (int j = 0; j < 4; j++) {
      const unsigned d = cvtpk(lo16(xf[j]) - mb, hi16(xf[j]) - mb);
      const f32x2 n = {lo16(d), hi16(d)};
      const f32x2 q = n * rden;
      const f32x2 r = (n - q * den) * rden + q;
      xf[j] = cvtpk(r[0], r[1]);
    }
    SB();
    f32x4 acc[8];
#pragma unroll
    for (int e = 0; e < 8; e++) {
      u32x4 f;
#pragma unroll
      for (int j = 0; j < 4; j++) f[j] = kpair(W[2 * j][e >> 1], W[2 * j + 1][e >> 1], e & 1);
      acc[e] = mfma32(xf, f, f32x4{0.f, 0.f, 0.f, 0.f});
    }
    SB();
    f32x4 o, z;
#pragma unroll
    for (int i = 0; i < 4; i++) {
      const float a = acc[i][0] + dpp<ROR8>(acc[i][1]), bq = acc[4 + i][1] + dpp<ROR8>(acc[4 + i][0]);
      o[i] = c < 8 ? a : bq;
    }
    if (meet4(meet_prep(lds, L_PR, F_P, w, lane, g * 16 + (c & 7) * 2 + (c >> 3)), o, z)) {
      const unsigned t0 = (__builtin_amdgcn_readfirstlane(ep) + 0x01010101u) >> ((c & 1) * 16);
      const u32x4 pv = {tagm(z[0], 0xfu, t0), tagm(z[1], 0xfu, t0 >> 4), tagm(z[2], 0xfu, t0 >> 8), tagm(z[3], 0xfu, t0 >> 12)};
      const int poff = isq ? WS_Q + (((kvq * 4 + g) * 2 + kh) * 512 + task * 64 + c * 4) * 4 : WS_GU + (((mat * 2 + kh) * 4 + g) * FF + cb * 64 + c * 4) * 4;
      ST128(pv, rws, poff, 0, SC1);
    }
  }
  const unsigned T = __builtin_amdgcn_readfirstlane(ep) + 0x01010101u;
  asm volatile("" : "+v"(pfd) : "s"(T) : "memory");
  LDSV(unsigned char, F_E + w) = 1;
  const int bb = c >> 2, q4 = c & 3, sh = g >> 1;
  if (w >= 2) {
    int hh3 = 0;
    if (w == 3) hh3 = partner(wait_place(lds), kvh) * 4 + hl;
    const int goff = w == 2 ? WS_Q + (kvh * 4 + b) * 4096 + lane * 16 : WS_GU + (g * FF + hh3 * 56 + (c < 14 ? c : 13) * 4) * 4;
    const int st1 = w == 2 ? 2048 : 4 * FF * 4, st2 = w == 2 ? 1024 : 8 * FF * 4;
    const unsigned tn = T >> (goff & 16);
    const u32x4 TN = {tn & 0xfu, (tn >> 4) & 0xfu, (tn >> 8) & 0xfu, (tn >> 12) & 0xfu};
    u32x4 q[4];
    lds_wait_byte(lds, F_G);
    unsigned spin = 0;
    NOUNROLL for (;;) {
      CBAR();
      q[0] = LD128(rws, goff, 0, SC1); q[1] = LD128(rws, goff, st1, SC1); q[2] = LD128(rws, goff, st2, SC1); q[3] = LD128(rws, goff, st1 + st2, SC1);
      unsigned bad = 0;
#pragma unroll
      for (int i = 0; i < 4; i++) { q[i] ^= TN; bad |= (q[i][0] | q[i][1]) | (q[i][2] | q[i][3]); }
      if (none((bad & 0xfu) != 0)) break;
      GIVEUP(spin, CAP_G);
      __builtin_amdgcn_s_sleep(8);
    }
    const f32x4 y0 = F4(q[0]) + F4(q[1]), y1 = F4(q[2]) + F4(q[3]);
    const u32x2 z0 = {cvtpk(y0[0], y0[1]), cvtpk(y0[2], y0[3])}, z1 = {cvtpk(y1[0], y1[1]), cvtpk(y1[2], y1[3])};
    if (w == 2) {
      LDSV(u32x2, L_Q + lane * 8) = z0;
      LDSV(u32x2, L_Q + 512 + lane * 8) = z1;
      lds_wait_eq(lds, F_E, 0x01010101u);
      LDSV(u32x2, F_Q) = u32x2{1u, T};
    } else {
      u32x2 hp;
#pragma unroll
      for (int i = 0; i < 2; i++) hp[i] = c < 14 ? cvtpk(lo16(z0[i]) * lo16(z1[i]), hi16(z0[i]) * hi16(z1[i])) : 0u;
      LDSV(u32x2, L_H + g * 128 + c * 8) = hp;
      LDSV(unsigned, F_H) = 1u;
    }
  } else if (w == 0) {
    unsigned lv, spin = 0;
    NOUNROLL for (;;) {
      lv = LDSV(unsigned, L_LW + (lane & 31) * 4);
      if (none(lv == 0u)) break;
      GIVEUP(spin, CAP_L);
      __builtin_amdgcn_s_sleep(2);
    }
    float mv = hi16(LDSV(unsigned, L_MW + (lane & 31) * 4));
    float lsum = __uint_as_float(lv);
    lsum += dpp<QX1>(lsum); lsum += dpp<QX2>(lsum); lsum += dpp<HMIR>(lsum);
    mv = fmaxf(mv, dpp<QX1>(mv)); mv = fmaxf(mv, dpp<QX2>(mv)); mv = fmaxf(mv, dpp<HMIR>(mv));
    const u32x4 st = {__float_as_uint(mv), T, __float_as_uint(lsum), T};
    if ((lane & 7) == 0) ST128(st, rws, WS_ST + ((kvh * 4 + b) * 8 + s) * 128 + (lane >> 3) * 16, 0, SC1);
  } else {
    if (gi == 31) {
      ST128(u32x4{T, T, T, T}, rws, WS_PR + (lane >> 5) * 8192 + kvh * 1024 + (lane & 31) * 16, 0, SC1);
      asm volatile("s_waitcnt vmcnt(0)" ::: "memory");
      unsigned best0 = ~0u, best1 = ~0u;
      NOUNROLL for (int i = 0; i < 8; i++) {
        unsigned long long c0, c1; unsigned d;
        asm volatile("s_memtime %0\n\ts_waitcnt lgkmcnt(0)\n\ts_buffer_load_dword %2, %3, %4\n\ts_waitcnt lgkmcnt(0)\n\ts_memtime %1\n\ts_waitcnt lgkmcnt(0)"
                     : "=&s"(c0), "=&s"(c1), "=&s"(d) : "s"(rws), "s"(WS_PR + (i & 1) * 8192 + kvh * 1024 + (i >> 1) * 128) : "memory");
        const unsigned dt = (unsigned)c1 - (unsigned)c0;
        if (i >= 2) { if (i & 1) best1 = min(best1, dt); else best0 = min(best0, dt); }
      }
      const unsigned xcc = __builtin_amdgcn_s_getreg(20 | (3 << 11)) & 7u;
      if (lane == 0) ST64(u32x2{T, (tstart << 4) | (xcc << 1) | (best1 < best0 ? 1u : 0u)}, rws, WS_PF + kvh * 8, 0, SC1);
    }
    lds_wait_byte(lds, F_G + 1);
    if (lane < 8) {
      u32x2 pf;
      unsigned spin = 0;
      NOUNROLL for (;;) {
        CBAR();
        pf = LD64(rws, WS_PF + lane * 8, 0, SC1);
        if (none(pf[0] != T)) break;
        GIVEUP(spin, CAP_G);
        __builtin_amdgcn_s_sleep(8);
      }
      unsigned kx = ((pf[1] & 14u) << 2) | lane;
      kx = min(kx, dppu<QX1>(kx)); kx = min(kx, dppu<QX2>(kx)); kx = min(kx, dppu<HMIR>(kx));
      unsigned dl = (unsigned)min(max(((int)(pf[1] - (tstart << 4))) >> 4, -100), 100);
      dl += dppu<QX1>(dl); dl += dppu<QX2>(dl); dl += dppu<HMIR>(dl);
      LDSV(unsigned, F_PF) = (unsigned)__builtin_amdgcn_ballot_w64(pf[1] & 1u) | 0x100u | ((kx & 7u) << 9) | ((unsigned)((int)dl >> 3) << 16);
    }
  }
  const unsigned place = wait_place(lds);
  const int kva = partner(place, kvh), hh = kva * 4 + hl;
  const i32x4 rwo = {KB[12], KB[13], KB[14], KB[15]}, rwd = ka[8];
  const int wos = (hh * 128 + 16 * (w >> 1) + 4 * (w & 1)) * 1024 + ng * 128, wds = (hh * 56 + w * 16) * 1024 + ng * 128;
  int aoff = WS_OA + ((kva * 4 + bb) * 2 + sh) * 16384 + ((place >> kvh) & 1) * 8192 + hl * 512 + w * 128 + (g & 1) * 64 + q4 * 16;
  int pboff = WS_PB + (ng * 4 + (hh >> 3)) * 16384 + ((place >> ((place >> 9) & 7)) & 1) * 8192 + (hh & 7) * 1024 + ((lane * 16 + (hh >> 2) * 128) & 1023);
  int wooff = (32 * (g >> 1) + 2 * (g & 1)) * 1024 + c * 8;
  int wdoff = (w == 3 && g >= 2) ? OOB - 4096 : g * 4096 + c * 8;
  unsigned ta = (T >> ((lane & 1) * 16)) & 0xffu, tb = (T >> ((lane & 1) * 16 + 8)) & 0xffu, mska = 0xffu;
  asm volatile("" : "+v"(ta), "+v"(tb));
  asm volatile("" : "+v"(aoff), "+v"(pboff), "+v"(wooff), "+v"(wdoff), "+v"(mska));
  lds_wait8(lds, F_VI);
  const unsigned tvi = __builtin_amdgcn_readfirstlane(LDSV(unsigned, F_TV));
  u32x2 WD[4], WO[8];
  {
    unsigned tw = tvi + WDEL + (w * WTAU) / 4;
#pragma unroll
    for (int j = 0; j < 4; j++) { pace(tw, WTAU); WD[j] = LD64(rwd, wdoff + j * 1024, wds, NTL); SB(); }
#pragma unroll
    for (int j = 0; j < 8; j++) { pace(tw, WTAU); WO[j] = LD64(rwo, wooff + (j >> 2) * 1024, wos + (j & 1) * 8192 + ((j >> 1) & 1) * 65536, NTL); SB(); }
  }
  lds_wait_eq(lds, F_H, 1u);
  const u32x2 hf = LDSV(u32x2, L_H + bb * 128 + w * 32 + g * 8);
  lds_wait8(lds, F_PV);
  __builtin_amdgcn_s_sleep(4);
  { unsigned tA = tvi + TA_ADD; pace(tA, 0); }
  u32x4 a[4];
#pragma unroll
  for (int i = 0; i < 4; i++) a[i] = LD128(rws, aoff, i * 2048, SC1);
  SB();
  u32x4 fo[4];
  f32x4 acc[4];
#pragma unroll
  for (int e = 0; e < 4; e++) {
#pragma unroll
    for (int j = 0; j < 4; j++) fo[e][j] = kpair(WO[2 * j][e >> 1], WO[2 * j + 1][e >> 1], e & 1);
    acc[e] = mfma16(hf, u32x2{kpair(WD[0][e >> 1], WD[1][e >> 1], e & 1), kpair(WD[2][e >> 1], WD[3][e >> 1], e & 1)}, f32x4{0.f, 0.f, 0.f, 0.f});
  }
  asm volatile("" : "+v"(acc[0]), "+v"(acc[1]), "+v"(acc[2]), "+v"(acc[3]), "+v"(fo[0]), "+v"(fo[1]), "+v"(fo[2]), "+v"(fo[3]));
  const Meet mt = meet_prep(lds, L_TR, F_T, w, lane, lane);
  lds_wait_eq(lds, F_W, 1u);
  f32x4 wt = LDSV(f32x4, L_WT + bb * 32 + sh * 16);
  OPQ(wt);
  {
    unsigned spin = 0;
    NOUNROLL for (;;) {
      unsigned bad = 0;
#pragma unroll
      for (int i = 0; i < 4; i++) { a[i][0] ^= ta; a[i][2] ^= tb; bad |= a[i][0] | a[i][2]; }
      if (none((bad & 0xffu) != 0)) break;
      GIVEUP(spin, CAP_G);
      __builtin_amdgcn_s_sleep(1);
      CBAR();
#pragma unroll
      for (int i = 0; i < 4; i++) a[i] = LD128(rws, aoff, i * 2048, SC1);
    }
  }
  f32x4 t = F4(a[0]) * wt[0];
#pragma unroll
  for (int i = 1; i < 4; i++) t = __builtin_elementwise_fma(F4(a[i]), f32x4{wt[i], wt[i], wt[i], wt[i]}, t);
  const unsigned own = cvtpk(fold32(t[0], t[2]), fold32(t[1], t[3]));
  const u32x4 A = {own, dppu<QB1>(own), dppu<QB2>(own), dppu<QB3>(own)};
#pragma unroll
  for (int e = 0; e < 4; e++) acc[e] = mfma32(A, fo[e], acc[e]);
  SB();
  f32x4 z;
  if (meet4(mt, f32x4{acc[0][0], acc[1][0], acc[2][0], acc[3][0]}, z)) {
    const u32x4 o = {tagm(z[0], mska, ta), __float_as_uint(z[1]), tagm(z[2], mska, tb), __float_as_uint(z[3])};
    ST128(o, rws, pboff, 0, SC1);
  }
}

extern "C" void kernel_launch(void* const* d_in, const int* in_sizes, int n_in, void* d_out, int out_size, void* d_ws, size_t ws_size, hipStream_t stream) {
  const unsigned fl = 0x00020000u;
  fused_decode<<<256, NTHR, 0, stream>>>(d_in[1], 33554432u, fl, d_in[2], 33554432u, fl, d_ws, (unsigned)WS_END, fl, d_in[0], 4096u, fl,
                                         d_in[3], 4194304u, fl, d_in[6], 1835008u, fl, d_in[7], 1835008u, fl, d_in[4], 4194304u, fl,
                                         d_in[8], 1835008u, fl, d_out, 4096u, fl);
}
```

```cpp
#include <hip/hip_runtime.h>
#include <stdint.h>

typedef unsigned short u16;
typedef int i32x4 __attribute__((ext_vector_type(4)));
typedef int i32x16 __attribute__((ext_vector_type(16)));
typedef unsigned u32x2 __attribute__((ext_vector_type(2)));
typedef unsigned u32x4 __attribute__((ext_vector_type(4)));
typedef float f32x2 __attribute__((ext_vector_type(2)));
typedef float f32x4 __attribute__((ext_vector_type(4)));
typedef short s16x4 __attribute__((ext_vector_type(4)));
typedef short s16x8 __attribute__((ext_vector_type(8)));
typedef __bf16 bf16x2 __attribute__((ext_vector_type(2)));
#define LAS __attribute__((address_space(3)))

__device__ u16 LD16(i32x4 r, int v, int s, int a) __asm("llvm.amdgcn.raw.buffer.load.i16");
__device__ unsigned LD32(i32x4 r, int v, int s, int a) __asm("llvm.amdgcn.raw.buffer.load.i32");
__device__ u32x2 LD64(i32x4 r, int v, int s, int a) __asm("llvm.amdgcn.raw.buffer.load.v2i32");
__device__ u32x4 LD128(i32x4 r, int v, int s, int a) __asm("llvm.amdgcn.raw.buffer.load.v4i32");
__device__ void ST16(u16 d, i32x4 r, int v, int s, int a) __asm("llvm.amdgcn.raw.buffer.store.i16");
__device__ void ST32(unsigned d, i32x4 r, int v, int s, int a) __asm("llvm.amdgcn.raw.buffer.store.i32");
__device__ void ST64(u32x2 d, i32x4 r, int v, int s, int a) __asm("llvm.amdgcn.raw.buffer.store.v2i32");
__device__ void ST128(u32x4 d, i32x4 r, int v, int s, int a) __asm("llvm.amdgcn.raw.buffer.store.v4i32");
constexpr int NTL = 2, SC1 = 16;

constexpr int DM = 512, FF = 1792;
constexpr int NT = 4, NTHR = (NT + 8) * 64;

constexpr int WIN = 8;
constexpr int N0 = 2;
constexpr int TOFF = 30, TAU1 = 360, I1 = 8, TAU = 270;
constexpr int W0 = 2, DEL = 40;
constexpr int REBASE = -45;
constexpr int QGPOS = 12, HGPOS = 18;
constexpr int QKPOS = 4, SMPOS = 7, ORGPOS = 8, PVPOS = 14;
constexpr int GOFF = 40, GTAU = 30;
constexpr int WDEL = 8, WTAU = 6;
constexpr int TA_ADD = 125;
constexpr int CODE_BYTES = 13568;
constexpr int sched(int i) { return TOFF + ((i < I1 ? i - N0 + 1 : I1 - N0) * TAU1 + (i < I1 ? 0 : i - I1 + 1) * TAU) / 10; }

constexpr int WS_EPOCH = 0;
constexpr int WS_PF = 1024;
constexpr int WS_Q = 4096;
constexpr int WS_GU = WS_Q + 131072;
constexpr int WS_ST = WS_GU + 114688;
constexpr int WS_PR = 294912;
constexpr int WS_OA = WS_PR + 16384;
constexpr int WS_PB = WS_OA + 64 * 16384;
constexpr int WS_END = WS_PB + 32 * 16384;
static_assert(WS_ST + 32768 <= WS_PR && WS_PR % 16384 == 0, "map");
constexpr int L_RED = 0;
constexpr int L_TR = L_RED + 20480;
constexpr int L_PR = L_TR + 4096;
constexpr int L_Q = L_PR + 4096;
constexpr int L_H = L_Q + 1024;
constexpr int L_WT = L_H + 512;
constexpr int L_MW = L_WT + 128;
constexpr int L_LW = L_MW + 128;
constexpr int L_FLAG = L_LW + 128;
constexpr int F_Q = L_FLAG, F_H = L_FLAG + 8, F_E = L_FLAG + 12, F_VI = L_FLAG + 16, F_PV = L_FLAG + 24, F_T = L_FLAG + 32, F_P = L_FLAG + 36,
              F_TV = L_FLAG + 48, F_W = L_FLAG + 52, F_G = L_FLAG + 56, F_PF = L_FLAG + 60;
constexpr int L_END = L_FLAG + 64;
constexpr unsigned CAP_G = 1u << 18, CAP_L = 1u << 22;
constexpr int OOB = 0x7ffffff0;
constexpr float LOG2E = 1.4426950408889634f;

__device__ __forceinline__ float lo16(unsigned u) { return __uint_as_float(u << 16); }
__device__ __forceinline__ float hi16(unsigned u) { return __uint_as_float(u & 0xffff0000u); }
__device__ __forceinline__ unsigned cvtpk(float lo, float hi) { f32x2 f = {lo, hi}; return __builtin_bit_cast(unsigned, __builtin_convertvector(f, bf16x2)); }
__device__ __forceinline__ float rbf(float f) { return lo16(cvtpk(f, f)); }
__device__ __forceinline__ f32x4 mfma32(u32x4 a, u32x4 b, f32x4 c) { return __builtin_amdgcn_mfma_f32_16x16x32_bf16(__builtin_bit_cast(s16x8, a), __builtin_bit_cast(s16x8, b), c, 0, 0, 0); }
__device__ __forceinline__ f32x4 mfma16(u32x2 a, u32x2 b, f32x4 c) { return __builtin_amdgcn_mfma_f32_16x16x16bf16_1k(__builtin_bit_cast(s16x4, a), __builtin_bit_cast(s16x4, b), c, 0, 0, 0); }
template <int CTRL> __device__ __forceinline__ float dpp(float x) { return __builtin_bit_cast(float, __builtin_amdgcn_mov_dpp(__builtin_bit_cast(int, x), CTRL, 0xf, 0xf, true)); }
template <int CTRL> __device__ __forceinline__ unsigned dppu(unsigned x) { return (unsigned)__builtin_amdgcn_mov_dpp((int)x, CTRL, 0xf, 0xf, true); }
constexpr int ROR8 = 0x128, HMIR = 0x141, QX1 = 0xB1, QX2 = 0x4E, QB0 = 0x00, QB1 = 0x55, QB2 = 0xAA, QB3 = 0xFF;
__device__ __forceinline__ float fold32(float a, float b) {
  auto t = __builtin_amdgcn_permlane32_swap(__float_as_uint(a), __float_as_uint(b), false, false);
  return __uint_as_float(t[0]) + __uint_as_float(t[1]);
}
__device__ __forceinline__ float fold16(float a, float b) {
  auto t = __builtin_amdgcn_permlane16_swap(__float_as_uint(a), __float_as_uint(b), false, false);
  return __uint_as_float(t[0]) + __uint_as_float(t[1]);
}
__device__ __forceinline__ float xrow_sum(float x) { x = fold16(x, x); return fold32(x, x); }
__device__ __forceinline__ float xrow_max(float x) {
  auto s = __builtin_amdgcn_permlane16_swap(__float_as_uint(x), __float_as_uint(x), false, false);
  x = fmaxf(__uint_as_float(s[0]), __uint_as_float(s[1]));
  auto t = __builtin_amdgcn_permlane32_swap(__float_as_uint(x), __float_as_uint(x), false, false);
  return fmaxf(__uint_as_float(t[0]), __uint_as_float(t[1]));
}
__device__ __forceinline__ float dot2(unsigned a, unsigned b, float c) { return __builtin_amdgcn_fdot2_f32_bf16(__builtin_bit_cast(bf16x2, a), __builtin_bit_cast(bf16x2, b), c, false); }
__device__ __forceinline__ unsigned tagm(float v, unsigned m, unsigned t) { return (__float_as_uint(v) & ~m) | (t & m); }
__device__ __forceinline__ unsigned kpair(unsigned r0, unsigned r1, int sel) { return __builtin_amdgcn_perm(r1, r0, sel ? 0x07060302u : 0x05040100u); }
__device__ __forceinline__ bool none(bool p) { return __builtin_amdgcn_ballot_w64(p) == 0ull; }
#define F4(v) __builtin_bit_cast(f32x4, v)
#define SB() __builtin_amdgcn_sched_barrier(0)
#define CBAR() asm volatile("" ::: "memory")
#define OPQ(x) asm volatile("" : "+v"(x))
#define LDSV(T, off) (*(volatile LAS T*)(lds + (off)))
#define NOUNROLL _Pragma("clang loop unroll(disable)")
#define GIVEUP(spin, cap) asm volatile("s_add_u32 %0, %0, 1\n\ts_cmp_lt_u32 %0, %1\n\ts_cbranch_scc1 1f\n\ts_endpgm\n1:" : "+s"(spin) : "n"(cap) : "scc")

__device__ __forceinline__ unsigned now32() { unsigned long long t; asm volatile("s_memrealtime %0\n\ts_waitcnt lgkmcnt(0)" : "=s"(t)::"memory"); return (unsigned)t; }
__device__ __forceinline__ void pace(unsigned& tgt, int step) {
  asm volatile("1:\n\ts_memrealtime vcc\n\ts_waitcnt lgkmcnt(0)\n\ts_sub_u32 vcc_lo, vcc_lo, %0\n\ts_cmp_lt_i32 vcc_lo, 0\n\ts_cbranch_scc1 1b\n\ts_add_u32 %0, %0, %1"
               : "+s"(tgt) : "n"(step) : "memory", "scc", "vcc");
}
__device__ __forceinline__ void apace(unsigned& tgt, int step) {
  unsigned tmp;
  asm volatile("1:\n\ts_memrealtime vcc\n\ts_getreg_b32 %1, hwreg(HW_REG_IB_STS, 0, 4)\n\ts_sub_i32 %1, %1, %3\n\ts_max_i32 %1, %1, 0\n\ts_mul_i32 %1, %1, %4\n\t"
               "s_waitcnt lgkmcnt(0)\n\ts_sub_u32 vcc_lo, vcc_lo, %0\n\ts_cmp_lt_i32 vcc_lo, %1\n\ts_cbranch_scc1 1b\n\ts_add_u32 %0, %0, %2"
               : "+s"(tgt), "=&s"(tmp) : "n"(step), "n"(W0 - 1), "n"(DEL) : "memory", "scc", "vcc");
}
__device__ __forceinline__ void lds_wait_eq(LAS unsigned char* lds, int off, unsigned n) {
  unsigned spin = 0;
  NOUNROLL while (LDSV(unsigned, off) != n) { GIVEUP(spin, CAP_L); __builtin_amdgcn_s_sleep(1); }
  CBAR();
}
__device__ __forceinline__ void lds_wait_byte(LAS unsigned char* lds, int off) {
  unsigned spin = 0;
  NOUNROLL while (LDSV(unsigned char, off) == 0) { GIVEUP(spin, CAP_L); __builtin_amdgcn_s_sleep(2); }
  CBAR();
}
__device__ __forceinline__ void lds_wait8(LAS unsigned char* lds, int off) {
  unsigned spin = 0;
  NOUNROLL for (;;) {
    const u32x2 f = LDSV(u32x2, off);
    if ((f[0] & f[1]) == 0x01010101u) break;
    GIVEUP(spin, CAP_L);
    __builtin_amdgcn_s_sleep(1);
  }
  CBAR();
}
struct Meet { unsigned wr, rd, cnt, one; };
__device__ __forceinline__ Meet meet_prep(LAS unsigned char* lds, int area, int cnt, int w, int lane, int wpos) {
  Meet m = {(unsigned)(uintptr_t)(lds + area) + w * 1024 + wpos * 16, (unsigned)(uintptr_t)(lds + area) + lane * 16, (unsigned)(uintptr_t)(lds + cnt), 1u};
  asm volatile("" : "+v"(m.wr), "+v"(m.rd), "+v"(m.cnt), "+v"(m.one));
  return m;
}
__device__ __forceinline__ bool meet4(const Meet& m, f32x4 v, f32x4& sum) {
  *(volatile LAS f32x4*)(uintptr_t)m.wr = v;
  unsigned prev;
  f32x4 r0, r1, r2, r3;
  asm volatile("s_mov_b64 exec, 1\n\tds_add_rtn_u32 %0, %6, %7\n\ts_mov_b64 exec, -1\n\t"
               "ds_read_b128 %1, %5\n\tds_read_b128 %2, %5 offset:1024\n\tds_read_b128 %3, %5 offset:2048\n\tds_read_b128 %4, %5 offset:3072\n\t"
               "s_waitcnt lgkmcnt(0)"
               : "=&v"(prev), "=&v"(r0), "=&v"(r1), "=&v"(r2), "=&v"(r3) : "v"(m.rd), "v"(m.cnt), "v"(m.one) : "memory");
  sum = (r0 + r1) + (r2 + r3);
  return __builtin_amdgcn_readfirstlane(prev) == 3u;
}
__device__ __forceinline__ unsigned wait_place(LAS unsigned char* lds) {
  unsigned p = LDSV(unsigned, F_PF), spin = 0;
  NOUNROLL while (p == 0u) { GIVEUP(spin, CAP_L); __builtin_amdgcn_s_sleep(1); p = LDSV(unsigned, F_PF); }
  return __builtin_amdgcn_readfirstlane(p);
}
__device__ __forceinline__ int partner(unsigned place, int g) {
  const unsigned m = (((place >> g) & 1u) ? place : ~place) & 0xffu;
  return (g + 1 + __builtin_ctz(((m | (m << 8)) >> (g + 1)) & 0xffu)) & 7;
}

#define DESC(n) const void* p_##n, unsigned n##_bytes, unsigned n##_flags
__global__ void __launch_bounds__(NTHR, 3) fused_decode(DESC(k), DESC(v), DESC(ws), DESC(x), DESC(wq), DESC(wg), DESC(wu), DESC(wo), DESC(wd), DESC(out)) {
  __shared__ __attribute__((aligned(16))) unsigned char smem[L_END];
  auto ka = (const __attribute__((address_space(4))) i32x4*)__builtin_amdgcn_kernarg_segment_ptr();
  i32x16 KA, KB;
  unsigned long long pc0;
  asm volatile("s_load_dwordx16 %0, %3, 0x0\n\ts_load_dwordx16 %1, %3, 0x40\n\ts_getpc_b64 %2" : "=&s"(KA), "=&s"(KB), "=&s"(pc0) : "s"(ka));
  SB();
  LAS unsigned char* lds = (LAS unsigned char*)smem;
  const int blk = blockIdx.x, kvh = blk & 7, gi = blk >> 3, b = gi & 3, s = gi >> 2;
  const int tid = threadIdx.x, lane = tid & 63, w = __builtin_amdgcn_readfirstlane(tid >> 6), g = lane >> 4, c = lane & 15;
  u32x2 pfd = {0u, 0u};
  if (w < NT) {
    if (w == 3 && lane < CODE_BYTES / 256) {
      const int off = gi * (CODE_BYTES / 32) + lane * 8;
      asm volatile("global_load_dwordx2 %0, %1, %2" : "=v"(pfd) : "v"(off), "s"(pc0 & ~0xffull));
    }
    if (w == 2) {
      LDSV(unsigned, L_MW + lane * 4) = 0u;
      LDSV(unsigned, L_FLAG + c * 4) = 0u;
    }
  }
  const int kv = w - NT;
  int koff = (c >> 2) * 256 + g * 64 + (c & 3) * 16;
  int kvbase = ((b * 8 + kvh) * 4096 + s * 512) * 256 + kv * 4096;
  int so0 = kvbase + (gi & 3) * 1024;
  OPQ(koff);
  asm volatile("s_waitcnt lgkmcnt(0)" : "+s"(KA), "+s"(KB), "+s"(kvbase), "+s"(so0)::"memory");
  const i32x4 rws = {KA[8], KA[9], KA[10], KA[11]}, rx = {KA[12], KA[13], KA[14], KA[15]};

  if (__builtin_expect(w >= NT, 1)) {
    const i32x4 rk = {KA[0], KA[1], KA[2], KA[3]}, rv = {KA[4], KA[5], KA[6], KA[7]};
    u32x4 K[16], V[16];
    const int voff = lane * 16;
#define SOFF(i) (((i) < 4 ? so0 : kvbase + ((((i) >> 2) + gi) & 3) * 1024) + ((i) & 3) * 32768)
#define WAITN(n) asm volatile("s_waitcnt vmcnt(%0)" ::"n"((n) - 1) : "memory")
#define PACE(i) apace(tgt, sched((i) + 1) - sched(i))
#define VLOAD(i) do { WAITN(WIN); PACE(16 + (i)); V[i] = LD128(rv, voff, SOFF(i), NTL); \
    if ((i) + 17 == HGPOS && kv == 7) LDSV(unsigned char, F_G + 1) = 1; \
    if ((i) == 15) { LDSV(unsigned, F_TV) = now32(); LDSV(unsigned char, F_VI + kv) = 1; } SB(); } while (0)
    unsigned long long t64;
    asm volatile("s_memrealtime %0" : "=s"(t64));
#pragma unroll
    for (int i = 0; i < N0; i++) { K[i] = LD128(rk, koff, SOFF(i), NTL); SB(); }
    asm volatile("s_barrier\n\ts_waitcnt lgkmcnt(0)" : "+s"(t64)::"memory");
    unsigned tgt = (unsigned)t64 + sched(N0) + (kv * TAU) / 80;
    WAITN(N0);
    { const unsigned t1 = now32() + REBASE + (kv * TAU) / 80; if ((int)(t1 - tgt) > 0) tgt = t1; }
#pragma unroll
    for (int i = N0; i < 16; i++) {
      if (i >= WIN) WAITN(WIN);
      PACE(i);
      K[i] = LD128(rk, koff, SOFF(i), NTL);
      if (i + 1 == QGPOS && kv == 7) LDSV(unsigned char, F_G) = 1;
      SB();
    }
#pragma unroll
    for (int i = 0; i < QKPOS; i++) VLOAD(i);
    u32x2 fl = LDSV(u32x2, F_Q);
    { unsigned spin = 0; NOUNROLL while (fl[0] == 0u) { GIVEUP(spin, CAP_L); __builtin_amdgcn_s_sleep(1); fl = LDSV(u32x2, F_Q); } }
    CBAR();
    const unsigned T = __builtin_amdgcn_readfirstlane(fl[1]);
    float S[4];
    {
      const u32x4 qf = LDSV(u32x4, L_Q + koff);
      const bool a1 = c & 1, a2 = c & 2;
#pragma unroll
      for (int t = 0; t < 4; t++) {
        float y[4];
#pragma unroll
        for (int i = 0; i < 4; i++) {
          const f32x4 d = mfma32(K[t * 4 + i], qf, f32x4{0.f, 0.f, 0.f, 0.f});
          float z = dpp<QB0>(d[0]);
          z += dpp<QB1>(d[1]); OPQ(z);
          z += dpp<QB2>(d[2]); OPQ(z);
          z += dpp<QB3>(d[3]); OPQ(z);
          y[i] = z;
        }
        const float u0 = a1 ? y[1] : y[0], u1 = a1 ? y[3] : y[2];
        S[t] = a2 ? u1 : u0;
      }
#pragma unroll
      for (int i = 0; i < 4; i += 2) {
        const unsigned p = cvtpk(S[i], S[i + 1]);
        const unsigned p2 = cvtpk(lo16(p) * 0.08838834764831845f, hi16(p) * 0.08838834764831845f);
        S[i] = lo16(p2); S[i + 1] = hi16(p2);
      }
      float mw = fmaxf(fmaxf(S[0], S[1]), fmaxf(S[2], S[3]));
      mw = fmaxf(mw, dpp<QX1>(mw)); mw = fmaxf(mw, dpp<QX2>(mw));
      mw = xrow_max(mw);
      LDSV(unsigned, L_MW + (c >> 2) * 32 + kv * 4) = __float_as_uint(mw) | 1u;
    }
    SB();
#pragma unroll
    for (int i = QKPOS; i < SMPOS; i++) VLOAD(i);
    u32x4 P[2];
    {
      u32x4 m0, m1;
      unsigned spin = 0;
      NOUNROLL for (;;) {
        m0 = LDSV(u32x4, L_MW + (c >> 2) * 32); m1 = LDSV(u32x4, L_MW + (c >> 2) * 32 + 16);
        const u32x4 mm = m0 & m1;
        if (none((mm[0] & mm[1] & mm[2] & mm[3] & 1u) == 0u)) break;
        GIVEUP(spin, CAP_L);
        __builtin_amdgcn_s_sleep(1);
      }
      float M = -INFINITY;
#pragma unroll
      for (int i = 0; i < 4; i++) M = fmaxf(M, fmaxf(hi16(m0[i]), hi16(m1[i])));
      float p[4];
#pragma unroll
      for (int i = 0; i < 4; i++) p[i] = __builtin_amdgcn_exp2f((S[i] - M) * LOG2E);
      float lsum = (p[0] + p[1]) + (p[2] + p[3]);
      lsum += dpp<QX1>(lsum); lsum += dpp<QX2>(lsum);
      lsum = xrow_sum(lsum);
      LDSV(unsigned, L_LW + (c >> 2) * 32 + kv * 4) = __float_as_uint(lsum) | 1u;
#pragma unroll
      for (int u = 0; u < 2; u++) { const unsigned pk = cvtpk(p[2 * u], p[2 * u + 1]); P[u] = u32x4{pk, dppu<QB1>(pk), dppu<QB2>(pk), dppu<QB3>(pk)}; }
    }
    unsigned mska = 0xffu;
    asm volatile("" : "+v"(mska), "+v"(P[0]), "+v"(P[1]));
    SB();
#pragma unroll
    for (int i = SMPOS; i < PVPOS; i++) {
      if (i == ORGPOS) tgt += ((int)__builtin_amdgcn_readfirstlane(LDSV(unsigned, F_PF)) >> 16) - 6;
      VLOAD(i);
    }
    f32x4 O[8];
#pragma unroll
    for (int e = 0; e < 8; e++) {
      u32x4 f;
#pragma unroll
      for (int j = 0; j < 4; j++) f[j] = kpair(V[j][e >> 1], V[4 + j][e >> 1], e & 1);
      O[e] = mfma32(P[0], f, f32x4{0.f, 0.f, 0.f, 0.f});
    }
    asm volatile("" : "+v"(O[0]), "+v"(O[1]), "+v"(O[2]), "+v"(O[3]), "+v"(O[4]), "+v"(O[5]), "+v"(O[6]), "+v"(O[7]));
    SB();
    const unsigned place = wait_place(lds);
    const int slot = (kvh * 4 + b) * 8 + s;
    const int side = ((place >> kvh) & 1) * 8192;
    int oaoff = WS_OA + (slot >> 2) * 16384 + side + (slot & 3) * 2048 + kv * 512 + lane * 8;
    OPQ(oaoff);
    const int soff = WS_ST + (partner(place, kvh) * 32 + (lane & 31)) * 128 + (gi >> 3) * 16;
    u32x4 s0 = {0u, ~T, 0u, 0u};
    if (kv == 5 && lane < 32) s0 = LD128(rws, soff, 0, SC1);
    SB();
    int redw = kv * 2560 + lane * 4, redw2 = redw + 1280, redr = (lane >> 5) * 10240 + ((lane & 31) >> 2) * 320 + kv * 64 + (lane & 3) * 16;
    unsigned tab = T >> ((lane & 3) * 8);
    asm volatile("" : "+v"(redw), "+v"(redw2), "+v"(redr), "+v"(tab));
#pragma unroll
    for (int i = PVPOS; i < 16; i++) VLOAD(i);
    {
      u32x4 F[8];
#pragma unroll
      for (int j = 0; j < 3; j++) {
#pragma unroll
        for (int e = 0; e < 8; e++) F[e][j] = kpair(V[8 + j][e >> 1], V[12 + j][e >> 1], e & 1);
        SB();
      }
#pragma unroll
      for (int i = 0; i < 4; i++) {
        F[2 * i][3] = kpair(V[11][i], V[15][i], 0); F[2 * i + 1][3] = kpair(V[11][i], V[15][i], 1);
        O[2 * i] = mfma32(P[1], F[2 * i], O[2 * i]); O[2 * i + 1] = mfma32(P[1], F[2 * i + 1], O[2 * i + 1]);
        SB();
      }
    }
#pragma unroll
    for (int e = 0; e < 4; e++) *(LAS float*)(lds + L_RED + redw + e * 320) = O[e][0];
#pragma unroll
    for (int e = 0; e < 4; e++) *(LAS float*)(lds + L_RED + redw2 + e * 320) = O[4 + e][0];
    CBAR();
    LDSV(unsigned char, F_PV + kv) = 1;
    if (kv < 4) {
      f32x4 r[4];
      unsigned spin = 0;
      NOUNROLL for (;;) {
        const u32x2 cnt = LDSV(u32x2, F_PV);
#pragma unroll
        for (int i = 0; i < 4; i++) r[i] = LDSV(f32x4, L_RED + redr + i * 2560);
        if ((cnt[0] & cnt[1]) == 0x01010101u) break;
        GIVEUP(spin, CAP_L);
      }
      const f32x4 a = (r[0] + r[1]) + (r[2] + r[3]);
      ST64(u32x2{tagm(fold32(a[0], a[2]), mska, tab), __float_as_uint(fold32(a[1], a[3]))}, rws, oaoff, 0, SC1);
    }
    if (kv == 5) {
      if (lane < 32) {
        unsigned spin = 0;
        NOUNROLL while (!none(((s0[1] ^ T) | (s0[3] ^ T)) != 0u)) {
          GIVEUP(spin, CAP_G);
          __builtin_amdgcn_s_sleep(2);
          CBAR();
          s0 = LD128(rws, soff, 0, SC1);
        }
        const float m1 = __uint_as_float(s0[0]);
        float MM = fmaxf(m1, dpp<QX1>(m1));
        MM = fmaxf(MM, dpp<QX2>(MM)); MM = fmaxf(MM, dpp<HMIR>(MM));
        const float e1 = __builtin_amdgcn_exp2f((m1 - MM) * LOG2E);
        float L = e1 * __uint_as_float(s0[2]);
        L += dpp<QX1>(L); L += dpp<QX2>(L); L += dpp<HMIR>(L);
        LDSV(float, L_WT + lane * 4) = e1 * __builtin_amdgcn_rcpf(L);
      }
      LDSV(unsigned, F_W) = 1u;
    }
    if (kv >= 6 && kvh == (int)((place >> 9) & 7u)) {
      const int lh = kv - 6, pg = lane >> 3;
      const int oi = (gi >> 3) * 512 + (gi & 7) * 64 + lh * 32 + (c & 7) * 4 + g;
      const i32x4 ro = ka[9];
      float xfin = lo16(LD16(rx, oi * 2, 0, 0));
      int go[4];
#pragma unroll
      for (int i = 0; i < 4; i++) go[i] = WS_PB + ((gi & 7) * 4 + i) * 16384 + side + pg * 1024 + (((gi >> 3) * 2 + lh + 2 * i + (pg >> 2)) & 7) * 128 + (lane & 7) * 16;
      int ooff = c < 8 ? oi * 2 : OOB;
      unsigned ta = (T >> ((lane & 1) * 16)) & 0xffu, tb = (T >> ((lane & 1) * 16 + 8)) & 0xffu;
      asm volatile("" : "+v"(xfin), "+v"(go[0]), "+v"(go[1]), "+v"(go[2]), "+v"(go[3]), "+v"(ooff), "+v"(ta), "+v"(tb));
      lds_wait_eq(lds, F_T, 4u);
      __builtin_amdgcn_s_sleep(17);
      f32x4 v;
      unsigned spin = 0, bad;
      NOUNROLL do {
        GIVEUP(spin, CAP_G);
        __builtin_amdgcn_s_sleep(1);
        CBAR();
        u32x4 pb[4];
#pragma unroll
        for (int i = 0; i < 4; i++) pb[i] = LD128(rws, go[i], 0, SC1);
        bad = 0;
#pragma unroll
        for (int i = 0; i < 4; i++) {
          pb[i][0] ^= ta; pb[i][2] ^= tb; bad |= pb[i][0] | pb[i][2];
          v = i ? v + F4(pb[i]) : F4(pb[0]);
          OPQ(v);
        }
      } while (!none((bad & 0xffu) != 0));
      float z = fold16(fold32(v[0], v[2]), fold32(v[1], v[3]));
      z += dpp<ROR8>(z);
      z += xfin;
      ST16((u16)cvtpk(z, z), ro, ooff, 0, SC1);
      if (gi == 0 && lh == 0 && lane == 0) ST32(T, rws, WS_EPOCH, 0, SC1);
    }
    return;
  }

  __builtin_amdgcn_s_setprio(3);
  const unsigned ep = LD32(rws, WS_EPOCH, 0, SC1);
  asm volatile("s_barrier" ::: "memory");
  const int hl = gi >> 3, ng = gi & 7;
  const int task = gi >> 1, kh = gi & 1;
  const unsigned tstart = (unsigned)__builtin_amdgcn_s_memrealtime();
  if (task < 15) {
    const bool isq = task < 8;
    u32x4 xs[4], xf, W[8];
#pragma unroll
    for (int i = 0; i < 4; i++) xs[i] = LD128(rx, (c >> 2) * 1024 + g * 64 + (c & 3) * 16 + i * 256, 0, 0);
    xf = LD128(rx, (c >> 2) * 1024 + (c & 1) * 64 + g * 16, kh * 512 + w * 128, 0);
    const int kvq = (kvh + 1) & 7, idx = kvh * 7 + task - 8, mat = idx >= 28 ? 1 : 0;
    const int cb = isq ? kvq * 8 + task : idx - 28 * mat;
    const int rstride = isq ? 8192 : FF * 2;
    const i32x4 rw = {isq ? KB[0] : mat ? KB[8] : KB[4], isq ? KB[1] : mat ? KB[9] : KB[5], isq ? KB[2] : KB[6], KB[3]};
    const int woff = ((c >> 3) * 32 + g * 8) * rstride + (c & 7) * 16;
    int wso = (kh * 256 + w * 64) * rstride + cb * 128;
    unsigned tg = tstart + GOFF + (w * GTAU) / 4;
#pragma unroll
    for (int j = 0; j < 8; j++) {
      if (!isq) pace(tg, GTAU);
      W[j] = LD128(rw, woff, wso, NTL);
      wso += rstride;
      SB();
    }
    float sm = 0.f;
#pragma unroll
    for (int i = 0; i < 4; i++)
#pragma unroll
      for (int j = 0; j < 4; j++) sm = dot2(xs[i][j], 0x3f803f80u, sm);
    sm += dpp<QX1>(sm); sm += dpp<QX2>(sm);
    const float mean = xrow_sum(sm) * (1.f / DM);
    f32x2 ssv = {0.f, 0.f};
#pragma unroll
    for (int i = 0; i < 4; i++)
#pragma unroll
      for (int j = 0; j < 4; j++) { const f32x2 d = f32x2{lo16(xs[i][j]), hi16(xs[i][j])} - mean; ssv += d * d; }
    float ss = ssv[0] + ssv[1];
    ss += dpp<QX1>(ss); ss += dpp<QX2>(ss);
    const float var = rbf(xrow_sum(ss) * (1.f / DM)), mb = rbf(mean);
    const float den = rbf(__builtin_amdgcn_sqrtf(rbf(var + 1e-5f)));
    const float rden = __builtin_amdgcn_rcpf(den);
#pragma unroll
    for (int j = 0; j < 4; j++) {
      const unsigned d = cvtpk(lo16(xf[j]) - mb, hi16(xf[j]) - mb);
      const f32x2 n = {lo16(d), hi16(d)};
      const f32x2 q = n * rden;
      const f32x2 r = (n - q * den) * rden + q;
      xf[j] = cvtpk(r[0], r[1]);
    }
    SB();
    f32x4 acc[8];
#pragma unroll
    for (int e = 0; e < 8; e++) {
      u32x4 f;
#pragma unroll
      for (int j = 0; j < 4; j++) f[j] = kpair(W[2 * j][e >> 1], W[2 * j + 1][e >> 1], e & 1);
      acc[e] = mfma32(xf, f, f32x4{0.f, 0.f, 0.f, 0.f});
    }
    SB();
    f32x4 o, z;
#pragma unroll
    for (int i = 0; i < 4; i++) {
      const float a = acc[i][0] + dpp<ROR8>(acc[i][1]), bq = acc[4 + i][1] + dpp<ROR8>(acc[4 + i][0]);
      o[i] = c < 8 ? a : bq;
    }
    if (meet4(meet_prep(lds, L_PR, F_P, w, lane, g * 16 + (c & 7) * 2 + (c >> 3)), o, z)) {
      const unsigned t0 = (__builtin_amdgcn_readfirstlane(ep) + 0x01010101u) >> ((c & 1) * 16);
      const u32x4 pv = {tagm(z[0], 0xfu, t0), tagm(z[1], 0xfu, t0 >> 4), tagm(z[2], 0xfu, t0 >> 8), tagm(z[3], 0xfu, t0 >> 12)};
      const int poff = isq ? WS_Q + (((kvq * 4 + g) * 2 + kh) * 512 + task * 64 + c * 4) * 4 : WS_GU + (((mat * 2 + kh) * 4 + g) * FF + cb * 64 + c * 4) * 4;
      ST128(pv, rws, poff, 0, SC1);
    }
  }
  const unsigned T = __builtin_amdgcn_readfirstlane(ep) + 0x01010101u;
  asm volatile("" : "+v"(pfd) : "s"(T) : "memory");
  LDSV(unsigned char, F_E + w) = 1;
  const int bb = c >> 2, q4 = c & 3, sh = g >> 1;
  if (w >= 2) {
    int hh3 = 0;
    if (w == 3) hh3 = partner(wait_place(lds), kvh) * 4 + hl;
    const int goff = w == 2 ? WS_Q + (kvh * 4 + b) * 4096 + lane * 16 : WS_GU + (g * FF + hh3 * 56 + (c < 14 ? c : 13) * 4) * 4;
    const int st1 = w == 2 ? 2048 : 4 * FF * 4, st2 = w == 2 ? 1024 : 8 * FF * 4;
    const unsigned tn = T >> (goff & 16);
    const u32x4 TN = {tn & 0xfu, (tn >> 4) & 0xfu, (tn >> 8) & 0xfu, (tn >> 12) & 0xfu};
    u32x4 q[4];
    lds_wait_byte(lds, F_G);
    unsigned spin = 0;
    NOUNROLL for (;;) {
      CBAR();
      q[0] = LD128(rws, goff, 0, SC1); q[1] = LD128(rws, goff, st1, SC1); q[2] = LD128(rws, goff, st2, SC1); q[3] = LD128(rws, goff, st1 + st2, SC1);
      unsigned bad = 0;
#pragma unroll
      for (int i = 0; i < 4; i++) { q[i] ^= TN; bad |= (q[i][0] | q[i][1]) | (q[i][2] | q[i][3]); }
      if (none((bad & 0xfu) != 0)) break;
      GIVEUP(spin, CAP_G);
      __builtin_amdgcn_s_sleep(8);
    }
    const f32x4 y0 = F4(q[0]) + F4(q[1]), y1 = F4(q[2]) + F4(q[3]);
    const u32x2 z0 = {cvtpk(y0[0], y0[1]), cvtpk(y0[2], y0[3])}, z1 = {cvtpk(y1[0], y1[1]), cvtpk(y1[2], y1[3])};
    if (w == 2) {
      LDSV(u32x2, L_Q + lane * 8) = z0;
      LDSV(u32x2, L_Q + 512 + lane * 8) = z1;
      lds_wait_eq(lds, F_E, 0x01010101u);
      LDSV(u32x2, F_Q) = u32x2{1u, T};
    } else {
      u32x2 hp;
#pragma unroll
      for (int i = 0; i < 2; i++) hp[i] = c < 14 ? cvtpk(lo16(z0[i]) * lo16(z1[i]), hi16(z0[i]) * hi16(z1[i])) : 0u;
      LDSV(u32x2, L_H + g * 128 + c * 8) = hp;
      LDSV(unsigned, F_H) = 1u;
    }
  } else if (w == 0) {
    unsigned lv, spin = 0;
    NOUNROLL for (;;) {
      lv = LDSV(unsigned, L_LW + (lane & 31) * 4);
      if (none(lv == 0u)) break;
      GIVEUP(spin, CAP_L);
      __builtin_amdgcn_s_sleep(2);
    }
    float mv = hi16(LDSV(unsigned, L_MW + (lane & 31) * 4));
    float lsum = __uint_as_float(lv);
    lsum += dpp<QX1>(lsum); lsum += dpp<QX2>(lsum); lsum += dpp<HMIR>(lsum);
    mv = fmaxf(mv, dpp<QX1>(mv)); mv = fmaxf(mv, dpp<QX2>(mv)); mv = fmaxf(mv, dpp<HMIR>(mv));
    const u32x4 st = {__float_as_uint(mv), T, __float_as_uint(lsum), T};
    if ((lane & 7) == 0) ST128(st, rws, WS_ST + ((kvh * 4 + b) * 8 + s) * 128 + (lane >> 3) * 16, 0, SC1);
  } else {
    if (gi == 31) {
      ST128(u32x4{T, T, T, T}, rws, WS_PR + (lane >> 5) * 8192 + kvh * 1024 + (lane & 31) * 16, 0, SC1);
      asm volatile("s_waitcnt vmcnt(0)" ::: "memory");
      unsigned best0 = ~0u, best1 = ~0u;
      NOUNROLL for (int i = 0; i < 8; i++) {
        unsigned long long c0, c1; unsigned d;
        asm volatile("s_memtime %0\n\ts_waitcnt lgkmcnt(0)\n\ts_buffer_load_dword %2, %3, %4\n\ts_waitcnt lgkmcnt(0)\n\ts_memtime %1\n\ts_waitcnt lgkmcnt(0)"
                     : "=&s"(c0), "=&s"(c1), "=&s"(d) : "s"(rws), "s"(WS_PR + (i & 1) * 8192 + kvh * 1024 + (i >> 1) * 128) : "memory");
        const unsigned dt = (unsigned)c1 - (unsigned)c0;
        if (i >= 2) { if (i & 1) best1 = min(best1, dt); else best0 = min(best0, dt); }
      }
      const unsigned xcc = __builtin_amdgcn_s_getreg(20 | (3 << 11)) & 7u;
      if (lane == 0) ST64(u32x2{T, (tstart << 4) | (xcc << 1) | (best1 < best0 ? 1u : 0u)}, rws, WS_PF + kvh * 8, 0, SC1);
    }
    lds_wait_byte(lds, F_G + 1);
    if (lane < 8) {
      u32x2 pf;
      unsigned spin = 0;
      NOUNROLL for (;;) {
        CBAR();
        pf = LD64(rws, WS_PF + lane * 8, 0, SC1);
        if (none(pf[0] != T)) break;
        GIVEUP(spin, CAP_G);
        __builtin_amdgcn_s_sleep(8);
      }
      unsigned kx = ((pf[1] & 14u) << 2) | lane;
      kx = min(kx, dppu<QX1>(kx)); kx = min(kx, dppu<QX2>(kx)); kx = min(kx, dppu<HMIR>(kx));
      unsigned dl = (unsigned)min(max(((int)(pf[1] - (tstart << 4))) >> 4, -100), 100);
      dl += dppu<QX1>(dl); dl += dppu<QX2>(dl); dl += dppu<HMIR>(dl);
      LDSV(unsigned, F_PF) = (unsigned)__builtin_amdgcn_ballot_w64(pf[1] & 1u) | 0x100u | ((kx & 7u) << 9) | ((unsigned)((int)dl >> 3) << 16);
    }
  }
  const unsigned place = wait_place(lds);
  const int kva = partner(place, kvh), hh = kva * 4 + hl;
  const i32x4 rwo = {KB[12], KB[13], KB[14], KB[15]}, rwd = ka[8];
  const int wos = (hh * 128 + 16 * (w >> 1) + 4 * (w & 1)) * 1024 + ng * 128, wds = (hh * 56 + w * 16) * 1024 + ng * 128;
  int aoff = WS_OA + ((kva * 4 + bb) * 2 + sh) * 16384 + ((place >> kvh) & 1) * 8192 + hl * 512 + w * 128 + (g & 1) * 64 + q4 * 16;
  int pboff = WS_PB + (ng * 4 + (hh >> 3)) * 16384 + ((place >> ((place >> 9) & 7)) & 1) * 8192 + (hh & 7) * 1024 + ((lane * 16 + (hh >> 2) * 128) & 1023);
  int wooff = (32 * (g >> 1) + 2 * (g & 1)) * 1024 + c * 8;
  int wdoff = (w == 3 && g >= 2) ? OOB - 4096 : g * 4096 + c * 8;
  unsigned ta = (T >> ((lane & 1) * 16)) & 0xffu, tb = (T >> ((lane & 1) * 16 + 8)) & 0xffu, mska = 0xffu;
  asm volatile("" : "+v"(ta), "+v"(tb));
  asm volatile("" : "+v"(aoff), "+v"(pboff), "+v"(wooff), "+v"(wdoff), "+v"(mska));
  lds_wait8(lds, F_VI);
  const unsigned tvi = __builtin_amdgcn_readfirstlane(LDSV(unsigned, F_TV));
  u32x2 WD[4], WO[8];
  {
    unsigned tw = tvi + WDEL + (w * WTAU) / 4;
#pragma unroll
    for (int j = 0; j < 4; j++) { pace(tw, WTAU); WD[j] = LD64(rwd, wdoff + j * 1024, wds, NTL); SB(); }
#pragma unroll
    for (int j = 0; j < 8; j++) { pace(tw, WTAU); WO[j] = LD64(rwo, wooff + (j >> 2) * 1024, wos + (j & 1) * 8192 + ((j >> 1) & 1) * 65536, NTL); SB(); }
  }
  lds_wait_eq(lds, F_H, 1u);
  const u32x2 hf = LDSV(u32x2, L_H + bb * 128 + w * 32 + g * 8);
  lds_wait8(lds, F_PV);
  __builtin_amdgcn_s_sleep(4);
  { unsigned tA = tvi + TA_ADD; pace(tA, 0); }
  u32x4 a[4];
#pragma unroll
  for (int i = 0; i < 4; i++) a[i] = LD128(rws, aoff, i * 2048, SC1);
  SB();
  f32x4 acc[4];
#pragma unroll
  for (int e = 0; e < 4; e++)
    acc[e] = mfma16(hf, u32x2{kpair(WD[0][e >> 1], WD[1][e >> 1], e & 1), kpair(WD[2][e >> 1], WD[3][e >> 1], e & 1)}, f32x4{0.f, 0.f, 0.f, 0.f});
  asm volatile("" : "+v"(acc[0]), "+v"(acc[1]), "+v"(acc[2]), "+v"(acc[3]));
  const Meet mt = meet_prep(lds, L_TR, F_T, w, lane, lane);
  lds_wait_eq(lds, F_W, 1u);
  f32x4 wt = LDSV(f32x4, L_WT + bb * 32 + sh * 16);
  OPQ(wt);
  u32x4 fo[4];
#pragma unroll
  for (int j = 0; j < 4; j++) {
#pragma unroll
    for (int e = 0; e < 4; e++) fo[e][j] = kpair(WO[2 * j][e >> 1], WO[2 * j + 1][e >> 1], e & 1);
    SB();
  }
  asm volatile("" : "+v"(fo[0]), "+v"(fo[1]), "+v"(fo[2]), "+v"(fo[3]));
  f32x4 t;
  auto weigh = [&]() {
    unsigned bad = 0;
#pragma unroll
    for (int i = 0; i < 4; i++) {
      a[i][0] ^= ta; a[i][2] ^= tb; bad |= a[i][0] | a[i][2];
      t = i ? __builtin_elementwise_fma(F4(a[i]), f32x4{wt[i], wt[i], wt[i], wt[i]}, t) : F4(a[0]) * wt[0];
      OPQ(t);
    }
    return !none((bad & 0xffu) != 0);
  };
  if (weigh()) {
    unsigned spin = 0;
    NOUNROLL do {
      GIVEUP(spin, CAP_G);
      __builtin_amdgcn_s_sleep(1);
      CBAR();
#pragma unroll
      for (int i = 0; i < 4; i++) a[i] = LD128(rws, aoff, i * 2048, SC1);
    } while (weigh());
  }
  const unsigned own = cvtpk(fold32(t[0], t[2]), fold32(t[1], t[3]));
  const u32x4 A = {own, dppu<QB1>(own), dppu<QB2>(own), dppu<QB3>(own)};
#pragma unroll
  for (int e = 0; e < 4; e++) acc[e] = mfma32(A, fo[e], acc[e]);
  SB();
  f32x4 z;
  if (meet4(mt, f32x4{acc[0][0], acc[1][0], acc[2][0], acc[3][0]}, z)) {
    const u32x4 o = {tagm(z[0], mska, ta), __float_as_uint(z[1]), tagm(z[2], mska, tb), __float_as_uint(z[3])};
    ST128(o, rws, pboff, 0, SC1);
  }
}

extern "C" void kernel_launch(void* const* d_in, const int* in_sizes, int n_in, void* d_out, int out_size, void* d_ws, size_t ws_size, hipStream_t stream) {
  const unsigned fl = 0x00020000u;
  fused_decode<<<256, NTHR, 0, stream>>>(d_in[1], 33554432u, fl, d_in[2], 33554432u, fl, d_ws, (unsigned)WS_END, fl, d_in[0], 4096u, fl,
                                         d_in[3], 4194304u, fl, d_in[6], 1835008u, fl, d_in[7], 1835008u, fl, d_in[4], 4194304u, fl,
                                         d_in[8], 1835008u, fl, d_out, 4096u, fl);
}
```
